```python
import torch

def _softmax(x, axis=-1):
    return torch.softmax(x, dim=axis)
def _gelu(x):
    return torch.nn.functional.gelu(x, approximate="tanh")
def _silu(x):
    return torch.nn.functional.silu(x)
def _sigmoid(x):
    return torch.sigmoid(x)
def _layernorm(x, axis=-1, eps=1e-5):
    m = x.mean(dim=axis, keepdim=True)
    v = x.var(dim=axis, keepdim=True, unbiased=False)
    return (x - m) / torch.sqrt(v + eps)
def _scatter_add(tgt, idx, src, axis=0):
    return tgt.clone().index_add_(axis, idx, src)
def _segment_sum(src, seg, n):
    out = torch.zeros((n,) + src.shape[1:], dtype=src.dtype)
    return out.index_add_(0, seg, src)
def _pad(x, pw):
    flat = tuple(v for pair in reversed(pw) for v in pair)
    return torch.nn.functional.pad(x, flat)

kernel_name = 'concat_heads__gqa_block_cached__mha_block_cached__near_ridge'

def setup_inputs():

    _w = (lambda a: (lambda t: t.to(torch.bfloat16) if t.is_floating_point() else t)(torch.as_tensor(a)))
    def _A_setup():
        B, Sq, D = 8, 288, 640
        Hq, Hkv, Sk, Dh = 32, 32, 1024, 128
        assert Hq % Hkv == 0, (Hq, Hkv)
        return {
            "x": _w((lambda *s: torch.randn(*s, dtype=torch.bfloat16) * 0.8)(B, Sq, D)),
            "k_cache": _w((lambda *s: torch.randn(*s, dtype=torch.bfloat16) * 0.8)(B, Hkv, Sk, Dh)),
            "v_cache": _w((lambda *s: torch.randn(*s, dtype=torch.bfloat16) * 0.05)(B, Hkv, Sk, Dh)),
            "wq": _w((lambda *s: torch.randn(*s, dtype=torch.bfloat16) * 0.8)(D, Hq * Dh) * (D ** -0.5)),
            "wo": _w((lambda *s: torch.randn(*s, dtype=torch.bfloat16) * 0.05)(Hq * Dh, D) * ((Hq * Dh) ** -0.5)),
        }
    _A_in = _A_setup()

    def _B_setup():
        B, Sq, D = 8, 288, 640
        H, Sk, Dh = 32, 1024, 128
        return {
            "x": _w((lambda *s: torch.randn(*s, dtype=torch.bfloat16) * 0.8)(B, Sq, D)),
            "k_cache": _w((lambda *s: torch.randn(*s, dtype=torch.bfloat16) * 0.8)(B, H, Sk, Dh)),
            "v_cache": _w((lambda *s: torch.randn(*s, dtype=torch.bfloat16) * 0.05)(B, H, Sk, Dh)),
            "wq": _w((lambda *s: torch.randn(*s, dtype=torch.bfloat16) * 0.8)(D, H * Dh) * (D ** -0.5)),
            "wo": _w((lambda *s: torch.randn(*s, dtype=torch.bfloat16) * 0.05)(H * Dh, D) * ((H * Dh) ** -0.5)),
        }
    _B_in = _B_setup()

    return {
        "A_x": _A_in["x"],
        "A_k_cache": _A_in["k_cache"],
        "A_v_cache": _A_in["v_cache"],
        "A_wq": _A_in["wq"],
        "A_wo": _A_in["wo"],
        "B_x": _B_in["x"],
        "B_k_cache": _B_in["k_cache"],
        "B_v_cache": _B_in["v_cache"],
        "B_wq": _B_in["wq"],
        "B_wo": _B_in["wo"],
    }

def reference(A_x, A_k_cache, A_v_cache, A_wq, A_wo, B_x, B_k_cache, B_v_cache, B_wq, B_wo):
    def _A_ref(x, k_cache, v_cache, wq, wo):
        B, Sq, D = x.shape
        Hq, Dh = 32, 128
        Hkv = k_cache.shape[1]
        rep = Hq // Hkv
        q = (lambda x, *d: x.permute(*d))((x @ wq).reshape(B, Sq, Hq, Dh), 0, 2, 1, 3)
        k = (lambda x, n, axis: torch.repeat_interleave(x, n, dim=axis))(k_cache, rep, axis=1)
        v = (lambda x, n, axis: torch.repeat_interleave(x, n, dim=axis))(v_cache, rep, axis=1)
        scores = torch.einsum("bhqd,bhkd->bhqk", q, k) * (Dh ** -0.5)
        p = _softmax(scores, axis=-1)
        o = torch.einsum("bhqk,bhkd->bhqd", p, v)
        return (lambda x, *d: x.permute(*d))(o, 0, 2, 1, 3).reshape(B, Sq, Hq * Dh) @ wo

    def _B_ref(x, k_cache, v_cache, wq, wo):
        B, Sq, D = x.shape
        H, Dh = 32, 128
        q = (lambda x, *d: x.permute(*d))((x @ wq).reshape(B, Sq, H, Dh), 0, 2, 1, 3)
        scores = torch.einsum("bhqd,bhkd->bhqk", q, k_cache) * (Dh ** -0.5)
        p = _softmax(scores, axis=-1)
        o = torch.einsum("bhqk,bhkd->bhqd", p, v_cache)
        return (lambda x, *d: x.permute(*d))(o, 0, 2, 1, 3).reshape(B, Sq, H * Dh) @ wo

    a = _A_ref(A_x, A_k_cache, A_v_cache, A_wq, A_wo)
    b = _B_ref(B_x, B_k_cache, B_v_cache, B_wq, B_wo)
    return torch.concatenate([a, b], axis=-1)
```

```cpp
#include <hip/hip_runtime.h>
#include <hip/hip_bf16.h>
typedef unsigned short u16;
__device__ __forceinline__ float bf2f(u16 v) { return __uint_as_float((unsigned)v << 16); }
__device__ __forceinline__ u16 f2bf(float x) { unsigned u = __float_as_uint(x); return (u16)((u + 0x7fff + ((u >> 16) & 1)) >> 16); }
constexpr int B = 8, SQ = 288, D = 640, H = 32, SK = 1024, DH = 128, HD = H * DH;
__global__ void qproj(const u16* x, const u16* wq, float* q) {
  int row = blockIdx.x;
  __shared__ float xs[D];
  for (int i = threadIdx.x; i < D; i += 256) xs[i] = bf2f(x[row * D + i]);
  __syncthreads();
  for (int n = threadIdx.x; n < HD; n += 256) {
    float a = 0; for (int k = 0; k < D; ++k) a += xs[k] * bf2f(wq[k * HD + n]);
    q[(size_t)row * HD + n] = bf2f(f2bf(a));
  }
}
__global__ void attn(const float* q, const u16* kc, const u16* vc, float* o) {
  int row = blockIdx.x, h = blockIdx.y; int b = row / SQ;
  __shared__ float qs[DH]; __shared__ float p[SK]; __shared__ float red[256];
  int t = threadIdx.x;
  if (t < DH) qs[t] = q[(size_t)row * HD + h * DH + t];
  __syncthreads();
  const u16* K = kc + (size_t)(b * H + h) * SK * DH; const u16* V = vc + (size_t)(b * H + h) * SK * DH;
  float mx = -1e30f;
  for (int j = t; j < SK; j += 256) { float a = 0; for (int d = 0; d < DH; ++d) a += qs[d] * bf2f(K[j * DH + d]); a *= 0.08838834764f; p[j] = a; mx = fmaxf(mx, a); }
  red[t] = mx; __syncthreads();
  for (int s = 128; s; s >>= 1) { if (t < s) red[t] = fmaxf(red[t], red[t + s]); __syncthreads(); }
  mx = red[0]; __syncthreads();
  float sm = 0; for (int j = t; j < SK; j += 256) { float e = __expf(p[j] - mx); p[j] = e; sm += e; }
  red[t] = sm; __syncthreads();
  for (int s = 128; s; s >>= 1) { if (t < s) red[t] += red[t + s]; __syncthreads(); }
  sm = red[0];
  if (t < DH) { float a = 0; for (int j = 0; j < SK; ++j) a += p[j] * bf2f(V[j * DH + t]); o[(size_t)row * HD + h * DH + t] = bf2f(f2bf(a / sm)); }
}
__global__ void oproj(const float* o, const u16* wo, u16* out, int coff) {
  int row = blockIdx.x;
  __shared__ float os[HD];
  for (int i = threadIdx.x; i < HD; i += 320) os[i] = o[(size_t)row * HD + i];
  __syncthreads();
  for (int n = threadIdx.x; n < D; n += 320) { float a = 0; for (int k = 0; k < HD; ++k) a += os[k] * bf2f(wo[k * D + n]); out[(size_t)row * 2 * D + coff + n] = f2bf(a); }
}
extern "C" void kernel_launch(void* const* d_in, const int* in_sizes, int n_in, void* d_out, int out_size, void* d_ws, size_t ws_size, hipStream_t stream) {
  float* q = (float*)d_ws; float* o = q + (size_t)B * SQ * HD;
  for (int br = 0; br < 2; ++br) {
    const u16* x = (const u16*)d_in[5 * br + 0]; const u16* kc = (const u16*)d_in[5 * br + 1]; const u16* vc = (const u16*)d_in[5 * br + 2];
    const u16* wq = (const u16*)d_in[5 * br + 3]; const u16* wo = (const u16*)d_in[5 * br + 4];
    qproj<<<B * SQ, 256, 0, stream>>>(x, wq, q);
    attn<<<dim3(B * SQ, H), 256, 0, stream>>>(q, kc, vc, o);
    oproj<<<B * SQ, 320, 0, stream>>>(o, wo, (u16*)d_out, br * D);
  }
}
```

```cpp
#include <hip/hip_runtime.h>
typedef unsigned long long u64;
typedef unsigned short u16;
constexpr size_t WS_O = 224u << 20, WS_Q2 = 40u << 20, WS_FLAG = 262u << 20, WS_DBG = 278u << 20;

__global__ __launch_bounds__(256, 1) void fused_kernel(const char* xA, const char* kA, const char* vA, const char* wA,
                                                       const char* xB, const char* kB, const char* vB, const char* wB, char* ws, const char* woA, const char* woB, char* out) {
  __shared__ __attribute__((aligned(1024))) char lds[163840];
  const unsigned g = blockIdx.x;
  const unsigned w = __builtin_amdgcn_readfirstlane(threadIdx.x >> 6);
  const u64 ka = (u64)__builtin_amdgcn_kernarg_segment_ptr();
  asm volatile("" :: "s"((unsigned)(size_t)lds));
  asm volatile(
R"A(.Lk1entry:
s_and_b32 s66,%1,4
s_lshl_b32 s67,s66,3
s_load_dwordx8 s[4:11],%0,s67
s_load_dwordx8 s[12:19],%0,0x40
v_mbcnt_lo_u32_b32 v0,-1,0
v_mbcnt_hi_u32_b32 v0,-1,v0
s_mov_b32 s60,%2
s_getpc_b64 s[30:31]
.Lk1pc:
s_lshl_b32 s66,s60,13
v_lshlrev_b32 v1,7,v0
v_add_u32 v1,s66,v1
global_load_dword a160,v1,s[30:31]
s_lshr_b32 s28,%1,3
s_and_b32 s25,%1,1
s_lshl_b32 s25,s25,2
s_and_b32 s66,s28,3
s_or_b32 s25,s25,s66
s_and_b32 s26,%1,2
s_lshl_b32 s26,s26,2
s_lshr_b32 s66,s28,2
s_or_b32 s26,s26,s66
s_lshl_b32 s63,s60,12
v_lshlrev_b32 v239,4,v0
v_bfe_u32 v1,v0,2,3
v_mul_u32_u24 v246,0x500,v1
v_lshrrev_b32 v1,5,v0
v_lshl_or_b32 v246,v1,6,v246
v_and_b32 v2,3,v0
v_lshl_or_b32 v246,v2,4,v246
v_xor_b32 v247,32,v246
s_and_b32 s66,s60,1
s_lshl_b32 s66,s66,5
v_xor_b32 v250,s66,v246
v_add_u32 v248,18432,v246
v_add_u32 v249,27648,v247
v_add_u32 v247,9216,v247
v_lshrrev_b32 v1,4,v0
v_lshlrev_b32 v1,6,v1
v_lshl_or_b32 v1,v2,4,v1
v_bfe_u32 v3,v0,2,2
v_lshl_or_b32 v235,v3,8,v1
v_xor_b32 v236,32,v235
v_xor_b32 v237,16,v235
v_lshl_or_b32 v4,v3,13,v1
v_xor_b32 v3,32,v4
v_add_u32 v251,1024,v4
v_add_u32 v252,256,v4
v_add_u32 v253,64512,v3
v_add_u32 v254,63744,v3
s_mul_i32 s68,s60,81920
s_mul_i32 s80,s60,10240
s_add_u32 s80,s80,327680
s_lshl_b32 s69,s60,17
s_mov_b32 s38,0x5a000
s_mov_b32 s39,0x20000
s_mov_b32 s42,0x4fe600
s_mov_b32 s43,0x20000
s_mul_i32 s66,s25,368640
s_lshl_b32 s67,s26,9
s_waitcnt lgkmcnt(0)
s_add_u32 s36,s4,s66
s_addc_u32 s37,s5,0
s_add_u32 s40,s10,s67
s_addc_u32 s41,s11,0
s_sub_u32 s40,s40,1024
s_subb_u32 s41,s41,0
s_add_u32 s66,s63,s63
s_mov_b32 s67,s69
s_add_u32 m0,s66,65536
s_nop 0
buffer_load_dwordx4 v251,s[40:43],s67 offen lds
buffer_load_dwordx4 v252,s[40:43],s67 offen offset:1024 lds
buffer_load_dwordx4 v253,s[40:43],s67 offen offset:2048 lds
buffer_load_dwordx4 v254,s[40:43],s67 offen offset:3072 lds
s_add_u32 s67,s69,32768
s_add_u32 m0,s66,69632
s_nop 0
buffer_load_dwordx4 v251,s[40:43],s67 offen lds
buffer_load_dwordx4 v252,s[40:43],s67 offen offset:1024 lds
buffer_load_dwordx4 v253,s[40:43],s67 offen offset:2048 lds
buffer_load_dwordx4 v254,s[40:43],s67 offen offset:3072 lds
s_mov_b32 s67,s68
s_add_u32 m0,s66,0
s_nop 0
buffer_load_dwordx4 v246,s[36:39],s67 offen lds
buffer_load_dwordx4 v247,s[36:39],s67 offen offset:1024 lds
buffer_load_dwordx4 v248,s[36:39],s67 offen offset:2048 lds
buffer_load_dwordx4 v249,s[36:39],s67 offen offset:3072 lds
s_add_u32 s67,s68,40960
s_add_u32 m0,s66,4096
s_nop 0
buffer_load_dwordx4 v246,s[36:39],s67 offen lds
buffer_load_dwordx4 v247,s[36:39],s67 offen offset:1024 lds
buffer_load_dwordx4 v248,s[36:39],s67 offen offset:2048 lds
buffer_load_dwordx4 v249,s[36:39],s67 offen offset:3072 lds
s_lshl_b32 s66,s60,10
s_add_u32 m0,s66,131072
s_add_u32 s68,s68,128
buffer_load_dwordx4 v250,s[36:39],s80 offen lds
s_add_u32 s80,s80,128
s_add_u32 s69,s69,524288
s_add_u32 s12,s12,0x10600000
s_addc_u32 s13,s13,0
s_load_dword s59,s[12:13],0x10000
s_lshr_b32 s66,s28,2
s_lshl_b32 s66,s66,2
s_add_u32 s66,s66,s60
s_mul_i32 s66,s66,11520
v_min_u32 v1,8,v0
v_mul_u32_u24 v1,0x500,v1
v_add_u32 v232,s66,v1
s_and_b32 s66,s28,3
s_lshl_b32 s66,s66,2
s_add_u32 s66,s66,s60
s_lshl_b32 s66,s66,15
s_add_u32 s66,s66,1024
v_bfe_u32 v1,v0,2,2
v_lshlrev_b32 v1,13,v1
v_and_b32 v233,3,v0
v_lshl_or_b32 v1,v233,7,v1
v_add_u32 v233,s66,v1
s_mov_b32 s27,256
s_mov_b32 s29,1048576
v_min_u32 v1,31,v0
v_lshlrev_b32 v230,7,v1
s_add_u32 s71,s63,0xc000
v_min_u32 v1,19,v0
v_lshlrev_b32 v255,7,v1
v_bfe_u32 v1,v0,3,1
v_lshrrev_b32 v2,4,v0
v_lshlrev_b32 v3,1,v1
v_xor_b32 v2,v2,v3
v_lshlrev_b32 v2,4,v2
v_lshl_or_b32 v2,v1,10,v2
v_and_b32 v3,7,v0
v_lshl_or_b32 v2,v3,6,v2
v_add_u32 v241,131072,v2
s_lshl_b32 s66,s60,13
v_add_u32 v240,s66,v2
v_bfe_u32 v2,v0,2,2
v_lshlrev_b32 v2,6,v2
v_and_b32 v3,3,v0
v_lshl_or_b32 v2,v3,3,v2
v_bfe_u32 v3,v0,4,1
v_mul_u32_u24 v3,0x820,v3
v_or_b32 v2,v2,v3
v_lshrrev_b32 v3,5,v0
v_lshl_or_b32 v2,v3,13,v2
v_add_u32 v242,65536,v2
v_xor_b32 v243,32,v242
s_lshl_b32 s66,s60,8
v_add_u32 v244,s66,v242
v_add_u32 v245,s66,v243
v_mov_b32 v8,0
v_mov_b32 v9,0
v_mov_b32 v10,0
v_mov_b32 v11,0
s_nop 1
v_mfma_f32_32x32x16_bf16 a[16:31],v[8:11],v[8:11],0
v_mfma_f32_32x32x16_bf16 a[48:63],v[8:11],v[8:11],0
v_mfma_f32_32x32x16_bf16 a[80:95],v[8:11],v[8:11],0
v_mfma_f32_32x32x16_bf16 a[112:127],v[8:11],v[8:11],0
s_add_u32 s66,s63,s63
s_mov_b32 s67,s69
s_add_u32 m0,s66,98304
s_nop 0
buffer_load_dwordx4 v251,s[40:43],s67 offen lds
buffer_load_dwordx4 v252,s[40:43],s67 offen offset:1024 lds
buffer_load_dwordx4 v253,s[40:43],s67 offen offset:2048 lds
buffer_load_dwordx4 v254,s[40:43],s67 offen offset:3072 lds
s_add_u32 s67,s69,32768
s_add_u32 m0,s66,102400
s_nop 0
buffer_load_dwordx4 v251,s[40:43],s67 offen lds
buffer_load_dwordx4 v252,s[40:43],s67 offen offset:1024 lds
buffer_load_dwordx4 v253,s[40:43],s67 offen offset:2048 lds
buffer_load_dwordx4 v254,s[40:43],s67 offen offset:3072 lds
s_mov_b32 s67,s68
s_add_u32 m0,s66,32768
s_nop 0
buffer_load_dwordx4 v246,s[36:39],s67 offen lds
buffer_load_dwordx4 v247,s[36:39],s67 offen offset:1024 lds
buffer_load_dwordx4 v248,s[36:39],s67 offen offset:2048 lds
buffer_load_dwordx4 v249,s[36:39],s67 offen offset:3072 lds
s_waitcnt vmcnt(12) lgkmcnt(0)
s_barrier
ds_read_b128 a[180:183],v241
ds_read_b128 a[204:207],v241 offset:512
ds_read_b64_tr_b16 v[128:129],v244
ds_read_b64_tr_b16 v[130:131],v244 offset:4096
ds_read_b64_tr_b16 v[132:133],v244 offset:16384
ds_read_b64_tr_b16 v[134:135],v244 offset:20480
ds_read_b128 a[184:187],v241 offset:2048
ds_read_b128 a[208:211],v241 offset:2560
ds_read_b64_tr_b16 v[136:137],v245
ds_read_b64_tr_b16 v[138:139],v245 offset:4096
ds_read_b64_tr_b16 v[140:141],v245 offset:16384
ds_read_b64_tr_b16 v[142:143],v245 offset:20480
ds_read_b64_tr_b16 v[144:145],v244 offset:1024
ds_read_b64_tr_b16 v[146:147],v244 offset:5120
ds_read_b64_tr_b16 v[148:149],v244 offset:17408
ds_read_b64_tr_b16 v[150:151],v244 offset:21504
ds_read_b64_tr_b16 v[152:153],v245 offset:1024
ds_read_b64_tr_b16 v[154:155],v245 offset:5120
ds_read_b64_tr_b16 v[156:157],v245 offset:17408
ds_read_b64_tr_b16 v[158:159],v245 offset:21504
ds_read_b128 a[164:167],v240
ds_read_b128 a[188:191],v240 offset:512
ds_read_b64_tr_b16 v[160:161],v242
ds_read_b64_tr_b16 v[162:163],v242 offset:4096
ds_read_b64_tr_b16 v[164:165],v242 offset:16384
ds_read_b64_tr_b16 v[166:167],v242 offset:20480
ds_read_b128 a[168:171],v240 offset:2048
ds_read_b128 a[192:195],v240 offset:2560
ds_read_b128 a[172:175],v240 offset:4096
ds_read_b128 a[196:199],v240 offset:4608
ds_read_b128 a[176:179],v240 offset:6144
ds_read_b128 a[200:203],v240 offset:6656
s_add_u32 s67,s68,40960
s_add_u32 m0,s66,36864
s_waitcnt lgkmcnt(15)
v_mfma_f32_16x16x32_bf16 a[128:131],v[128:131],a[180:183],0
buffer_load_dwordx4 v246,s[36:39],s67 offen lds
buffer_load_dwordx4 v247,s[36:39],s67 offen offset:1024 lds
v_mfma_f32_16x16x32_bf16 a[128:131],v[132:135],a[204:207],a[128:131]
v_mfma_f32_16x16x32_bf16 a[136:139],v[128:131],a[184:187],0
ds_read_b64_tr_b16 v[128:129],v243
ds_read_b64_tr_b16 v[130:131],v243 offset:4096
v_mfma_f32_16x16x32_bf16 a[136:139],v[132:135],a[208:211],a[136:139]
ds_read_b64_tr_b16 v[132:133],v243 offset:16384
ds_read_b64_tr_b16 v[134:135],v243 offset:20480
v_mfma_f32_16x16x32_bf16 a[132:135],v[136:139],a[180:183],0
buffer_load_dwordx4 v248,s[36:39],s67 offen offset:2048 lds
v_mfma_f32_16x16x32_bf16 a[132:135],v[140:143],a[204:207],a[132:135]
v_mfma_f32_16x16x32_bf16 a[140:143],v[136:139],a[184:187],0
ds_read_b64_tr_b16 v[136:137],v242 offset:256
ds_read_b64_tr_b16 v[138:139],v242 offset:4352
v_mfma_f32_16x16x32_bf16 a[140:143],v[140:143],a[208:211],a[140:143]
ds_read_b64_tr_b16 v[140:141],v242 offset:16640
ds_read_b64_tr_b16 v[142:143],v242 offset:20736
v_mfma_f32_16x16x32_bf16 a[144:147],v[144:147],a[180:183],0
buffer_load_dwordx4 v249,s[36:39],s67 offen offset:3072 lds
v_mfma_f32_16x16x32_bf16 a[144:147],v[148:151],a[204:207],a[144:147]
v_mfma_f32_16x16x32_bf16 a[152:155],v[144:147],a[184:187],0
s_lshl_b32 s66,s60,10
ds_read_b64_tr_b16 v[144:145],v243 offset:256
ds_read_b64_tr_b16 v[146:147],v243 offset:4352
v_mfma_f32_16x16x32_bf16 a[152:155],v[148:151],a[208:211],a[152:155]
s_add_u32 m0,s66,135168
ds_read_b64_tr_b16 v[148:149],v243 offset:16640
ds_read_b64_tr_b16 v[150:151],v243 offset:20736
s_waitcnt lgkmcnt(15)
v_mfma_f32_16x16x32_bf16 a[148:151],v[152:155],a[180:183],0
s_add_u32 s68,s68,128
buffer_load_dwordx4 v250,s[36:39],s80 offen lds
v_mfma_f32_16x16x32_bf16 a[148:151],v[156:159],a[204:207],a[148:151]
v_mfma_f32_16x16x32_bf16 a[156:159],v[152:155],a[184:187],0
ds_read_b64_tr_b16 v[152:153],v242 offset:512
ds_read_b64_tr_b16 v[154:155],v242 offset:4608
v_mfma_f32_16x16x32_bf16 a[156:159],v[156:159],a[208:211],a[156:159]
s_add_u32 s80,s80,128
ds_read_b64_tr_b16 v[156:157],v242 offset:16896
ds_read_b64_tr_b16 v[158:159],v242 offset:20992
v_mfma_f32_16x16x32_bf16 v[0:3],v[160:163],a[164:167],0
s_add_u32 s69,s69,524288
v_mfma_f32_16x16x32_bf16 v[0:3],v[164:167],a[188:191],v[0:3]
buffer_load_dword a160,v232,s[36:39],s27 offen
v_mfma_f32_16x16x32_bf16 v[32:35],v[160:163],a[168:171],0
v_mfma_f32_16x16x32_bf16 v[32:35],v[164:167],a[192:195],v[32:35]
v_mfma_f32_16x16x32_bf16 v[64:67],v[160:163],a[172:175],0
s_add_u32 s27,s27,128
s_waitcnt lgkmcnt(15)
v_mfma_f32_16x16x32_bf16 v[64:67],v[164:167],a[196:199],v[64:67]
v_mfma_f32_16x16x32_bf16 v[96:99],v[160:163],a[176:179],0
ds_read_b64_tr_b16 v[160:161],v243 offset:512
ds_read_b64_tr_b16 v[162:163],v243 offset:4608
v_mfma_f32_16x16x32_bf16 v[96:99],v[164:167],a[200:203],v[96:99]
s_waitcnt lgkmcnt(14)
ds_read_b64_tr_b16 v[164:165],v243 offset:16896
ds_read_b64_tr_b16 v[166:167],v243 offset:20992
v_mfma_f32_16x16x32_bf16 v[4:7],v[128:131],a[164:167],0
buffer_load_dword a160,v233,s[40:43],s29 offen
v_mfma_f32_16x16x32_bf16 v[4:7],v[132:135],a[188:191],v[4:7]
s_add_u32 s29,s29,524288
v_mfma_f32_16x16x32_bf16 v[36:39],v[128:131],a[168:171],0
buffer_load_dword a160,v232,s[36:39],s27 offen
v_mfma_f32_16x16x32_bf16 v[36:39],v[132:135],a[192:195],v[36:39]
v_mfma_f32_16x16x32_bf16 v[68:71],v[128:131],a[172:175],0
v_mfma_f32_16x16x32_bf16 v[68:71],v[132:135],a[196:199],v[68:71]
s_add_u32 s27,s27,128
v_mfma_f32_16x16x32_bf16 v[100:103],v[128:131],a[176:179],0
ds_read_b64_tr_b16 v[128:129],v242 offset:768
ds_read_b64_tr_b16 v[130:131],v242 offset:4864
v_mfma_f32_16x16x32_bf16 v[100:103],v[132:135],a[200:203],v[100:103]
s_waitcnt lgkmcnt(14)
ds_read_b64_tr_b16 v[132:133],v242 offset:17152
ds_read_b64_tr_b16 v[134:135],v242 offset:21248
v_mfma_f32_16x16x32_bf16 v[8:11],v[136:139],a[164:167],0
buffer_load_dword a160,v233,s[40:43],s29 offen
v_mfma_f32_16x16x32_bf16 v[8:11],v[140:143],a[188:191],v[8:11]
v_mfma_f32_16x16x32_bf16 v[40:43],v[136:139],a[168:171],0
s_add_u32 s29,s29,524288
v_mfma_f32_16x16x32_bf16 v[40:43],v[140:143],a[192:195],v[40:43]
v_mfma_f32_16x16x32_bf16 v[72:75],v[136:139],a[172:175],0
v_mfma_f32_16x16x32_bf16 v[72:75],v[140:143],a[196:199],v[72:75]
v_mfma_f32_16x16x32_bf16 v[104:107],v[136:139],a[176:179],0
ds_read_b64_tr_b16 v[136:137],v243 offset:768
ds_read_b64_tr_b16 v[138:139],v243 offset:4864
v_mfma_f32_16x16x32_bf16 v[104:107],v[140:143],a[200:203],v[104:107]
s_waitcnt lgkmcnt(14)
ds_read_b64_tr_b16 v[140:141],v243 offset:17152
ds_read_b64_tr_b16 v[142:143],v243 offset:21248
v_mfma_f32_16x16x32_bf16 v[12:15],v[144:147],a[164:167],0
s_bitcmp1_b32 %1,2
s_cselect_b64 s[14:15],s[16:17],s[14:15]
s_bfe_u32 s16,%1,0x10002
v_mfma_f32_16x16x32_bf16 v[12:15],v[148:151],a[188:191],v[12:15]
s_lshl_b32 s66,s25,4
s_add_u32 s66,s66,s26
s_lshl_b32 s66,s66,19
v_mfma_f32_16x16x32_bf16 v[44:47],v[144:147],a[168:171],0
s_add_u32 s44,s6,s66
s_addc_u32 s45,s7,0
v_mfma_f32_16x16x32_bf16 v[44:47],v[148:151],a[192:195],v[44:47]
s_add_u32 s48,s8,s66
v_mfma_f32_16x16x32_bf16 v[76:79],v[144:147],a[172:175],0
s_addc_u32 s49,s9,0
v_mfma_f32_16x16x32_bf16 v[76:79],v[148:151],a[196:199],v[76:79]
s_lshl_b32 s67,s16,3
v_mfma_f32_16x16x32_bf16 v[108:111],v[144:147],a[176:179],0
s_add_u32 s25,s25,s67
ds_read_b64_tr_b16 v[144:145],v242 offset:1024
ds_read_b64_tr_b16 v[146:147],v242 offset:5120
)A"
R"A(v_mfma_f32_16x16x32_bf16 v[108:111],v[148:151],a[200:203],v[108:111]
s_waitcnt lgkmcnt(14)
ds_read_b64_tr_b16 v[148:149],v242 offset:17408
ds_read_b64_tr_b16 v[150:151],v242 offset:21504
v_mfma_f32_16x16x32_bf16 v[16:19],v[152:155],a[164:167],0
s_mul_i32 s66,s25,2359296
s_mul_i32 s72,s26,49152
v_mfma_f32_16x16x32_bf16 v[16:19],v[156:159],a[188:191],v[16:19]
s_add_u32 s66,s66,s72
v_mfma_f32_16x16x32_bf16 v[48:51],v[152:155],a[168:171],0
s_sub_u32 s66,0x2600000,s66
v_mfma_f32_16x16x32_bf16 v[48:51],v[156:159],a[192:195],v[48:51]
s_sub_u32 s52,s12,s66
v_mfma_f32_16x16x32_bf16 v[80:83],v[152:155],a[172:175],0
s_subb_u32 s53,s13,0
v_mfma_f32_16x16x32_bf16 v[80:83],v[156:159],a[196:199],v[80:83]
s_min_u32 s24,s28,29
v_mfma_f32_16x16x32_bf16 v[112:115],v[152:155],a[176:179],0
s_mul_i32 s66,s24,52
ds_read_b64_tr_b16 v[152:153],v243 offset:1024
ds_read_b64_tr_b16 v[154:155],v243 offset:5120
v_mfma_f32_16x16x32_bf16 v[112:115],v[156:159],a[200:203],v[112:115]
s_waitcnt lgkmcnt(14)
ds_read_b64_tr_b16 v[156:157],v243 offset:17408
ds_read_b64_tr_b16 v[158:159],v243 offset:21504
v_mfma_f32_16x16x32_bf16 v[20:23],v[160:163],a[164:167],0
s_lshr_b32 s66,s66,8
s_mul_i32 s72,s66,5
v_mfma_f32_16x16x32_bf16 v[20:23],v[164:167],a[188:191],v[20:23]
s_sub_u32 s24,s24,s72
v_mfma_f32_16x16x32_bf16 v[52:55],v[160:163],a[168:171],0
s_lshl_b32 s24,s24,8
v_mfma_f32_16x16x32_bf16 v[52:55],v[164:167],a[192:195],v[52:55]
s_and_b32 s17,%1,3
v_mfma_f32_16x16x32_bf16 v[84:87],v[160:163],a[172:175],0
s_lshl_b32 s72,s17,5
v_mfma_f32_16x16x32_bf16 v[84:87],v[164:167],a[196:199],v[84:87]
s_add_u32 s72,s72,s28
v_mfma_f32_16x16x32_bf16 v[116:119],v[160:163],a[176:179],0
s_lshl_b32 s72,s72,2
ds_read_b64_tr_b16 v[160:161],v242 offset:1280
ds_read_b64_tr_b16 v[162:163],v242 offset:5376
v_mfma_f32_16x16x32_bf16 v[116:119],v[164:167],a[200:203],v[116:119]
s_waitcnt lgkmcnt(14)
ds_read_b64_tr_b16 v[164:165],v242 offset:17664
ds_read_b64_tr_b16 v[166:167],v242 offset:21760
v_mfma_f32_16x16x32_bf16 v[24:27],v[128:131],a[164:167],0
s_add_u32 s72,s72,s60
s_mul_i32 s79,s72,10240
v_mfma_f32_16x16x32_bf16 v[24:27],v[132:135],a[188:191],v[24:27]
s_mul_i32 s17,s17,6
v_mfma_f32_16x16x32_bf16 v[56:59],v[128:131],a[168:171],0
s_add_u32 s17,s17,s66
v_mfma_f32_16x16x32_bf16 v[56:59],v[132:135],a[192:195],v[56:59]
s_mul_i32 s66,s16,24
v_mfma_f32_16x16x32_bf16 v[88:91],v[128:131],a[172:175],0
s_add_u32 s66,s66,s17
v_mfma_f32_16x16x32_bf16 v[88:91],v[132:135],a[196:199],v[88:91]
s_mul_i32 s66,s66,786432
v_mfma_f32_16x16x32_bf16 v[120:123],v[128:131],a[176:179],0
s_sub_u32 s66,0x2600000,s66
ds_read_b64_tr_b16 v[128:129],v243 offset:1280
ds_read_b64_tr_b16 v[130:131],v243 offset:5376
v_mfma_f32_16x16x32_bf16 v[120:123],v[132:135],a[200:203],v[120:123]
s_waitcnt lgkmcnt(14)
ds_read_b64_tr_b16 v[132:133],v243 offset:17664
ds_read_b64_tr_b16 v[134:135],v243 offset:21760
v_mfma_f32_16x16x32_bf16 v[28:31],v[136:139],a[164:167],0
s_sub_u32 s22,s12,s66
s_subb_u32 s23,s13,0
v_mfma_f32_16x16x32_bf16 v[28:31],v[140:143],a[188:191],v[28:31]
s_add_u32 s14,s14,s24
v_mfma_f32_16x16x32_bf16 v[60:63],v[136:139],a[168:171],0
s_addc_u32 s15,s15,0
v_mfma_f32_16x16x32_bf16 v[60:63],v[140:143],a[192:195],v[60:63]
s_mul_i32 s66,s17,245760
v_mfma_f32_16x16x32_bf16 v[92:95],v[136:139],a[172:175],0
s_mul_i32 s72,s16,1280
v_mfma_f32_16x16x32_bf16 v[92:95],v[140:143],a[196:199],v[92:95]
s_add_u32 s66,s66,s72
v_mfma_f32_16x16x32_bf16 v[124:127],v[136:139],a[176:179],0
s_add_u32 s66,s66,s24
ds_read_b64_tr_b16 v[136:137],v242 offset:1536
ds_read_b64_tr_b16 v[138:139],v242 offset:5632
v_mfma_f32_16x16x32_bf16 v[124:127],v[140:143],a[200:203],v[124:127]
s_waitcnt lgkmcnt(14)
ds_read_b64_tr_b16 v[140:141],v242 offset:17920
ds_read_b64_tr_b16 v[142:143],v242 offset:22016
v_mfma_f32_16x16x32_bf16 a[0:3],v[144:147],a[164:167],0
s_add_u32 s18,s18,s66
s_addc_u32 s19,s19,0
v_mfma_f32_16x16x32_bf16 a[0:3],v[148:151],a[188:191],a[0:3]
s_mul_i32 s66,s17,86
v_mfma_f32_16x16x32_bf16 a[32:35],v[144:147],a[168:171],0
s_lshr_b32 s66,s66,8
v_mfma_f32_16x16x32_bf16 a[32:35],v[148:151],a[192:195],a[32:35]
s_add_u32 s66,s66,s67
v_mfma_f32_16x16x32_bf16 a[64:67],v[144:147],a[172:175],0
s_lshl_b32 s66,s66,28
v_mfma_f32_16x16x32_bf16 a[64:67],v[148:151],a[196:199],a[64:67]
s_lshl_b32 s24,s25,12
v_mfma_f32_16x16x32_bf16 a[96:99],v[144:147],a[176:179],0
s_or_b32 s24,s24,s66
ds_read_b64_tr_b16 v[144:145],v243 offset:1536
ds_read_b64_tr_b16 v[146:147],v243 offset:5632
v_mfma_f32_16x16x32_bf16 a[96:99],v[148:151],a[200:203],a[96:99]
s_waitcnt lgkmcnt(14)
ds_read_b64_tr_b16 v[148:149],v243 offset:17920
ds_read_b64_tr_b16 v[150:151],v243 offset:22016
v_mfma_f32_16x16x32_bf16 a[4:7],v[152:155],a[164:167],0
s_lshr_b32 s66,s26,2
s_lshl_b32 s66,s66,10
v_mfma_f32_16x16x32_bf16 a[4:7],v[156:159],a[188:191],a[4:7]
s_or_b32 s24,s24,s66
v_mfma_f32_16x16x32_bf16 a[36:39],v[152:155],a[168:171],0
s_and_b32 s66,s26,3
v_mfma_f32_16x16x32_bf16 a[36:39],v[156:159],a[192:195],a[36:39]
s_lshl_b32 s66,s66,7
v_mfma_f32_16x16x32_bf16 a[68:71],v[152:155],a[172:175],0
s_or_b32 s24,s24,s66
v_mfma_f32_16x16x32_bf16 a[68:71],v[156:159],a[196:199],a[68:71]
s_mov_b64 s[32:33],s[14:15]
v_mfma_f32_16x16x32_bf16 a[100:103],v[152:155],a[176:179],0
s_mov_b32 s34,0x4ffc00
ds_read_b64_tr_b16 v[152:153],v242 offset:1792
ds_read_b64_tr_b16 v[154:155],v242 offset:5888
v_mfma_f32_16x16x32_bf16 a[100:103],v[156:159],a[200:203],a[100:103]
s_waitcnt lgkmcnt(14)
ds_read_b64_tr_b16 v[156:157],v242 offset:18176
ds_read_b64_tr_b16 v[158:159],v242 offset:22272
v_mfma_f32_16x16x32_bf16 a[8:11],v[160:163],a[164:167],0
s_mov_b32 s35,0x20000
s_mov_b32 s46,0x80000
v_mfma_f32_16x16x32_bf16 a[8:11],v[164:167],a[188:191],a[8:11]
s_mov_b32 s47,0x20000
v_mfma_f32_16x16x32_bf16 a[40:43],v[160:163],a[168:171],0
s_mov_b32 s50,0x80000
v_mfma_f32_16x16x32_bf16 a[40:43],v[164:167],a[192:195],a[40:43]
s_mov_b32 s51,0x20000
v_mfma_f32_16x16x32_bf16 a[72:75],v[160:163],a[172:175],0
s_mov_b32 s54,0x18c000
v_mfma_f32_16x16x32_bf16 a[72:75],v[164:167],a[196:199],a[72:75]
s_mov_b32 s55,0x20000
v_mfma_f32_16x16x32_bf16 a[104:107],v[160:163],a[176:179],0
s_and_b32 s75,s28,0
ds_read_b64_tr_b16 v[160:161],v243 offset:1792
ds_read_b64_tr_b16 v[162:163],v243 offset:5888
v_mfma_f32_16x16x32_bf16 a[104:107],v[164:167],a[200:203],a[104:107]
s_waitcnt lgkmcnt(14)
ds_read_b64_tr_b16 v[164:165],v243 offset:18176
ds_read_b64_tr_b16 v[166:167],v243 offset:22272
v_mfma_f32_16x16x32_bf16 a[12:15],v[128:131],a[164:167],0
s_lshl_b32 s75,s75,14
s_xor_b32 s61,s63,s75
v_mfma_f32_16x16x32_bf16 a[12:15],v[132:135],a[188:191],a[12:15]
s_mov_b32 s62,s63
v_mfma_f32_16x16x32_bf16 a[44:47],v[128:131],a[168:171],0
s_mov_b32 s76,0x3e0293ee
v_mfma_f32_16x16x32_bf16 a[44:47],v[132:135],a[192:195],a[44:47]
s_mov_b32 s77,0x3e0293ee
v_mfma_f32_16x16x32_bf16 a[76:79],v[128:131],a[172:175],0
s_mov_b32 s78,0x46800000
v_mfma_f32_16x16x32_bf16 a[76:79],v[132:135],a[196:199],a[76:79]
s_mov_b32 s65,0
v_mfma_f32_16x16x32_bf16 a[108:111],v[128:131],a[176:179],0
s_mov_b32 s56,0
v_mfma_f32_16x16x32_bf16 a[108:111],v[132:135],a[200:203],a[108:111]
s_waitcnt lgkmcnt(12)
s_mov_b32 s58,16384
v_mfma_f32_16x16x32_bf16 a[16:19],v[136:139],a[164:167],a[16:19]
v_mfma_f32_16x16x32_bf16 a[16:19],v[140:143],a[188:191],a[16:19]
v_mfma_f32_16x16x32_bf16 a[48:51],v[136:139],a[168:171],a[48:51]
v_mfma_f32_16x16x32_bf16 a[48:51],v[140:143],a[192:195],a[48:51]
v_mfma_f32_16x16x32_bf16 a[80:83],v[136:139],a[172:175],a[80:83]
v_mfma_f32_16x16x32_bf16 a[80:83],v[140:143],a[196:199],a[80:83]
v_mfma_f32_16x16x32_bf16 a[112:115],v[136:139],a[176:179],a[112:115]
v_mfma_f32_16x16x32_bf16 a[112:115],v[140:143],a[200:203],a[112:115]
s_waitcnt lgkmcnt(8)
v_mfma_f32_16x16x32_bf16 a[20:23],v[144:147],a[164:167],a[20:23]
s_mov_b32 s64,4
.Lq:
v_mfma_f32_16x16x32_bf16 a[20:23],v[148:151],a[188:191],a[20:23]
s_waitcnt vmcnt(3) lgkmcnt(0)
s_barrier
v_mfma_f32_16x16x32_bf16 a[52:55],v[144:147],a[168:171],a[52:55]
s_add_u32 s66,s63,s63
ds_read_b64_tr_b16 v[128:129],v244 offset:32768
ds_read_b64_tr_b16 v[130:131],v244 offset:36864
v_mfma_f32_16x16x32_bf16 a[52:55],v[148:151],a[192:195],a[52:55]
s_mov_b32 s67,s69
ds_read_b64_tr_b16 v[132:133],v244 offset:49152
ds_read_b64_tr_b16 v[134:135],v244 offset:53248
v_mfma_f32_16x16x32_bf16 a[84:87],v[144:147],a[172:175],a[84:87]
s_add_u32 m0,s66,65536
ds_read_b64_tr_b16 v[136:137],v245 offset:32768
ds_read_b64_tr_b16 v[138:139],v245 offset:36864
v_mfma_f32_16x16x32_bf16 a[84:87],v[148:151],a[196:199],a[84:87]
ds_read_b64_tr_b16 v[140:141],v245 offset:49152
ds_read_b64_tr_b16 v[142:143],v245 offset:53248
v_mfma_f32_16x16x32_bf16 a[116:119],v[144:147],a[176:179],a[116:119]
ds_read_b128 v[184:187],v241 offset:4096
buffer_load_dwordx4 v251,s[40:43],s67 offen lds
v_mfma_f32_16x16x32_bf16 a[116:119],v[148:151],a[200:203],a[116:119]
ds_read_b128 v[208:211],v241 offset:4608
ds_read_b64_tr_b16 v[144:145],v244 offset:33792
v_mfma_f32_16x16x32_bf16 a[24:27],v[152:155],a[164:167],a[24:27]
ds_read_b64_tr_b16 v[146:147],v244 offset:37888
ds_read_b128 v[188:191],v241 offset:6144
v_mfma_f32_16x16x32_bf16 a[24:27],v[156:159],a[188:191],a[24:27]
ds_read_b64_tr_b16 v[148:149],v244 offset:50176
ds_read_b64_tr_b16 v[150:151],v244 offset:54272
v_mfma_f32_16x16x32_bf16 a[56:59],v[152:155],a[168:171],a[56:59]
buffer_load_dwordx4 v252,s[40:43],s67 offen offset:1024 lds
ds_read_b128 v[212:215],v241 offset:6656
v_mfma_f32_16x16x32_bf16 a[56:59],v[156:159],a[192:195],a[56:59]
ds_read_b128 v[168:171],v240 offset:32768
ds_read_b128 v[192:195],v240 offset:33280
v_mfma_f32_16x16x32_bf16 a[88:91],v[152:155],a[172:175],a[88:91]
buffer_load_dwordx4 v253,s[40:43],s67 offen offset:2048 lds
ds_read_b128 v[172:175],v240 offset:34816
v_mfma_f32_16x16x32_bf16 a[88:91],v[156:159],a[196:199],a[88:91]
ds_read_b128 v[196:199],v240 offset:35328
ds_read_b128 v[176:179],v240 offset:36864
v_mfma_f32_16x16x32_bf16 a[120:123],v[152:155],a[176:179],a[120:123]
ds_read_b64_tr_b16 v[152:153],v245 offset:33792
ds_read_b64_tr_b16 v[154:155],v245 offset:37888
v_mfma_f32_16x16x32_bf16 a[120:123],v[156:159],a[200:203],a[120:123]
ds_read_b64_tr_b16 v[156:157],v245 offset:50176
ds_read_b64_tr_b16 v[158:159],v245 offset:54272
v_mfma_f32_16x16x32_bf16 a[28:31],v[160:163],a[164:167],a[28:31]
buffer_load_dwordx4 v254,s[40:43],s67 offen offset:3072 lds
ds_read_b128 v[200:203],v240 offset:37376
v_mfma_f32_16x16x32_bf16 a[28:31],v[164:167],a[188:191],a[28:31]
ds_read_b128 v[180:183],v240 offset:38912
v_mfma_f32_16x16x32_bf16 a[60:63],v[160:163],a[168:171],a[60:63]
s_add_u32 s67,s69,32768
s_add_u32 m0,s66,69632
v_mfma_f32_16x16x32_bf16 a[60:63],v[164:167],a[192:195],a[60:63]
ds_read_b128 v[204:207],v240 offset:39424
v_mfma_f32_16x16x32_bf16 a[92:95],v[160:163],a[172:175],a[92:95]
buffer_load_dwordx4 v251,s[40:43],s67 offen lds
v_mfma_f32_16x16x32_bf16 a[92:95],v[164:167],a[196:199],a[92:95]
v_mfma_f32_16x16x32_bf16 a[124:127],v[160:163],a[176:179],a[124:127]
ds_read_b64_tr_b16 v[160:161],v242 offset:32768
ds_read_b64_tr_b16 v[162:163],v242 offset:36864
v_mfma_f32_16x16x32_bf16 a[124:127],v[164:167],a[200:203],a[124:127]
s_waitcnt lgkmcnt(14)
ds_read_b64_tr_b16 v[164:165],v242 offset:49152
v_mfma_f32_16x16x32_bf16 a[128:131],v[128:131],v[184:187],a[128:131]
ds_read_b64_tr_b16 v[166:167],v242 offset:53248
v_mfma_f32_16x16x32_bf16 a[128:131],v[132:135],v[208:211],a[128:131]
buffer_load_dwordx4 v252,s[40:43],s67 offen offset:1024 lds
v_mfma_f32_16x16x32_bf16 a[136:139],v[128:131],v[188:191],a[136:139]
ds_read_b64_tr_b16 v[128:129],v243 offset:32768
ds_read_b64_tr_b16 v[130:131],v243 offset:36864
v_mfma_f32_16x16x32_bf16 a[136:139],v[132:135],v[212:215],a[136:139]
ds_read_b64_tr_b16 v[132:133],v243 offset:49152
ds_read_b64_tr_b16 v[134:135],v243 offset:53248
v_mfma_f32_16x16x32_bf16 a[132:135],v[136:139],v[184:187],a[132:135]
v_mfma_f32_16x16x32_bf16 a[132:135],v[140:143],v[208:211],a[132:135]
v_mfma_f32_16x16x32_bf16 a[140:143],v[136:139],v[188:191],a[140:143]
ds_read_b64_tr_b16 v[136:137],v242 offset:33024
ds_read_b64_tr_b16 v[138:139],v242 offset:37120
v_mfma_f32_16x16x32_bf16 a[140:143],v[140:143],v[212:215],a[140:143]
)A"
R"A(ds_read_b64_tr_b16 v[140:141],v242 offset:49408
ds_read_b64_tr_b16 v[142:143],v242 offset:53504
v_mfma_f32_16x16x32_bf16 a[144:147],v[144:147],v[184:187],a[144:147]
buffer_load_dwordx4 v253,s[40:43],s67 offen offset:2048 lds
v_mfma_f32_16x16x32_bf16 a[144:147],v[148:151],v[208:211],a[144:147]
v_mfma_f32_16x16x32_bf16 a[152:155],v[144:147],v[188:191],a[152:155]
ds_read_b64_tr_b16 v[144:145],v243 offset:33024
ds_read_b64_tr_b16 v[146:147],v243 offset:37120
v_mfma_f32_16x16x32_bf16 a[152:155],v[148:151],v[212:215],a[152:155]
s_waitcnt lgkmcnt(15)
ds_read_b64_tr_b16 v[148:149],v243 offset:49408
ds_read_b64_tr_b16 v[150:151],v243 offset:53504
v_mfma_f32_16x16x32_bf16 a[148:151],v[152:155],v[184:187],a[148:151]
v_mfma_f32_16x16x32_bf16 a[148:151],v[156:159],v[208:211],a[148:151]
buffer_load_dwordx4 v254,s[40:43],s67 offen offset:3072 lds
v_mfma_f32_16x16x32_bf16 a[156:159],v[152:155],v[188:191],a[156:159]
ds_read_b64_tr_b16 v[152:153],v242 offset:33280
ds_read_b64_tr_b16 v[154:155],v242 offset:37376
v_mfma_f32_16x16x32_bf16 a[156:159],v[156:159],v[212:215],a[156:159]
s_waitcnt lgkmcnt(14)
ds_read_b64_tr_b16 v[156:157],v242 offset:49664
v_mfma_f32_16x16x32_bf16 v[0:3],v[160:163],v[168:171],v[0:3]
ds_read_b64_tr_b16 v[158:159],v242 offset:53760
v_mfma_f32_16x16x32_bf16 v[0:3],v[164:167],v[192:195],v[0:3]
s_mov_b32 s67,s68
s_add_u32 m0,s66,0
v_mfma_f32_16x16x32_bf16 v[32:35],v[160:163],v[172:175],v[32:35]
v_mfma_f32_16x16x32_bf16 v[32:35],v[164:167],v[196:199],v[32:35]
buffer_load_dwordx4 v246,s[36:39],s67 offen lds
v_mfma_f32_16x16x32_bf16 v[64:67],v[160:163],v[176:179],v[64:67]
v_mfma_f32_16x16x32_bf16 v[64:67],v[164:167],v[200:203],v[64:67]
v_mfma_f32_16x16x32_bf16 v[96:99],v[160:163],v[180:183],v[96:99]
ds_read_b64_tr_b16 v[160:161],v243 offset:33280
ds_read_b64_tr_b16 v[162:163],v243 offset:37376
v_mfma_f32_16x16x32_bf16 v[96:99],v[164:167],v[204:207],v[96:99]
s_waitcnt lgkmcnt(14)
ds_read_b64_tr_b16 v[164:165],v243 offset:49664
ds_read_b64_tr_b16 v[166:167],v243 offset:53760
v_mfma_f32_16x16x32_bf16 v[4:7],v[128:131],v[168:171],v[4:7]
v_mfma_f32_16x16x32_bf16 v[4:7],v[132:135],v[192:195],v[4:7]
buffer_load_dwordx4 v247,s[36:39],s67 offen offset:1024 lds
v_mfma_f32_16x16x32_bf16 v[36:39],v[128:131],v[172:175],v[36:39]
v_mfma_f32_16x16x32_bf16 v[36:39],v[132:135],v[196:199],v[36:39]
v_mfma_f32_16x16x32_bf16 v[68:71],v[128:131],v[176:179],v[68:71]
v_mfma_f32_16x16x32_bf16 v[68:71],v[132:135],v[200:203],v[68:71]
v_mfma_f32_16x16x32_bf16 v[100:103],v[128:131],v[180:183],v[100:103]
ds_read_b64_tr_b16 v[128:129],v242 offset:33536
ds_read_b64_tr_b16 v[130:131],v242 offset:37632
v_mfma_f32_16x16x32_bf16 v[100:103],v[132:135],v[204:207],v[100:103]
s_waitcnt lgkmcnt(14)
ds_read_b64_tr_b16 v[132:133],v242 offset:49920
ds_read_b64_tr_b16 v[134:135],v242 offset:54016
v_mfma_f32_16x16x32_bf16 v[8:11],v[136:139],v[168:171],v[8:11]
buffer_load_dwordx4 v248,s[36:39],s67 offen offset:2048 lds
v_mfma_f32_16x16x32_bf16 v[8:11],v[140:143],v[192:195],v[8:11]
v_mfma_f32_16x16x32_bf16 v[40:43],v[136:139],v[172:175],v[40:43]
v_mfma_f32_16x16x32_bf16 v[40:43],v[140:143],v[196:199],v[40:43]
v_mfma_f32_16x16x32_bf16 v[72:75],v[136:139],v[176:179],v[72:75]
buffer_load_dwordx4 v249,s[36:39],s67 offen offset:3072 lds
v_mfma_f32_16x16x32_bf16 v[72:75],v[140:143],v[200:203],v[72:75]
v_mfma_f32_16x16x32_bf16 v[104:107],v[136:139],v[180:183],v[104:107]
ds_read_b64_tr_b16 v[136:137],v243 offset:33536
ds_read_b64_tr_b16 v[138:139],v243 offset:37632
v_mfma_f32_16x16x32_bf16 v[104:107],v[140:143],v[204:207],v[104:107]
s_waitcnt lgkmcnt(14)
ds_read_b64_tr_b16 v[140:141],v243 offset:49920
ds_read_b64_tr_b16 v[142:143],v243 offset:54016
v_mfma_f32_16x16x32_bf16 v[12:15],v[144:147],v[168:171],v[12:15]
s_add_u32 s67,s68,40960
s_add_u32 m0,s66,4096
v_mfma_f32_16x16x32_bf16 v[12:15],v[148:151],v[192:195],v[12:15]
v_mfma_f32_16x16x32_bf16 v[44:47],v[144:147],v[172:175],v[44:47]
buffer_load_dwordx4 v246,s[36:39],s67 offen lds
v_mfma_f32_16x16x32_bf16 v[44:47],v[148:151],v[196:199],v[44:47]
v_mfma_f32_16x16x32_bf16 v[76:79],v[144:147],v[176:179],v[76:79]
v_mfma_f32_16x16x32_bf16 v[76:79],v[148:151],v[200:203],v[76:79]
v_mfma_f32_16x16x32_bf16 v[108:111],v[144:147],v[180:183],v[108:111]
ds_read_b64_tr_b16 v[144:145],v242 offset:33792
ds_read_b64_tr_b16 v[146:147],v242 offset:37888
v_mfma_f32_16x16x32_bf16 v[108:111],v[148:151],v[204:207],v[108:111]
s_waitcnt lgkmcnt(14)
ds_read_b64_tr_b16 v[148:149],v242 offset:50176
ds_read_b64_tr_b16 v[150:151],v242 offset:54272
v_mfma_f32_16x16x32_bf16 v[16:19],v[152:155],v[168:171],v[16:19]
buffer_load_dwordx4 v247,s[36:39],s67 offen offset:1024 lds
v_mfma_f32_16x16x32_bf16 v[16:19],v[156:159],v[192:195],v[16:19]
v_mfma_f32_16x16x32_bf16 v[48:51],v[152:155],v[172:175],v[48:51]
v_mfma_f32_16x16x32_bf16 v[48:51],v[156:159],v[196:199],v[48:51]
v_mfma_f32_16x16x32_bf16 v[80:83],v[152:155],v[176:179],v[80:83]
v_mfma_f32_16x16x32_bf16 v[80:83],v[156:159],v[200:203],v[80:83]
v_mfma_f32_16x16x32_bf16 v[112:115],v[152:155],v[180:183],v[112:115]
ds_read_b64_tr_b16 v[152:153],v243 offset:33792
ds_read_b64_tr_b16 v[154:155],v243 offset:37888
v_mfma_f32_16x16x32_bf16 v[112:115],v[156:159],v[204:207],v[112:115]
s_waitcnt lgkmcnt(14)
ds_read_b64_tr_b16 v[156:157],v243 offset:50176
ds_read_b64_tr_b16 v[158:159],v243 offset:54272
v_mfma_f32_16x16x32_bf16 v[20:23],v[160:163],v[168:171],v[20:23]
buffer_load_dwordx4 v248,s[36:39],s67 offen offset:2048 lds
v_mfma_f32_16x16x32_bf16 v[20:23],v[164:167],v[192:195],v[20:23]
v_mfma_f32_16x16x32_bf16 v[52:55],v[160:163],v[172:175],v[52:55]
v_mfma_f32_16x16x32_bf16 v[52:55],v[164:167],v[196:199],v[52:55]
buffer_load_dwordx4 v249,s[36:39],s67 offen offset:3072 lds
v_mfma_f32_16x16x32_bf16 v[84:87],v[160:163],v[176:179],v[84:87]
v_mfma_f32_16x16x32_bf16 v[84:87],v[164:167],v[200:203],v[84:87]
v_mfma_f32_16x16x32_bf16 v[116:119],v[160:163],v[180:183],v[116:119]
ds_read_b64_tr_b16 v[160:161],v242 offset:34048
ds_read_b64_tr_b16 v[162:163],v242 offset:38144
v_mfma_f32_16x16x32_bf16 v[116:119],v[164:167],v[204:207],v[116:119]
s_waitcnt lgkmcnt(14)
ds_read_b64_tr_b16 v[164:165],v242 offset:50432
ds_read_b64_tr_b16 v[166:167],v242 offset:54528
v_mfma_f32_16x16x32_bf16 v[24:27],v[128:131],v[168:171],v[24:27]
s_lshl_b32 s66,s60,10
s_add_u32 m0,s66,131072
s_add_u32 s68,s68,128
v_mfma_f32_16x16x32_bf16 v[24:27],v[132:135],v[192:195],v[24:27]
buffer_load_dwordx4 v250,s[36:39],s80 offen lds
v_mfma_f32_16x16x32_bf16 v[56:59],v[128:131],v[172:175],v[56:59]
v_mfma_f32_16x16x32_bf16 v[56:59],v[132:135],v[196:199],v[56:59]
v_mfma_f32_16x16x32_bf16 v[88:91],v[128:131],v[176:179],v[88:91]
v_mfma_f32_16x16x32_bf16 v[88:91],v[132:135],v[200:203],v[88:91]
s_add_u32 s80,s80,128
s_add_u32 s69,s69,524288
v_mfma_f32_16x16x32_bf16 v[120:123],v[128:131],v[180:183],v[120:123]
ds_read_b64_tr_b16 v[128:129],v243 offset:34048
ds_read_b64_tr_b16 v[130:131],v243 offset:38144
v_mfma_f32_16x16x32_bf16 v[120:123],v[132:135],v[204:207],v[120:123]
s_waitcnt lgkmcnt(14)
ds_read_b64_tr_b16 v[132:133],v243 offset:50432
ds_read_b64_tr_b16 v[134:135],v243 offset:54528
v_mfma_f32_16x16x32_bf16 v[28:31],v[136:139],v[168:171],v[28:31]
buffer_load_dword a160,v232,s[36:39],s27 offen
v_mfma_f32_16x16x32_bf16 v[28:31],v[140:143],v[192:195],v[28:31]
v_mfma_f32_16x16x32_bf16 v[60:63],v[136:139],v[172:175],v[60:63]
v_mfma_f32_16x16x32_bf16 v[60:63],v[140:143],v[196:199],v[60:63]
s_add_u32 s27,s27,128
v_mfma_f32_16x16x32_bf16 v[92:95],v[136:139],v[176:179],v[92:95]
v_mfma_f32_16x16x32_bf16 v[92:95],v[140:143],v[200:203],v[92:95]
buffer_load_dword a160,v233,s[40:43],s29 offen
v_mfma_f32_16x16x32_bf16 v[124:127],v[136:139],v[180:183],v[124:127]
ds_read_b64_tr_b16 v[136:137],v242 offset:34304
ds_read_b64_tr_b16 v[138:139],v242 offset:38400
v_mfma_f32_16x16x32_bf16 v[124:127],v[140:143],v[204:207],v[124:127]
s_waitcnt lgkmcnt(14)
ds_read_b64_tr_b16 v[140:141],v242 offset:50688
ds_read_b64_tr_b16 v[142:143],v242 offset:54784
v_mfma_f32_16x16x32_bf16 a[0:3],v[144:147],v[168:171],a[0:3]
v_mfma_f32_16x16x32_bf16 a[0:3],v[148:151],v[192:195],a[0:3]
s_add_u32 s29,s29,524288
v_mfma_f32_16x16x32_bf16 a[32:35],v[144:147],v[172:175],a[32:35]
v_mfma_f32_16x16x32_bf16 a[32:35],v[148:151],v[196:199],a[32:35]
v_mfma_f32_16x16x32_bf16 a[64:67],v[144:147],v[176:179],a[64:67]
v_mfma_f32_16x16x32_bf16 a[64:67],v[148:151],v[200:203],a[64:67]
v_mfma_f32_16x16x32_bf16 a[96:99],v[144:147],v[180:183],a[96:99]
ds_read_b64_tr_b16 v[144:145],v243 offset:34304
ds_read_b64_tr_b16 v[146:147],v243 offset:38400
v_mfma_f32_16x16x32_bf16 a[96:99],v[148:151],v[204:207],a[96:99]
s_waitcnt lgkmcnt(14)
ds_read_b64_tr_b16 v[148:149],v243 offset:50688
ds_read_b64_tr_b16 v[150:151],v243 offset:54784
v_mfma_f32_16x16x32_bf16 a[4:7],v[152:155],v[168:171],a[4:7]
v_mfma_f32_16x16x32_bf16 a[4:7],v[156:159],v[192:195],a[4:7]
v_mfma_f32_16x16x32_bf16 a[36:39],v[152:155],v[172:175],a[36:39]
v_mfma_f32_16x16x32_bf16 a[36:39],v[156:159],v[196:199],a[36:39]
v_mfma_f32_16x16x32_bf16 a[68:71],v[152:155],v[176:179],a[68:71]
v_mfma_f32_16x16x32_bf16 a[68:71],v[156:159],v[200:203],a[68:71]
v_mfma_f32_16x16x32_bf16 a[100:103],v[152:155],v[180:183],a[100:103]
ds_read_b64_tr_b16 v[152:153],v242 offset:34560
ds_read_b64_tr_b16 v[154:155],v242 offset:38656
v_mfma_f32_16x16x32_bf16 a[100:103],v[156:159],v[204:207],a[100:103]
s_waitcnt lgkmcnt(14)
ds_read_b64_tr_b16 v[156:157],v242 offset:50944
ds_read_b64_tr_b16 v[158:159],v242 offset:55040
v_mfma_f32_16x16x32_bf16 a[8:11],v[160:163],v[168:171],a[8:11]
v_mfma_f32_16x16x32_bf16 a[8:11],v[164:167],v[192:195],a[8:11]
v_mfma_f32_16x16x32_bf16 a[40:43],v[160:163],v[172:175],a[40:43]
v_mfma_f32_16x16x32_bf16 a[40:43],v[164:167],v[196:199],a[40:43]
v_mfma_f32_16x16x32_bf16 a[72:75],v[160:163],v[176:179],a[72:75]
v_mfma_f32_16x16x32_bf16 a[72:75],v[164:167],v[200:203],a[72:75]
v_mfma_f32_16x16x32_bf16 a[104:107],v[160:163],v[180:183],a[104:107]
ds_read_b64_tr_b16 v[160:161],v243 offset:34560
ds_read_b64_tr_b16 v[162:163],v243 offset:38656
v_mfma_f32_16x16x32_bf16 a[104:107],v[164:167],v[204:207],a[104:107]
s_waitcnt lgkmcnt(14)
ds_read_b64_tr_b16 v[164:165],v243 offset:50944
ds_read_b64_tr_b16 v[166:167],v243 offset:55040
v_mfma_f32_16x16x32_bf16 a[12:15],v[128:131],v[168:171],a[12:15]
v_mfma_f32_16x16x32_bf16 a[12:15],v[132:135],v[192:195],a[12:15]
v_mfma_f32_16x16x32_bf16 a[44:47],v[128:131],v[172:175],a[44:47]
v_mfma_f32_16x16x32_bf16 a[44:47],v[132:135],v[196:199],a[44:47]
v_mfma_f32_16x16x32_bf16 a[76:79],v[128:131],v[176:179],a[76:79]
v_mfma_f32_16x16x32_bf16 a[76:79],v[132:135],v[200:203],a[76:79]
v_mfma_f32_16x16x32_bf16 a[108:111],v[128:131],v[180:183],a[108:111]
v_mfma_f32_16x16x32_bf16 a[108:111],v[132:135],v[204:207],a[108:111]
s_waitcnt lgkmcnt(12)
v_mfma_f32_16x16x32_bf16 a[16:19],v[136:139],v[168:171],a[16:19]
v_mfma_f32_16x16x32_bf16 a[16:19],v[140:143],v[192:195],a[16:19]
v_mfma_f32_16x16x32_bf16 a[48:51],v[136:139],v[172:175],a[48:51]
v_mfma_f32_16x16x32_bf16 a[48:51],v[140:143],v[196:199],a[48:51]
v_mfma_f32_16x16x32_bf16 a[80:83],v[136:139],v[176:179],a[80:83]
v_mfma_f32_16x16x32_bf16 a[80:83],v[140:143],v[200:203],a[80:83]
v_mfma_f32_16x16x32_bf16 a[112:115],v[136:139],v[180:183],a[112:115]
v_mfma_f32_16x16x32_bf16 a[112:115],v[140:143],v[204:207],a[112:115]
s_waitcnt lgkmcnt(8)
v_mfma_f32_16x16x32_bf16 a[20:23],v[144:147],v[168:171],a[20:23]
v_mfma_f32_16x16x32_bf16 a[20:23],v[148:151],v[192:195],a[20:23]
s_waitcnt vmcnt(2) lgkmcnt(0)
s_barrier
v_mfma_f32_16x16x32_bf16 a[52:55],v[144:147],v[172:175],a[52:55]
s_add_u32 s66,s63,s63
ds_read_b64_tr_b16 v[128:129],v244
ds_read_b64_tr_b16 v[130:131],v244 offset:4096
v_mfma_f32_16x16x32_bf16 a[52:55],v[148:151],v[196:199],a[52:55]
s_mov_b32 s67,s69
ds_read_b64_tr_b16 v[132:133],v244 offset:16384
ds_read_b64_tr_b16 v[134:135],v244 offset:20480
v_mfma_f32_16x16x32_bf16 a[84:87],v[144:147],v[176:179],a[84:87]
s_add_u32 m0,s66,98304
ds_read_b64_tr_b16 v[136:137],v245
ds_read_b64_tr_b16 v[138:139],v245 offset:4096
v_mfma_f32_16x16x32_bf16 a[84:87],v[148:151],v[200:203],a[84:87]
ds_read_b64_tr_b16 v[140:141],v245 offset:16384
)A"
R"A(ds_read_b64_tr_b16 v[142:143],v245 offset:20480
v_mfma_f32_16x16x32_bf16 a[116:119],v[144:147],v[180:183],a[116:119]
ds_read_b128 a[180:183],v241
buffer_load_dwordx4 v251,s[40:43],s67 offen lds
v_mfma_f32_16x16x32_bf16 a[116:119],v[148:151],v[204:207],a[116:119]
ds_read_b128 a[204:207],v241 offset:512
ds_read_b64_tr_b16 v[144:145],v244 offset:1024
v_mfma_f32_16x16x32_bf16 a[24:27],v[152:155],v[168:171],a[24:27]
ds_read_b64_tr_b16 v[146:147],v244 offset:5120
ds_read_b128 a[184:187],v241 offset:2048
v_mfma_f32_16x16x32_bf16 a[24:27],v[156:159],v[192:195],a[24:27]
ds_read_b64_tr_b16 v[148:149],v244 offset:17408
ds_read_b64_tr_b16 v[150:151],v244 offset:21504
v_mfma_f32_16x16x32_bf16 a[56:59],v[152:155],v[172:175],a[56:59]
buffer_load_dwordx4 v252,s[40:43],s67 offen offset:1024 lds
ds_read_b128 a[208:211],v241 offset:2560
v_mfma_f32_16x16x32_bf16 a[56:59],v[156:159],v[196:199],a[56:59]
ds_read_b128 a[164:167],v240
ds_read_b128 a[188:191],v240 offset:512
v_mfma_f32_16x16x32_bf16 a[88:91],v[152:155],v[176:179],a[88:91]
buffer_load_dwordx4 v253,s[40:43],s67 offen offset:2048 lds
ds_read_b128 a[168:171],v240 offset:2048
v_mfma_f32_16x16x32_bf16 a[88:91],v[156:159],v[200:203],a[88:91]
ds_read_b128 a[192:195],v240 offset:2560
ds_read_b128 a[172:175],v240 offset:4096
v_mfma_f32_16x16x32_bf16 a[120:123],v[152:155],v[180:183],a[120:123]
ds_read_b64_tr_b16 v[152:153],v245 offset:1024
ds_read_b64_tr_b16 v[154:155],v245 offset:5120
v_mfma_f32_16x16x32_bf16 a[120:123],v[156:159],v[204:207],a[120:123]
ds_read_b64_tr_b16 v[156:157],v245 offset:17408
ds_read_b64_tr_b16 v[158:159],v245 offset:21504
v_mfma_f32_16x16x32_bf16 a[28:31],v[160:163],v[168:171],a[28:31]
buffer_load_dwordx4 v254,s[40:43],s67 offen offset:3072 lds
ds_read_b128 a[196:199],v240 offset:4608
v_mfma_f32_16x16x32_bf16 a[28:31],v[164:167],v[192:195],a[28:31]
s_add_u32 s67,s69,32768
s_add_u32 m0,s66,102400
ds_read_b128 a[176:179],v240 offset:6144
v_mfma_f32_16x16x32_bf16 a[60:63],v[160:163],v[172:175],a[60:63]
v_mfma_f32_16x16x32_bf16 a[60:63],v[164:167],v[196:199],a[60:63]
ds_read_b128 a[200:203],v240 offset:6656
buffer_load_dwordx4 v251,s[40:43],s67 offen lds
v_mfma_f32_16x16x32_bf16 a[92:95],v[160:163],v[176:179],a[92:95]
v_mfma_f32_16x16x32_bf16 a[92:95],v[164:167],v[200:203],a[92:95]
v_mfma_f32_16x16x32_bf16 a[124:127],v[160:163],v[180:183],a[124:127]
ds_read_b64_tr_b16 v[160:161],v242
ds_read_b64_tr_b16 v[162:163],v242 offset:4096
v_mfma_f32_16x16x32_bf16 a[124:127],v[164:167],v[204:207],a[124:127]
s_waitcnt lgkmcnt(14)
ds_read_b64_tr_b16 v[164:165],v242 offset:16384
v_mfma_f32_16x16x32_bf16 a[128:131],v[128:131],a[180:183],a[128:131]
ds_read_b64_tr_b16 v[166:167],v242 offset:20480
buffer_load_dwordx4 v252,s[40:43],s67 offen offset:1024 lds
v_mfma_f32_16x16x32_bf16 a[128:131],v[132:135],a[204:207],a[128:131]
v_mfma_f32_16x16x32_bf16 a[136:139],v[128:131],a[184:187],a[136:139]
ds_read_b64_tr_b16 v[128:129],v243
ds_read_b64_tr_b16 v[130:131],v243 offset:4096
v_mfma_f32_16x16x32_bf16 a[136:139],v[132:135],a[208:211],a[136:139]
ds_read_b64_tr_b16 v[132:133],v243 offset:16384
ds_read_b64_tr_b16 v[134:135],v243 offset:20480
v_mfma_f32_16x16x32_bf16 a[132:135],v[136:139],a[180:183],a[132:135]
v_mfma_f32_16x16x32_bf16 a[132:135],v[140:143],a[204:207],a[132:135]
v_mfma_f32_16x16x32_bf16 a[140:143],v[136:139],a[184:187],a[140:143]
ds_read_b64_tr_b16 v[136:137],v242 offset:256
ds_read_b64_tr_b16 v[138:139],v242 offset:4352
v_mfma_f32_16x16x32_bf16 a[140:143],v[140:143],a[208:211],a[140:143]
ds_read_b64_tr_b16 v[140:141],v242 offset:16640
ds_read_b64_tr_b16 v[142:143],v242 offset:20736
v_mfma_f32_16x16x32_bf16 a[144:147],v[144:147],a[180:183],a[144:147]
buffer_load_dwordx4 v253,s[40:43],s67 offen offset:2048 lds
v_mfma_f32_16x16x32_bf16 a[144:147],v[148:151],a[204:207],a[144:147]
v_mfma_f32_16x16x32_bf16 a[152:155],v[144:147],a[184:187],a[152:155]
ds_read_b64_tr_b16 v[144:145],v243 offset:256
ds_read_b64_tr_b16 v[146:147],v243 offset:4352
v_mfma_f32_16x16x32_bf16 a[152:155],v[148:151],a[208:211],a[152:155]
s_waitcnt lgkmcnt(15)
ds_read_b64_tr_b16 v[148:149],v243 offset:16640
ds_read_b64_tr_b16 v[150:151],v243 offset:20736
v_mfma_f32_16x16x32_bf16 a[148:151],v[152:155],a[180:183],a[148:151]
buffer_load_dwordx4 v254,s[40:43],s67 offen offset:3072 lds
v_mfma_f32_16x16x32_bf16 a[148:151],v[156:159],a[204:207],a[148:151]
v_mfma_f32_16x16x32_bf16 a[156:159],v[152:155],a[184:187],a[156:159]
ds_read_b64_tr_b16 v[152:153],v242 offset:512
ds_read_b64_tr_b16 v[154:155],v242 offset:4608
v_mfma_f32_16x16x32_bf16 a[156:159],v[156:159],a[208:211],a[156:159]
s_waitcnt lgkmcnt(14)
ds_read_b64_tr_b16 v[156:157],v242 offset:16896
v_mfma_f32_16x16x32_bf16 v[0:3],v[160:163],a[164:167],v[0:3]
s_mov_b32 s67,s68
s_add_u32 m0,s66,32768
ds_read_b64_tr_b16 v[158:159],v242 offset:20992
v_mfma_f32_16x16x32_bf16 v[0:3],v[164:167],a[188:191],v[0:3]
buffer_load_dwordx4 v246,s[36:39],s67 offen lds
v_mfma_f32_16x16x32_bf16 v[32:35],v[160:163],a[168:171],v[32:35]
v_mfma_f32_16x16x32_bf16 v[32:35],v[164:167],a[192:195],v[32:35]
v_mfma_f32_16x16x32_bf16 v[64:67],v[160:163],a[172:175],v[64:67]
v_mfma_f32_16x16x32_bf16 v[64:67],v[164:167],a[196:199],v[64:67]
v_mfma_f32_16x16x32_bf16 v[96:99],v[160:163],a[176:179],v[96:99]
ds_read_b64_tr_b16 v[160:161],v243 offset:512
ds_read_b64_tr_b16 v[162:163],v243 offset:4608
v_mfma_f32_16x16x32_bf16 v[96:99],v[164:167],a[200:203],v[96:99]
s_waitcnt lgkmcnt(14)
ds_read_b64_tr_b16 v[164:165],v243 offset:16896
ds_read_b64_tr_b16 v[166:167],v243 offset:20992
v_mfma_f32_16x16x32_bf16 v[4:7],v[128:131],a[164:167],v[4:7]
buffer_load_dwordx4 v247,s[36:39],s67 offen offset:1024 lds
v_mfma_f32_16x16x32_bf16 v[4:7],v[132:135],a[188:191],v[4:7]
v_mfma_f32_16x16x32_bf16 v[36:39],v[128:131],a[168:171],v[36:39]
v_mfma_f32_16x16x32_bf16 v[36:39],v[132:135],a[192:195],v[36:39]
v_mfma_f32_16x16x32_bf16 v[68:71],v[128:131],a[172:175],v[68:71]
buffer_load_dwordx4 v248,s[36:39],s67 offen offset:2048 lds
v_mfma_f32_16x16x32_bf16 v[68:71],v[132:135],a[196:199],v[68:71]
v_mfma_f32_16x16x32_bf16 v[100:103],v[128:131],a[176:179],v[100:103]
ds_read_b64_tr_b16 v[128:129],v242 offset:768
ds_read_b64_tr_b16 v[130:131],v242 offset:4864
v_mfma_f32_16x16x32_bf16 v[100:103],v[132:135],a[200:203],v[100:103]
s_waitcnt lgkmcnt(14)
ds_read_b64_tr_b16 v[132:133],v242 offset:17152
ds_read_b64_tr_b16 v[134:135],v242 offset:21248
v_mfma_f32_16x16x32_bf16 v[8:11],v[136:139],a[164:167],v[8:11]
v_mfma_f32_16x16x32_bf16 v[8:11],v[140:143],a[188:191],v[8:11]
buffer_load_dwordx4 v249,s[36:39],s67 offen offset:3072 lds
v_mfma_f32_16x16x32_bf16 v[40:43],v[136:139],a[168:171],v[40:43]
v_mfma_f32_16x16x32_bf16 v[40:43],v[140:143],a[192:195],v[40:43]
v_mfma_f32_16x16x32_bf16 v[72:75],v[136:139],a[172:175],v[72:75]
v_mfma_f32_16x16x32_bf16 v[72:75],v[140:143],a[196:199],v[72:75]
s_add_u32 s67,s68,40960
s_add_u32 m0,s66,36864
v_mfma_f32_16x16x32_bf16 v[104:107],v[136:139],a[176:179],v[104:107]
ds_read_b64_tr_b16 v[136:137],v243 offset:768
ds_read_b64_tr_b16 v[138:139],v243 offset:4864
v_mfma_f32_16x16x32_bf16 v[104:107],v[140:143],a[200:203],v[104:107]
s_waitcnt lgkmcnt(14)
ds_read_b64_tr_b16 v[140:141],v243 offset:17152
ds_read_b64_tr_b16 v[142:143],v243 offset:21248
v_mfma_f32_16x16x32_bf16 v[12:15],v[144:147],a[164:167],v[12:15]
buffer_load_dwordx4 v246,s[36:39],s67 offen lds
v_mfma_f32_16x16x32_bf16 v[12:15],v[148:151],a[188:191],v[12:15]
v_mfma_f32_16x16x32_bf16 v[44:47],v[144:147],a[168:171],v[44:47]
v_mfma_f32_16x16x32_bf16 v[44:47],v[148:151],a[192:195],v[44:47]
v_mfma_f32_16x16x32_bf16 v[76:79],v[144:147],a[172:175],v[76:79]
buffer_load_dwordx4 v247,s[36:39],s67 offen offset:1024 lds
v_mfma_f32_16x16x32_bf16 v[76:79],v[148:151],a[196:199],v[76:79]
v_mfma_f32_16x16x32_bf16 v[108:111],v[144:147],a[176:179],v[108:111]
ds_read_b64_tr_b16 v[144:145],v242 offset:1024
ds_read_b64_tr_b16 v[146:147],v242 offset:5120
v_mfma_f32_16x16x32_bf16 v[108:111],v[148:151],a[200:203],v[108:111]
s_waitcnt lgkmcnt(14)
ds_read_b64_tr_b16 v[148:149],v242 offset:17408
ds_read_b64_tr_b16 v[150:151],v242 offset:21504
v_mfma_f32_16x16x32_bf16 v[16:19],v[152:155],a[164:167],v[16:19]
v_mfma_f32_16x16x32_bf16 v[16:19],v[156:159],a[188:191],v[16:19]
v_mfma_f32_16x16x32_bf16 v[48:51],v[152:155],a[168:171],v[48:51]
buffer_load_dwordx4 v248,s[36:39],s67 offen offset:2048 lds
v_mfma_f32_16x16x32_bf16 v[48:51],v[156:159],a[192:195],v[48:51]
v_mfma_f32_16x16x32_bf16 v[80:83],v[152:155],a[172:175],v[80:83]
v_mfma_f32_16x16x32_bf16 v[80:83],v[156:159],a[196:199],v[80:83]
v_mfma_f32_16x16x32_bf16 v[112:115],v[152:155],a[176:179],v[112:115]
ds_read_b64_tr_b16 v[152:153],v243 offset:1024
ds_read_b64_tr_b16 v[154:155],v243 offset:5120
v_mfma_f32_16x16x32_bf16 v[112:115],v[156:159],a[200:203],v[112:115]
s_waitcnt lgkmcnt(14)
ds_read_b64_tr_b16 v[156:157],v243 offset:17408
ds_read_b64_tr_b16 v[158:159],v243 offset:21504
v_mfma_f32_16x16x32_bf16 v[20:23],v[160:163],a[164:167],v[20:23]
buffer_load_dwordx4 v249,s[36:39],s67 offen offset:3072 lds
v_mfma_f32_16x16x32_bf16 v[20:23],v[164:167],a[188:191],v[20:23]
v_mfma_f32_16x16x32_bf16 v[52:55],v[160:163],a[168:171],v[52:55]
v_mfma_f32_16x16x32_bf16 v[52:55],v[164:167],a[192:195],v[52:55]
s_lshl_b32 s66,s60,10
s_add_u32 m0,s66,135168
s_add_u32 s68,s68,128
v_mfma_f32_16x16x32_bf16 v[84:87],v[160:163],a[172:175],v[84:87]
v_mfma_f32_16x16x32_bf16 v[84:87],v[164:167],a[196:199],v[84:87]
buffer_load_dwordx4 v250,s[36:39],s80 offen lds
v_mfma_f32_16x16x32_bf16 v[116:119],v[160:163],a[176:179],v[116:119]
ds_read_b64_tr_b16 v[160:161],v242 offset:1280
ds_read_b64_tr_b16 v[162:163],v242 offset:5376
v_mfma_f32_16x16x32_bf16 v[116:119],v[164:167],a[200:203],v[116:119]
s_waitcnt lgkmcnt(14)
ds_read_b64_tr_b16 v[164:165],v242 offset:17664
ds_read_b64_tr_b16 v[166:167],v242 offset:21760
v_mfma_f32_16x16x32_bf16 v[24:27],v[128:131],a[164:167],v[24:27]
s_add_u32 s80,s80,128
s_add_u32 s69,s69,524288
v_mfma_f32_16x16x32_bf16 v[24:27],v[132:135],a[188:191],v[24:27]
v_mfma_f32_16x16x32_bf16 v[56:59],v[128:131],a[168:171],v[56:59]
buffer_load_dword a160,v232,s[36:39],s27 offen
v_mfma_f32_16x16x32_bf16 v[56:59],v[132:135],a[192:195],v[56:59]
v_mfma_f32_16x16x32_bf16 v[88:91],v[128:131],a[172:175],v[88:91]
v_mfma_f32_16x16x32_bf16 v[88:91],v[132:135],a[196:199],v[88:91]
v_mfma_f32_16x16x32_bf16 v[120:123],v[128:131],a[176:179],v[120:123]
s_add_u32 s27,s27,128
ds_read_b64_tr_b16 v[128:129],v243 offset:1280
ds_read_b64_tr_b16 v[130:131],v243 offset:5376
v_mfma_f32_16x16x32_bf16 v[120:123],v[132:135],a[200:203],v[120:123]
s_waitcnt lgkmcnt(14)
ds_read_b64_tr_b16 v[132:133],v243 offset:17664
ds_read_b64_tr_b16 v[134:135],v243 offset:21760
v_mfma_f32_16x16x32_bf16 v[28:31],v[136:139],a[164:167],v[28:31]
buffer_load_dword a160,v233,s[40:43],s29 offen
v_mfma_f32_16x16x32_bf16 v[28:31],v[140:143],a[188:191],v[28:31]
v_mfma_f32_16x16x32_bf16 v[60:63],v[136:139],a[168:171],v[60:63]
v_mfma_f32_16x16x32_bf16 v[60:63],v[140:143],a[192:195],v[60:63]
s_add_u32 s29,s29,524288
v_mfma_f32_16x16x32_bf16 v[92:95],v[136:139],a[172:175],v[92:95]
v_mfma_f32_16x16x32_bf16 v[92:95],v[140:143],a[196:199],v[92:95]
buffer_load_dword a160,v255,s[32:35],s79 offen
v_mfma_f32_16x16x32_bf16 v[124:127],v[136:139],a[176:179],v[124:127]
ds_read_b64_tr_b16 v[136:137],v242 offset:1536
ds_read_b64_tr_b16 v[138:139],v242 offset:5632
v_mfma_f32_16x16x32_bf16 v[124:127],v[140:143],a[200:203],v[124:127]
s_waitcnt lgkmcnt(14)
ds_read_b64_tr_b16 v[140:141],v242 offset:17920
ds_read_b64_tr_b16 v[142:143],v242 offset:22016
v_mfma_f32_16x16x32_bf16 a[0:3],v[144:147],a[164:167],a[0:3]
v_mfma_f32_16x16x32_bf16 a[0:3],v[148:151],a[188:191],a[0:3]
s_add_u32 s79,s79,2560
v_mfma_f32_16x16x32_bf16 a[32:35],v[144:147],a[168:171],a[32:35]
v_mfma_f32_16x16x32_bf16 a[32:35],v[148:151],a[192:195],a[32:35]
v_mfma_f32_16x16x32_bf16 a[64:67],v[144:147],a[172:175],a[64:67]
v_mfma_f32_16x16x32_bf16 a[64:67],v[148:151],a[196:199],a[64:67]
v_mfma_f32_16x16x32_bf16 a[96:99],v[144:147],a[176:179],a[96:99]
ds_read_b64_tr_b16 v[144:145],v243 offset:1536
ds_read_b64_tr_b16 v[146:147],v243 offset:5632
)A"
R"A(v_mfma_f32_16x16x32_bf16 a[96:99],v[148:151],a[200:203],a[96:99]
s_waitcnt lgkmcnt(14)
ds_read_b64_tr_b16 v[148:149],v243 offset:17920
ds_read_b64_tr_b16 v[150:151],v243 offset:22016
v_mfma_f32_16x16x32_bf16 a[4:7],v[152:155],a[164:167],a[4:7]
v_mfma_f32_16x16x32_bf16 a[4:7],v[156:159],a[188:191],a[4:7]
v_mfma_f32_16x16x32_bf16 a[36:39],v[152:155],a[168:171],a[36:39]
v_mfma_f32_16x16x32_bf16 a[36:39],v[156:159],a[192:195],a[36:39]
v_mfma_f32_16x16x32_bf16 a[68:71],v[152:155],a[172:175],a[68:71]
v_mfma_f32_16x16x32_bf16 a[68:71],v[156:159],a[196:199],a[68:71]
v_mfma_f32_16x16x32_bf16 a[100:103],v[152:155],a[176:179],a[100:103]
ds_read_b64_tr_b16 v[152:153],v242 offset:1792
ds_read_b64_tr_b16 v[154:155],v242 offset:5888
v_mfma_f32_16x16x32_bf16 a[100:103],v[156:159],a[200:203],a[100:103]
s_waitcnt lgkmcnt(14)
ds_read_b64_tr_b16 v[156:157],v242 offset:18176
ds_read_b64_tr_b16 v[158:159],v242 offset:22272
v_mfma_f32_16x16x32_bf16 a[8:11],v[160:163],a[164:167],a[8:11]
v_mfma_f32_16x16x32_bf16 a[8:11],v[164:167],a[188:191],a[8:11]
v_mfma_f32_16x16x32_bf16 a[40:43],v[160:163],a[168:171],a[40:43]
v_mfma_f32_16x16x32_bf16 a[40:43],v[164:167],a[192:195],a[40:43]
v_mfma_f32_16x16x32_bf16 a[72:75],v[160:163],a[172:175],a[72:75]
v_mfma_f32_16x16x32_bf16 a[72:75],v[164:167],a[196:199],a[72:75]
v_mfma_f32_16x16x32_bf16 a[104:107],v[160:163],a[176:179],a[104:107]
ds_read_b64_tr_b16 v[160:161],v243 offset:1792
ds_read_b64_tr_b16 v[162:163],v243 offset:5888
v_mfma_f32_16x16x32_bf16 a[104:107],v[164:167],a[200:203],a[104:107]
s_waitcnt lgkmcnt(14)
ds_read_b64_tr_b16 v[164:165],v243 offset:18176
ds_read_b64_tr_b16 v[166:167],v243 offset:22272
v_mfma_f32_16x16x32_bf16 a[12:15],v[128:131],a[164:167],a[12:15]
v_mfma_f32_16x16x32_bf16 a[12:15],v[132:135],a[188:191],a[12:15]
v_mfma_f32_16x16x32_bf16 a[44:47],v[128:131],a[168:171],a[44:47]
v_mfma_f32_16x16x32_bf16 a[44:47],v[132:135],a[192:195],a[44:47]
v_mfma_f32_16x16x32_bf16 a[76:79],v[128:131],a[172:175],a[76:79]
v_mfma_f32_16x16x32_bf16 a[76:79],v[132:135],a[196:199],a[76:79]
v_mfma_f32_16x16x32_bf16 a[108:111],v[128:131],a[176:179],a[108:111]
v_mfma_f32_16x16x32_bf16 a[108:111],v[132:135],a[200:203],a[108:111]
s_waitcnt lgkmcnt(12)
v_mfma_f32_16x16x32_bf16 a[16:19],v[136:139],a[164:167],a[16:19]
v_mfma_f32_16x16x32_bf16 a[16:19],v[140:143],a[188:191],a[16:19]
v_mfma_f32_16x16x32_bf16 a[48:51],v[136:139],a[168:171],a[48:51]
v_mfma_f32_16x16x32_bf16 a[48:51],v[140:143],a[192:195],a[48:51]
v_mfma_f32_16x16x32_bf16 a[80:83],v[136:139],a[172:175],a[80:83]
v_mfma_f32_16x16x32_bf16 a[80:83],v[140:143],a[196:199],a[80:83]
v_mfma_f32_16x16x32_bf16 a[112:115],v[136:139],a[176:179],a[112:115]
v_mfma_f32_16x16x32_bf16 a[112:115],v[140:143],a[200:203],a[112:115]
s_waitcnt lgkmcnt(8)
v_mfma_f32_16x16x32_bf16 a[20:23],v[144:147],a[164:167],a[20:23]
s_sub_u32 s64,s64,1
s_cmp_lg_u32 s64,0
s_cbranch_scc1 .Lq
v_mfma_f32_16x16x32_bf16 a[20:23],v[148:151],a[188:191],a[20:23]
s_waitcnt vmcnt(3) lgkmcnt(0)
s_barrier
v_mfma_f32_16x16x32_bf16 a[52:55],v[144:147],a[168:171],a[52:55]
ds_read_b64_tr_b16 v[128:129],v242 offset:32768
ds_read_b64_tr_b16 v[130:131],v242 offset:36864
v_mfma_f32_16x16x32_bf16 a[52:55],v[148:151],a[192:195],a[52:55]
ds_read_b64_tr_b16 v[132:133],v242 offset:49152
ds_read_b64_tr_b16 v[134:135],v242 offset:53248
v_mfma_f32_16x16x32_bf16 a[84:87],v[144:147],a[172:175],a[84:87]
ds_read_b64_tr_b16 v[136:137],v243 offset:32768
ds_read_b64_tr_b16 v[138:139],v243 offset:36864
v_mfma_f32_16x16x32_bf16 a[84:87],v[148:151],a[196:199],a[84:87]
v_lshlrev_b32 v251,3,v239
s_lshl_b32 s73,s28,2
s_add_u32 s73,s73,s60
s_mul_i32 s73,s73,768
s_add_u32 s73,s73,.Litem0-.Lk1pc
v_min_u32 v252,640,v251
v_add_u32 v252,s73,v252
v_min_u32 v252,.Lend-.Lk1pc,v252
global_load_dword v253,v252,s[30:31]
v_mfma_f32_16x16x32_bf16 a[116:119],v[144:147],a[176:179],a[116:119]
s_add_u32 m0,s63,0
ds_read_b64_tr_b16 v[140:141],v243 offset:49152
ds_read_b64_tr_b16 v[142:143],v243 offset:53248
v_mfma_f32_16x16x32_bf16 a[116:119],v[148:151],a[200:203],a[116:119]
ds_read_b128 v[168:171],v240 offset:32768
buffer_load_dwordx4 v235,s[44:47],s61 offen nt lds
v_mfma_f32_16x16x32_bf16 a[24:27],v[152:155],a[164:167],a[24:27]
s_lshl_b32 s66,s60,10
ds_read_b128 v[192:195],v240 offset:33280
ds_read_b64_tr_b16 v[144:145],v242 offset:33024
v_mfma_f32_16x16x32_bf16 a[24:27],v[156:159],a[188:191],a[24:27]
s_add_u32 s66,s66,131072
ds_read_b64_tr_b16 v[146:147],v242 offset:37120
ds_read_b128 v[172:175],v240 offset:34816
v_mfma_f32_16x16x32_bf16 a[56:59],v[152:155],a[168:171],a[56:59]
ds_read_b64_tr_b16 v[148:149],v242 offset:49408
ds_read_b64_tr_b16 v[150:151],v242 offset:53504
v_mfma_f32_16x16x32_bf16 a[56:59],v[156:159],a[192:195],a[56:59]
ds_read_b128 v[196:199],v240 offset:35328
v_add_u32 v221,s66,v239
v_mfma_f32_16x16x32_bf16 a[88:91],v[152:155],a[172:175],a[88:91]
s_lshl_b32 s66,s60,13
s_add_u32 s67,s66,49152
s_add_u32 s66,s66,131072
v_mfma_f32_16x16x32_bf16 a[88:91],v[156:159],a[196:199],a[88:91]
s_cmp_lt_u32 s60,2
s_cselect_b32 s66,s67,s66
ds_read_b128 v[176:179],v240 offset:36864
v_mfma_f32_16x16x32_bf16 a[120:123],v[152:155],a[176:179],a[120:123]
ds_read_b128 v[200:203],v240 offset:37376
v_add_u32 v222,s66,v239
v_mfma_f32_16x16x32_bf16 a[120:123],v[156:159],a[200:203],a[120:123]
s_mov_b32 s74,s66
v_lshrrev_b32 v216,4,v239
v_mfma_f32_16x16x32_bf16 a[28:31],v[160:163],a[164:167],a[28:31]
ds_read_b128 v[180:183],v240 offset:38912
ds_read_b128 v[204:207],v240 offset:39424
v_mfma_f32_16x16x32_bf16 a[28:31],v[164:167],a[188:191],a[28:31]
v_bfe_u32 v217,v216,2,2
v_bfe_u32 v218,v216,4,1
v_mfma_f32_16x16x32_bf16 a[60:63],v[160:163],a[168:171],a[60:63]
ds_read_b64_tr_b16 v[152:153],v243 offset:33024
v_lshrrev_b32 v219,5,v216
v_mfma_f32_16x16x32_bf16 a[60:63],v[164:167],a[192:195],a[60:63]
ds_read_b64_tr_b16 v[154:155],v243 offset:37120
v_lshl_or_b32 v218,v218,1,v219
v_bfe_u32 v219,v216,3,1
v_mfma_f32_16x16x32_bf16 a[92:95],v[160:163],a[172:175],a[92:95]
ds_read_b64_tr_b16 v[156:157],v243 offset:49408
ds_read_b64_tr_b16 v[158:159],v243 offset:53504
v_mfma_f32_16x16x32_bf16 a[92:95],v[164:167],a[196:199],a[92:95]
v_xor_b32 v218,v218,v219
v_lshlrev_b32 v218,4,v218
v_lshrrev_b32 v219,4,v216
v_lshl_or_b32 v218,v217,10,v218
v_mfma_f32_16x16x32_bf16 a[124:127],v[160:163],a[176:179],a[124:127]
buffer_load_dwordx4 v235,s[44:47],s61 offen offset:1024 nt lds
v_and_b32 v220,3,v216
v_mfma_f32_16x16x32_bf16 a[124:127],v[164:167],a[200:203],a[124:127]
s_waitcnt lgkmcnt(4)
ds_read_b128 v[184:187],v241 offset:4096
v_lshl_or_b32 v232,v220,6,v218
v_mfma_f32_16x16x32_bf16 v[0:3],v[128:131],v[168:171],v[0:3]
s_and_b32 s66,s60,1
s_lshl_b32 s66,s66,13
v_add_u32 v232,s66,v232
v_mfma_f32_16x16x32_bf16 v[0:3],v[132:135],v[192:195],v[0:3]
ds_read_b128 v[208:211],v241 offset:4608
v_xor_b32 v226,8192,v232
v_lshlrev_b32 v218,6,v217
v_lshl_or_b32 v218,v220,3,v218
v_mfma_f32_16x16x32_bf16 v[32:35],v[128:131],v[172:175],v[32:35]
v_lshl_or_b32 v218,v219,10,v218
v_bfe_u32 v219,v216,4,1
v_mfma_f32_16x16x32_bf16 v[32:35],v[132:135],v[196:199],v[32:35]
ds_read_b64_tr_b16 v[160:161],v242 offset:33280
ds_read_b64_tr_b16 v[162:163],v242 offset:37376
ds_read_b128 v[188:191],v241 offset:6144
v_lshl_or_b32 v218,v219,5,v218
v_mfma_f32_16x16x32_bf16 v[64:67],v[128:131],v[176:179],v[64:67]
s_add_u32 s66,s66,65536
ds_read_b64_tr_b16 v[164:165],v242 offset:49664
ds_read_b64_tr_b16 v[166:167],v242 offset:53760
v_mfma_f32_16x16x32_bf16 v[64:67],v[132:135],v[200:203],v[64:67]
ds_read_b128 v[212:215],v241 offset:6656
v_add_u32 v233,s66,v218
v_xor_b32 v234,32,v233
v_xor_b32 v230,8192,v233
v_xor_b32 v231,8192,v234
v_mfma_f32_16x16x32_bf16 v[96:99],v[128:131],v[180:183],v[96:99]
s_lshr_b32 s66,s60,1
s_lshl_b32 s67,s66,12
s_add_u32 s67,s67,131072
v_mfma_f32_16x16x32_bf16 v[96:99],v[132:135],v[204:207],v[96:99]
ds_read_b64_tr_b16 v[128:129],v243 offset:33280
v_add_u32 v252,s67,v239
v_mfma_f32_16x16x32_bf16 v[4:7],v[136:139],v[168:171],v[4:7]
s_lshl_b32 s81,s66,10
s_and_b32 s66,s60,1
s_mul_i32 s66,s66,12288
v_mfma_f32_16x16x32_bf16 v[4:7],v[140:143],v[192:195],v[4:7]
s_add_u32 s81,s81,s66
s_add_u32 s81,s81,1576960
s_lshl_b32 s66,s60,1
v_mfma_f32_16x16x32_bf16 v[36:39],v[136:139],v[172:175],v[36:39]
s_mul_i32 s67,s66,171
s_lshr_b32 s67,s67,9
ds_read_b64_tr_b16 v[130:131],v243 offset:37376
v_mfma_f32_16x16x32_bf16 v[36:39],v[140:143],v[196:199],v[36:39]
s_lshl_b32 s27,s66,11
ds_read_b64_tr_b16 v[132:133],v243 offset:49664
ds_read_b64_tr_b16 v[134:135],v243 offset:53760
v_mfma_f32_16x16x32_bf16 v[68:71],v[136:139],v[176:179],v[68:71]
s_mul_i32 s67,s67,780288
s_add_u32 s27,s27,s67
s_lshl_b32 s66,s60,1
v_mfma_f32_16x16x32_bf16 v[68:71],v[140:143],v[200:203],v[68:71]
s_add_u32 s66,s66,1
s_mul_i32 s67,s66,171
buffer_load_dwordx4 v237,s[44:47],s61 offen offset:2048 nt lds
v_mfma_f32_16x16x32_bf16 v[100:103],v[136:139],v[180:183],v[100:103]
s_lshr_b32 s67,s67,9
s_lshl_b32 s29,s66,11
s_mul_i32 s67,s67,780288
v_mfma_f32_16x16x32_bf16 v[100:103],v[140:143],v[204:207],v[100:103]
s_add_u32 s29,s29,s67
s_mov_b64 s[36:37],s[52:53]
s_mov_b32 s38,0
s_mov_b64 s[40:41],s[12:13]
s_mov_b32 s42,0x10080
s_and_b32 s68,s24,0xffff
s_lshr_b32 s69,s24,16
s_lshl_b32 s66,s60,10
s_add_u32 s69,s69,s66
s_add_u32 s59,s59,0x9e3779b9
v_mov_b32 v224,0x20000
v_mov_b32 v225,s59
s_lshl_b32 s66,s60,9
s_add_u32 s66,s66,131072
v_lshl_add_u32 v227,v216,3,s66
v_mfma_f32_16x16x32_bf16 v[8:11],v[144:147],v[168:171],v[8:11]
ds_read_b64_tr_b16 v[136:137],v242 offset:33536
ds_read_b64_tr_b16 v[138:139],v242 offset:37632
v_mfma_f32_16x16x32_bf16 v[8:11],v[148:151],v[192:195],v[8:11]
ds_read_b64_tr_b16 v[140:141],v242 offset:49920
ds_read_b64_tr_b16 v[142:143],v242 offset:54016
v_mfma_f32_16x16x32_bf16 v[40:43],v[144:147],v[172:175],v[40:43]
v_mul_f32 v0,s76,v0
v_mul_f32 v32,s76,v32
v_mfma_f32_16x16x32_bf16 v[40:43],v[148:151],v[196:199],v[40:43]
v_mul_f32 v1,s76,v1
v_mul_f32 v33,s76,v33
v_mfma_f32_16x16x32_bf16 v[72:75],v[144:147],v[176:179],v[72:75]
v_mul_f32 v2,s76,v2
v_mul_f32 v34,s76,v34
v_mfma_f32_16x16x32_bf16 v[72:75],v[148:151],v[200:203],v[72:75]
v_mul_f32 v3,s76,v3
v_mul_f32 v35,s76,v35
v_mul_f32 v4,s76,v4
v_mfma_f32_16x16x32_bf16 v[104:107],v[144:147],v[180:183],v[104:107]
v_mul_f32 v36,s76,v36
v_mul_f32 v5,s76,v5
v_mfma_f32_16x16x32_bf16 v[104:107],v[148:151],v[204:207],v[104:107]
s_waitcnt lgkmcnt(15)
v_mul_f32 v37,s76,v37
v_mul_f32 v6,s76,v6
v_mul_f32 v38,s76,v38
v_mul_f32 v7,s76,v7
v_mul_f32 v39,s76,v39
v_cvt_pk_bf16_f32 v0,v0,v1
v_cvt_pk_bf16_f32 v32,v32,v33
v_mfma_f32_16x16x32_bf16 v[12:15],v[152:155],v[168:171],v[12:15]
v_cvt_pk_bf16_f32 v1,v2,v3
v_cvt_pk_bf16_f32 v33,v34,v35
v_mfma_f32_16x16x32_bf16 v[12:15],v[156:159],v[192:195],v[12:15]
v_cvt_pk_bf16_f32 v2,v4,v5
v_cvt_pk_bf16_f32 v34,v36,v37
v_cvt_pk_bf16_f32 v3,v6,v7
v_cvt_pk_bf16_f32 v35,v38,v39
v_permlane16_swap_b32 v0,v2
v_permlane16_swap_b32 v32,v34
v_mfma_f32_16x16x32_bf16 v[44:47],v[152:155],v[172:175],v[44:47]
v_permlane16_swap_b32 v1,v3
v_mfma_f32_16x16x32_bf16 v[44:47],v[156:159],v[196:199],v[44:47]
ds_read_b64_tr_b16 v[144:145],v243 offset:33536
ds_read_b64_tr_b16 v[146:147],v243 offset:37632
v_permlane16_swap_b32 v33,v35
v_accvgpr_write_b32 a192,v0
v_accvgpr_write_b32 a208,v32
v_accvgpr_write_b32 a193,v1
v_mfma_f32_16x16x32_bf16 v[76:79],v[152:155],v[176:179],v[76:79]
ds_read_b64_tr_b16 v[148:149],v243 offset:49920
ds_read_b64_tr_b16 v[150:151],v243 offset:54016
v_mfma_f32_16x16x32_bf16 v[76:79],v[156:159],v[200:203],v[76:79]
buffer_load_dwordx4 v237,s[44:47],s61 offen offset:3072 nt lds
v_accvgpr_write_b32 a209,v33
v_accvgpr_write_b32 a194,v2
v_accvgpr_write_b32 a210,v34
v_accvgpr_write_b32 a195,v3
v_accvgpr_write_b32 a211,v35
v_mul_f32 v64,s76,v64
v_mul_f32 v96,s76,v96
v_mul_f32 v65,s76,v65
v_mul_f32 v97,s76,v97
v_mfma_f32_16x16x32_bf16 v[108:111],v[152:155],v[180:183],v[108:111]
v_mul_f32 v66,s76,v66
v_mul_f32 v98,s76,v98
v_mfma_f32_16x16x32_bf16 v[108:111],v[156:159],v[204:207],v[108:111]
s_waitcnt lgkmcnt(13)
v_mul_f32 v67,s76,v67
v_mul_f32 v99,s76,v99
v_mul_f32 v68,s76,v68
v_mul_f32 v100,s76,v100
v_mul_f32 v69,s76,v69
v_mul_f32 v101,s76,v101
)A"
R"A(v_mul_f32 v70,s76,v70
v_mfma_f32_16x16x32_bf16 v[16:19],v[160:163],v[168:171],v[16:19]
v_mul_f32 v102,s76,v102
v_mul_f32 v71,s76,v71
v_mfma_f32_16x16x32_bf16 v[16:19],v[164:167],v[192:195],v[16:19]
s_add_u32 m0,s63,65536
v_mul_f32 v103,s76,v103
v_cvt_pk_bf16_f32 v64,v64,v65
v_cvt_pk_bf16_f32 v96,v96,v97
v_cvt_pk_bf16_f32 v65,v66,v67
v_cvt_pk_bf16_f32 v97,v98,v99
v_cvt_pk_bf16_f32 v66,v68,v69
v_cvt_pk_bf16_f32 v98,v100,v101
v_mfma_f32_16x16x32_bf16 v[48:51],v[160:163],v[172:175],v[48:51]
v_cvt_pk_bf16_f32 v67,v70,v71
v_cvt_pk_bf16_f32 v99,v102,v103
v_mfma_f32_16x16x32_bf16 v[48:51],v[164:167],v[196:199],v[48:51]
ds_read_b64_tr_b16 v[152:153],v244 offset:32768
ds_read_b64_tr_b16 v[154:155],v244 offset:36864
v_permlane16_swap_b32 v64,v66
v_permlane16_swap_b32 v96,v98
v_permlane16_swap_b32 v65,v67
v_mfma_f32_16x16x32_bf16 v[80:83],v[160:163],v[176:179],v[80:83]
ds_read_b64_tr_b16 v[156:157],v244 offset:49152
ds_read_b64_tr_b16 v[158:159],v244 offset:53248
v_mfma_f32_16x16x32_bf16 v[80:83],v[164:167],v[200:203],v[80:83]
v_permlane16_swap_b32 v97,v99
v_accvgpr_write_b32 a224,v64
v_accvgpr_write_b32 a240,v96
v_accvgpr_write_b32 a225,v65
v_accvgpr_write_b32 a241,v97
v_accvgpr_write_b32 a226,v66
v_accvgpr_write_b32 a242,v98
v_accvgpr_write_b32 a227,v67
v_mfma_f32_16x16x32_bf16 v[112:115],v[160:163],v[180:183],v[112:115]
v_accvgpr_write_b32 a243,v99
v_mul_f32 v8,s76,v8
v_mfma_f32_16x16x32_bf16 v[112:115],v[164:167],v[204:207],v[112:115]
s_waitcnt lgkmcnt(12)
v_mul_f32 v40,s76,v40
v_mul_f32 v9,s76,v9
v_mul_f32 v41,s76,v41
v_mul_f32 v10,s76,v10
v_mul_f32 v42,s76,v42
v_mul_f32 v11,s76,v11
v_mul_f32 v43,s76,v43
v_mfma_f32_16x16x32_bf16 v[20:23],v[128:131],v[168:171],v[20:23]
v_mul_f32 v12,s76,v12
v_mul_f32 v44,s76,v44
v_mfma_f32_16x16x32_bf16 v[20:23],v[132:135],v[192:195],v[20:23]
v_mul_f32 v13,s76,v13
v_mul_f32 v45,s76,v45
v_mul_f32 v14,s76,v14
v_mul_f32 v46,s76,v46
v_mul_f32 v15,s76,v15
v_mul_f32 v47,s76,v47
v_cvt_pk_bf16_f32 v8,v8,v9
v_mfma_f32_16x16x32_bf16 v[52:55],v[128:131],v[172:175],v[52:55]
v_cvt_pk_bf16_f32 v40,v40,v41
v_cvt_pk_bf16_f32 v9,v10,v11
v_mfma_f32_16x16x32_bf16 v[52:55],v[132:135],v[196:199],v[52:55]
ds_read_b64_tr_b16 v[160:161],v245 offset:32768
ds_read_b64_tr_b16 v[162:163],v245 offset:36864
v_cvt_pk_bf16_f32 v41,v42,v43
v_cvt_pk_bf16_f32 v10,v12,v13
v_cvt_pk_bf16_f32 v42,v44,v45
v_cvt_pk_bf16_f32 v11,v14,v15
v_cvt_pk_bf16_f32 v43,v46,v47
v_permlane16_swap_b32 v8,v10
v_mfma_f32_16x16x32_bf16 v[84:87],v[128:131],v[176:179],v[84:87]
ds_read_b64_tr_b16 v[164:165],v245 offset:49152
ds_read_b64_tr_b16 v[166:167],v245 offset:53248
v_mfma_f32_16x16x32_bf16 v[84:87],v[132:135],v[200:203],v[84:87]
buffer_load_dwordx4 v235,s[48:51],s61 offen nt lds
v_permlane16_swap_b32 v40,v42
v_permlane16_swap_b32 v9,v11
v_permlane16_swap_b32 v41,v43
v_accvgpr_write_b32 a196,v8
v_accvgpr_write_b32 a212,v40
v_mfma_f32_16x16x32_bf16 v[116:119],v[128:131],v[180:183],v[116:119]
v_accvgpr_write_b32 a197,v9
v_accvgpr_write_b32 a213,v41
v_mfma_f32_16x16x32_bf16 v[116:119],v[132:135],v[204:207],v[116:119]
s_waitcnt lgkmcnt(12)
v_accvgpr_write_b32 a198,v10
v_accvgpr_write_b32 a214,v42
v_accvgpr_write_b32 a199,v11
v_accvgpr_write_b32 a215,v43
v_mul_f32 v72,s76,v72
v_mul_f32 v104,s76,v104
v_mul_f32 v73,s76,v73
v_mfma_f32_16x16x32_bf16 v[24:27],v[136:139],v[168:171],v[24:27]
v_mul_f32 v105,s76,v105
v_mul_f32 v74,s76,v74
v_mfma_f32_16x16x32_bf16 v[24:27],v[140:143],v[192:195],v[24:27]
v_mul_f32 v106,s76,v106
v_mul_f32 v75,s76,v75
v_mul_f32 v107,s76,v107
v_mul_f32 v76,s76,v76
v_mul_f32 v108,s76,v108
v_mul_f32 v77,s76,v77
v_mul_f32 v109,s76,v109
v_mfma_f32_16x16x32_bf16 v[56:59],v[136:139],v[172:175],v[56:59]
v_mul_f32 v78,s76,v78
v_mul_f32 v110,s76,v110
v_mfma_f32_16x16x32_bf16 v[56:59],v[140:143],v[196:199],v[56:59]
ds_read_b64_tr_b16 v[128:129],v244 offset:33792
ds_read_b64_tr_b16 v[130:131],v244 offset:37888
v_mul_f32 v79,s76,v79
v_mul_f32 v111,s76,v111
v_cvt_pk_bf16_f32 v72,v72,v73
v_cvt_pk_bf16_f32 v104,v104,v105
v_cvt_pk_bf16_f32 v73,v74,v75
v_cvt_pk_bf16_f32 v105,v106,v107
v_mfma_f32_16x16x32_bf16 v[88:91],v[136:139],v[176:179],v[88:91]
ds_read_b64_tr_b16 v[132:133],v244 offset:50176
ds_read_b64_tr_b16 v[134:135],v244 offset:54272
v_mfma_f32_16x16x32_bf16 v[88:91],v[140:143],v[200:203],v[88:91]
v_cvt_pk_bf16_f32 v74,v76,v77
v_cvt_pk_bf16_f32 v106,v108,v109
v_cvt_pk_bf16_f32 v75,v78,v79
v_cvt_pk_bf16_f32 v107,v110,v111
v_permlane16_swap_b32 v72,v74
v_permlane16_swap_b32 v104,v106
v_permlane16_swap_b32 v73,v75
v_mfma_f32_16x16x32_bf16 v[120:123],v[136:139],v[180:183],v[120:123]
v_permlane16_swap_b32 v105,v107
v_mfma_f32_16x16x32_bf16 v[120:123],v[140:143],v[204:207],v[120:123]
s_waitcnt lgkmcnt(12)
v_accvgpr_write_b32 a228,v72
v_accvgpr_write_b32 a244,v104
v_accvgpr_write_b32 a229,v73
v_accvgpr_write_b32 a245,v105
v_accvgpr_write_b32 a230,v74
v_accvgpr_write_b32 a246,v106
v_mfma_f32_16x16x32_bf16 v[28:31],v[144:147],v[168:171],v[28:31]
v_accvgpr_write_b32 a231,v75
v_accvgpr_write_b32 a247,v107
v_mfma_f32_16x16x32_bf16 v[28:31],v[148:151],v[192:195],v[28:31]
v_mul_f32 v16,s76,v16
v_mul_f32 v48,s76,v48
v_mul_f32 v17,s76,v17
v_mul_f32 v49,s76,v49
v_mul_f32 v18,s76,v18
v_mul_f32 v50,s76,v50
v_mul_f32 v19,s76,v19
v_mfma_f32_16x16x32_bf16 v[60:63],v[144:147],v[172:175],v[60:63]
v_mul_f32 v51,s76,v51
v_mul_f32 v20,s76,v20
v_mfma_f32_16x16x32_bf16 v[60:63],v[148:151],v[196:199],v[60:63]
ds_read_b64_tr_b16 v[136:137],v245 offset:33792
ds_read_b64_tr_b16 v[138:139],v245 offset:37888
v_mul_f32 v52,s76,v52
v_mul_f32 v21,s76,v21
v_mul_f32 v53,s76,v53
v_mul_f32 v22,s76,v22
v_mul_f32 v54,s76,v54
v_mul_f32 v23,s76,v23
v_mfma_f32_16x16x32_bf16 v[92:95],v[144:147],v[176:179],v[92:95]
ds_read_b64_tr_b16 v[140:141],v245 offset:50176
ds_read_b64_tr_b16 v[142:143],v245 offset:54272
v_mfma_f32_16x16x32_bf16 v[92:95],v[148:151],v[200:203],v[92:95]
buffer_load_dwordx4 v236,s[48:51],s61 offen offset:1024 nt lds
v_mul_f32 v55,s76,v55
v_cvt_pk_bf16_f32 v16,v16,v17
v_cvt_pk_bf16_f32 v48,v48,v49
v_cvt_pk_bf16_f32 v17,v18,v19
v_cvt_pk_bf16_f32 v49,v50,v51
v_cvt_pk_bf16_f32 v18,v20,v21
v_cvt_pk_bf16_f32 v50,v52,v53
v_cvt_pk_bf16_f32 v19,v22,v23
v_cvt_pk_bf16_f32 v51,v54,v55
v_mfma_f32_16x16x32_bf16 v[124:127],v[144:147],v[180:183],v[124:127]
v_permlane16_swap_b32 v16,v18
v_mfma_f32_16x16x32_bf16 v[124:127],v[148:151],v[204:207],v[124:127]
s_waitcnt lgkmcnt(12)
ds_read_b64_tr_b16 v[144:145],v242 offset:33792
v_permlane16_swap_b32 v48,v50
v_permlane16_swap_b32 v17,v19
v_permlane16_swap_b32 v49,v51
v_accvgpr_write_b32 a200,v16
v_mfma_f32_16x16x32_bf16 a[128:131],v[152:155],v[184:187],a[128:131]
v_accvgpr_write_b32 a216,v48
v_accvgpr_write_b32 a201,v17
v_mfma_f32_16x16x32_bf16 a[128:131],v[156:159],v[208:211],a[128:131]
v_accvgpr_write_b32 a217,v49
v_accvgpr_write_b32 a202,v18
v_accvgpr_write_b32 a218,v50
v_accvgpr_write_b32 a203,v19
v_accvgpr_write_b32 a219,v51
v_mul_f32 v80,s76,v80
v_mul_f32 v112,s76,v112
v_mfma_f32_16x16x32_bf16 a[136:139],v[152:155],v[188:191],a[136:139]
v_mul_f32 v81,s76,v81
v_mul_f32 v113,s76,v113
v_mfma_f32_16x16x32_bf16 a[136:139],v[156:159],v[212:215],a[136:139]
s_waitcnt lgkmcnt(9)
ds_read_b64_tr_b16 v[146:147],v242 offset:37888
v_mul_f32 v82,s76,v82
v_mul_f32 v114,s76,v114
v_mul_f32 v83,s76,v83
v_mul_f32 v115,s76,v115
v_mul_f32 v84,s76,v84
v_mul_f32 v116,s76,v116
v_mfma_f32_16x16x32_bf16 a[132:135],v[160:163],v[184:187],a[132:135]
ds_read_b64_tr_b16 v[148:149],v242 offset:50176
ds_read_b64_tr_b16 v[150:151],v242 offset:54272
v_mfma_f32_16x16x32_bf16 a[132:135],v[164:167],v[208:211],a[132:135]
v_mul_f32 v85,s76,v85
v_mul_f32 v117,s76,v117
v_mul_f32 v86,s76,v86
v_mul_f32 v118,s76,v118
v_mul_f32 v87,s76,v87
v_mul_f32 v119,s76,v119
v_cvt_pk_bf16_f32 v80,v80,v81
v_cvt_pk_bf16_f32 v112,v112,v113
v_cvt_pk_bf16_f32 v81,v82,v83
v_mfma_f32_16x16x32_bf16 a[140:143],v[160:163],v[188:191],a[140:143]
v_cvt_pk_bf16_f32 v113,v114,v115
v_cvt_pk_bf16_f32 v82,v84,v85
v_mfma_f32_16x16x32_bf16 a[140:143],v[164:167],v[212:215],a[140:143]
s_waitcnt lgkmcnt(8)
ds_read_b64_tr_b16 v[152:153],v243 offset:33792
ds_read_b64_tr_b16 v[154:155],v243 offset:37888
v_cvt_pk_bf16_f32 v114,v116,v117
v_cvt_pk_bf16_f32 v83,v86,v87
v_cvt_pk_bf16_f32 v115,v118,v119
v_permlane16_swap_b32 v80,v82
v_permlane16_swap_b32 v112,v114
v_mfma_f32_16x16x32_bf16 a[144:147],v[128:131],v[184:187],a[144:147]
ds_read_b64_tr_b16 v[156:157],v243 offset:50176
ds_read_b64_tr_b16 v[158:159],v243 offset:54272
v_mfma_f32_16x16x32_bf16 a[144:147],v[132:135],v[208:211],a[144:147]
v_permlane16_swap_b32 v81,v83
v_permlane16_swap_b32 v113,v115
v_accvgpr_write_b32 a232,v80
v_accvgpr_write_b32 a248,v112
v_accvgpr_write_b32 a233,v81
v_accvgpr_write_b32 a249,v113
v_accvgpr_write_b32 a234,v82
v_mfma_f32_16x16x32_bf16 a[152:155],v[128:131],v[188:191],a[152:155]
v_accvgpr_write_b32 a250,v114
v_accvgpr_write_b32 a235,v83
v_mfma_f32_16x16x32_bf16 a[152:155],v[132:135],v[212:215],a[152:155]
s_waitcnt lgkmcnt(8)
ds_read_b64_tr_b16 v[160:161],v242 offset:34048
ds_read_b64_tr_b16 v[162:163],v242 offset:38144
v_accvgpr_write_b32 a251,v115
v_mul_f32 v24,s76,v24
v_mul_f32 v56,s76,v56
v_mul_f32 v25,s76,v25
v_mul_f32 v57,s76,v57
v_mul_f32 v26,s76,v26
v_mfma_f32_16x16x32_bf16 a[148:151],v[136:139],v[184:187],a[148:151]
ds_read_b64_tr_b16 v[164:165],v242 offset:50432
ds_read_b64_tr_b16 v[166:167],v242 offset:54528
v_mfma_f32_16x16x32_bf16 a[148:151],v[140:143],v[208:211],a[148:151]
buffer_load_dwordx4 v235,s[48:51],s61 offen offset:2048 nt lds
v_mul_f32 v58,s76,v58
v_mul_f32 v27,s76,v27
v_mul_f32 v59,s76,v59
v_mul_f32 v28,s76,v28
v_mul_f32 v60,s76,v60
v_mul_f32 v29,s76,v29
v_mul_f32 v61,s76,v61
v_mul_f32 v30,s76,v30
v_mul_f32 v62,s76,v62
v_mfma_f32_16x16x32_bf16 a[156:159],v[136:139],v[188:191],a[156:159]
v_mul_f32 v31,s76,v31
v_mul_f32 v63,s76,v63
v_mfma_f32_16x16x32_bf16 a[156:159],v[140:143],v[212:215],a[156:159]
s_waitcnt lgkmcnt(8)
ds_read_b64_tr_b16 v[128:129],v243 offset:34048
ds_read_b64_tr_b16 v[130:131],v243 offset:38144
v_cvt_pk_bf16_f32 v24,v24,v25
v_cvt_pk_bf16_f32 v56,v56,v57
v_cvt_pk_bf16_f32 v25,v26,v27
v_cvt_pk_bf16_f32 v57,v58,v59
v_cvt_pk_bf16_f32 v26,v28,v29
v_cvt_pk_bf16_f32 v58,v60,v61
v_cvt_pk_bf16_f32 v27,v30,v31
v_mfma_f32_16x16x32_bf16 a[0:3],v[144:147],v[168:171],a[0:3]
ds_read_b64_tr_b16 v[132:133],v243 offset:50432
ds_read_b64_tr_b16 v[134:135],v243 offset:54528
v_mfma_f32_16x16x32_bf16 a[0:3],v[148:151],v[192:195],a[0:3]
ds_read_b64_tr_b16 v[136:137],v242 offset:34304
v_cvt_pk_bf16_f32 v59,v62,v63
v_permlane16_swap_b32 v24,v26
v_permlane16_swap_b32 v56,v58
v_permlane16_swap_b32 v25,v27
v_permlane16_swap_b32 v57,v59
v_mfma_f32_16x16x32_bf16 a[32:35],v[144:147],v[172:175],a[32:35]
v_accvgpr_write_b32 a204,v24
v_accvgpr_write_b32 a220,v56
v_mfma_f32_16x16x32_bf16 a[32:35],v[148:151],v[196:199],a[32:35]
ds_read_b64_tr_b16 v[138:139],v242 offset:38400
v_accvgpr_write_b32 a205,v25
v_accvgpr_write_b32 a221,v57
v_accvgpr_write_b32 a206,v26
v_accvgpr_write_b32 a222,v58
v_accvgpr_write_b32 a207,v27
v_accvgpr_write_b32 a223,v59
v_mfma_f32_16x16x32_bf16 a[64:67],v[144:147],v[176:179],a[64:67]
ds_read_b64_tr_b16 v[140:141],v242 offset:50688
ds_read_b64_tr_b16 v[142:143],v242 offset:54784
v_mfma_f32_16x16x32_bf16 a[64:67],v[148:151],v[200:203],a[64:67]
v_mul_f32 v88,s76,v88
v_mul_f32 v120,s76,v120
v_mul_f32 v89,s76,v89
v_mul_f32 v121,s76,v121
v_mul_f32 v90,s76,v90
v_mul_f32 v122,s76,v122
v_mul_f32 v91,s76,v91
v_mul_f32 v123,s76,v123
v_mul_f32 v92,s76,v92
v_mfma_f32_16x16x32_bf16 a[96:99],v[144:147],v[180:183],a[96:99]
v_mul_f32 v124,s76,v124
v_mul_f32 v93,s76,v93
v_mfma_f32_16x16x32_bf16 a[96:99],v[148:151],v[204:207],a[96:99]
s_waitcnt lgkmcnt(12)
v_mul_f32 v125,s76,v125
v_mul_f32 v94,s76,v94
v_mul_f32 v126,s76,v126
v_mul_f32 v95,s76,v95
v_mul_f32 v127,s76,v127
v_cvt_pk_bf16_f32 v88,v88,v89
v_cvt_pk_bf16_f32 v120,v120,v121
v_mfma_f32_16x16x32_bf16 a[4:7],v[152:155],v[168:171],a[4:7]
v_cvt_pk_bf16_f32 v89,v90,v91
v_cvt_pk_bf16_f32 v121,v122,v123
v_mfma_f32_16x16x32_bf16 a[4:7],v[156:159],v[192:195],a[4:7]
v_cvt_pk_bf16_f32 v90,v92,v93
v_cvt_pk_bf16_f32 v122,v124,v125
v_cvt_pk_bf16_f32 v91,v94,v95
)A"
R"A(v_cvt_pk_bf16_f32 v123,v126,v127
v_permlane16_swap_b32 v88,v90
v_permlane16_swap_b32 v120,v122
v_mfma_f32_16x16x32_bf16 a[36:39],v[152:155],v[172:175],a[36:39]
v_permlane16_swap_b32 v89,v91
v_mfma_f32_16x16x32_bf16 a[36:39],v[156:159],v[196:199],a[36:39]
ds_read_b64_tr_b16 v[144:145],v243 offset:34304
ds_read_b64_tr_b16 v[146:147],v243 offset:38400
v_permlane16_swap_b32 v121,v123
v_accvgpr_write_b32 a236,v88
v_accvgpr_write_b32 a252,v120
v_accvgpr_write_b32 a237,v89
v_mfma_f32_16x16x32_bf16 a[68:71],v[152:155],v[176:179],a[68:71]
ds_read_b64_tr_b16 v[148:149],v243 offset:50688
ds_read_b64_tr_b16 v[150:151],v243 offset:54784
v_mfma_f32_16x16x32_bf16 a[68:71],v[156:159],v[200:203],a[68:71]
buffer_load_dwordx4 v236,s[48:51],s61 offen offset:3072 nt lds
v_accvgpr_write_b32 a253,v121
v_accvgpr_write_b32 a238,v90
v_accvgpr_write_b32 a254,v122
v_accvgpr_write_b32 a239,v91
v_accvgpr_write_b32 a255,v123
v_accvgpr_read_b32 v0,a128
v_accvgpr_read_b32 v8,a136
v_accvgpr_read_b32 v1,a129
v_accvgpr_read_b32 v9,a137
v_mfma_f32_16x16x32_bf16 a[100:103],v[152:155],v[180:183],a[100:103]
v_accvgpr_read_b32 v2,a130
v_accvgpr_read_b32 v10,a138
v_mfma_f32_16x16x32_bf16 a[100:103],v[156:159],v[204:207],a[100:103]
s_waitcnt lgkmcnt(12)
v_accvgpr_read_b32 v3,a131
v_accvgpr_read_b32 v11,a139
v_accvgpr_read_b32 v4,a132
v_accvgpr_read_b32 v12,a140
v_accvgpr_read_b32 v5,a133
v_accvgpr_read_b32 v13,a141
v_accvgpr_read_b32 v6,a134
v_mfma_f32_16x16x32_bf16 a[8:11],v[160:163],v[168:171],a[8:11]
s_add_u32 s62,s62,16384
v_accvgpr_read_b32 v14,a142
v_accvgpr_read_b32 v7,a135
v_mfma_f32_16x16x32_bf16 a[8:11],v[164:167],v[192:195],a[8:11]
s_xor_b32 s61,s62,s75
v_accvgpr_read_b32 v15,a143
v_mul_f32 v0,s76,v0
v_mul_f32 v8,s76,v8
v_mul_f32 v1,s76,v1
v_mul_f32 v9,s76,v9
v_mul_f32 v2,s76,v2
v_mul_f32 v10,s76,v10
v_mfma_f32_16x16x32_bf16 a[40:43],v[160:163],v[172:175],a[40:43]
v_mul_f32 v3,s76,v3
v_mul_f32 v11,s76,v11
v_mfma_f32_16x16x32_bf16 a[40:43],v[164:167],v[196:199],a[40:43]
ds_read_b64_tr_b16 v[152:153],v242 offset:34560
ds_read_b64_tr_b16 v[154:155],v242 offset:38656
v_mul_f32 v4,s76,v4
v_mul_f32 v12,s76,v12
v_mul_f32 v5,s76,v5
v_mul_f32 v13,s76,v13
v_mul_f32 v6,s76,v6
v_mul_f32 v14,s76,v14
v_mfma_f32_16x16x32_bf16 a[72:75],v[160:163],v[176:179],a[72:75]
ds_read_b64_tr_b16 v[156:157],v242 offset:50944
ds_read_b64_tr_b16 v[158:159],v242 offset:55040
v_mfma_f32_16x16x32_bf16 a[72:75],v[164:167],v[200:203],a[72:75]
v_mul_f32 v7,s76,v7
v_mul_f32 v15,s76,v15
v_cvt_pk_bf16_f32 v0,v0,v1
v_cvt_pk_bf16_f32 v8,v8,v9
v_cvt_pk_bf16_f32 v1,v2,v3
v_cvt_pk_bf16_f32 v9,v10,v11
v_cvt_pk_bf16_f32 v2,v4,v5
v_cvt_pk_bf16_f32 v10,v12,v13
v_cvt_pk_bf16_f32 v3,v6,v7
v_mfma_f32_16x16x32_bf16 a[104:107],v[160:163],v[180:183],a[104:107]
ds_read_b64_tr_b16 v[160:161],v243 offset:34560
v_cvt_pk_bf16_f32 v11,v14,v15
v_mfma_f32_16x16x32_bf16 a[104:107],v[164:167],v[204:207],a[104:107]
s_waitcnt lgkmcnt(13)
v_permlane16_swap_b32 v0,v2
v_permlane16_swap_b32 v8,v10
v_permlane16_swap_b32 v1,v3
v_permlane16_swap_b32 v9,v11
v_mfma_f32_16x16x32_bf16 a[12:15],v[128:131],v[168:171],a[12:15]
s_add_u32 m0,s63,16384
v_accvgpr_read_b32 v16,a144
v_accvgpr_read_b32 v24,a152
v_mfma_f32_16x16x32_bf16 a[12:15],v[132:135],v[192:195],a[12:15]
v_accvgpr_read_b32 v17,a145
v_accvgpr_read_b32 v25,a153
v_accvgpr_read_b32 v18,a146
v_accvgpr_read_b32 v26,a154
v_accvgpr_read_b32 v19,a147
v_accvgpr_read_b32 v27,a155
v_accvgpr_read_b32 v20,a148
v_mfma_f32_16x16x32_bf16 a[44:47],v[128:131],v[172:175],a[44:47]
v_accvgpr_read_b32 v28,a156
v_accvgpr_read_b32 v21,a149
v_mfma_f32_16x16x32_bf16 a[44:47],v[132:135],v[196:199],a[44:47]
ds_read_b64_tr_b16 v[162:163],v243 offset:38656
v_accvgpr_read_b32 v29,a157
v_accvgpr_read_b32 v22,a150
v_accvgpr_read_b32 v30,a158
v_accvgpr_read_b32 v23,a151
v_accvgpr_read_b32 v31,a159
v_mul_f32 v16,s76,v16
v_mfma_f32_16x16x32_bf16 a[76:79],v[128:131],v[176:179],a[76:79]
ds_read_b64_tr_b16 v[164:165],v243 offset:50944
ds_read_b64_tr_b16 v[166:167],v243 offset:55040
v_mfma_f32_16x16x32_bf16 a[76:79],v[132:135],v[200:203],a[76:79]
buffer_load_dwordx4 v235,s[44:47],s61 offen nt lds
v_mul_f32 v24,s76,v24
v_mul_f32 v17,s76,v17
v_mul_f32 v25,s76,v25
v_mul_f32 v18,s76,v18
v_mul_f32 v26,s76,v26
v_mul_f32 v19,s76,v19
v_mul_f32 v27,s76,v27
v_mul_f32 v20,s76,v20
v_mul_f32 v28,s76,v28
v_mfma_f32_16x16x32_bf16 a[108:111],v[128:131],v[180:183],a[108:111]
v_mul_f32 v21,s76,v21
v_mul_f32 v29,s76,v29
v_mfma_f32_16x16x32_bf16 a[108:111],v[132:135],v[204:207],a[108:111]
s_waitcnt lgkmcnt(12)
v_mul_f32 v22,s76,v22
v_mul_f32 v30,s76,v30
v_mul_f32 v23,s76,v23
v_mul_f32 v31,s76,v31
v_cvt_pk_bf16_f32 v16,v16,v17
v_cvt_pk_bf16_f32 v24,v24,v25
v_cvt_pk_bf16_f32 v17,v18,v19
v_mfma_f32_16x16x32_bf16 a[16:19],v[136:139],v[168:171],a[16:19]
v_cvt_pk_bf16_f32 v25,v26,v27
v_cvt_pk_bf16_f32 v18,v20,v21
v_mfma_f32_16x16x32_bf16 a[16:19],v[140:143],v[192:195],a[16:19]
buffer_load_dwordx4 v235,s[44:47],s61 offen offset:1024 nt lds
v_cvt_pk_bf16_f32 v26,v28,v29
v_cvt_pk_bf16_f32 v19,v22,v23
v_cvt_pk_bf16_f32 v27,v30,v31
v_permlane16_swap_b32 v16,v18
v_permlane16_swap_b32 v24,v26
v_mfma_f32_16x16x32_bf16 a[48:51],v[136:139],v[172:175],a[48:51]
v_permlane16_swap_b32 v17,v19
v_mfma_f32_16x16x32_bf16 a[48:51],v[140:143],v[196:199],a[48:51]
v_permlane16_swap_b32 v25,v27
v_accvgpr_read_b32 v32,a0
v_accvgpr_read_b32 v64,a32
v_accvgpr_read_b32 v33,a1
v_accvgpr_read_b32 v65,a33
v_mfma_f32_16x16x32_bf16 a[80:83],v[136:139],v[176:179],a[80:83]
v_accvgpr_read_b32 v34,a2
v_accvgpr_read_b32 v66,a34
v_mfma_f32_16x16x32_bf16 a[80:83],v[140:143],v[200:203],a[80:83]
v_accvgpr_read_b32 v35,a3
v_accvgpr_read_b32 v67,a35
v_accvgpr_read_b32 v36,a4
v_accvgpr_read_b32 v68,a36
v_accvgpr_read_b32 v37,a5
v_accvgpr_read_b32 v69,a37
v_accvgpr_read_b32 v38,a6
v_mfma_f32_16x16x32_bf16 a[112:115],v[136:139],v[180:183],a[112:115]
buffer_load_dwordx4 v237,s[44:47],s61 offen offset:2048 nt lds
v_accvgpr_read_b32 v70,a38
v_mfma_f32_16x16x32_bf16 a[112:115],v[140:143],v[204:207],a[112:115]
s_waitcnt lgkmcnt(8)
v_accvgpr_read_b32 v39,a7
v_accvgpr_read_b32 v71,a39
v_mul_f32 v32,s76,v32
v_mul_f32 v64,s76,v64
v_mul_f32 v33,s76,v33
v_mul_f32 v65,s76,v65
v_mul_f32 v34,s76,v34
v_mul_f32 v66,s76,v66
v_mfma_f32_16x16x32_bf16 a[20:23],v[144:147],v[168:171],a[20:23]
v_mul_f32 v35,s76,v35
v_mul_f32 v67,s76,v67
v_mfma_f32_16x16x32_bf16 a[20:23],v[148:151],v[192:195],a[20:23]
s_waitcnt vmcnt(12) lgkmcnt(0)
s_barrier
v_mul_f32 v36,s76,v36
v_mul_f32 v68,s76,v68
v_mul_f32 v37,s76,v37
v_mul_f32 v69,s76,v69
v_mul_f32 v38,s76,v38
v_mul_f32 v70,s76,v70
v_mul_f32 v39,s76,v39
v_mfma_f32_16x16x32_bf16 a[52:55],v[144:147],v[172:175],a[52:55]
v_mul_f32 v71,s76,v71
v_cvt_pk_bf16_f32 v32,v32,v33
v_mfma_f32_16x16x32_bf16 a[52:55],v[148:151],v[196:199],a[52:55]
buffer_load_dwordx4 v237,s[44:47],s61 offen offset:3072 nt lds
v_cvt_pk_bf16_f32 v64,v64,v65
v_cvt_pk_bf16_f32 v33,v34,v35
v_cvt_pk_bf16_f32 v65,v66,v67
v_cvt_pk_bf16_f32 v34,v36,v37
v_cvt_pk_bf16_f32 v66,v68,v69
v_cvt_pk_bf16_f32 v35,v38,v39
v_mfma_f32_16x16x32_bf16 a[84:87],v[144:147],v[176:179],a[84:87]
s_add_u32 m0,s63,81920
v_cvt_pk_bf16_f32 v67,v70,v71
v_mfma_f32_16x16x32_bf16 a[84:87],v[148:151],v[200:203],a[84:87]
v_permlane16_swap_b32 v32,v34
v_permlane16_swap_b32 v64,v66
v_permlane16_swap_b32 v33,v35
v_permlane16_swap_b32 v65,v67
v_mfma_f32_16x16x32_bf16 a[116:119],v[144:147],v[180:183],a[116:119]
v_accvgpr_read_b32 v40,a8
v_accvgpr_read_b32 v72,a40
v_mfma_f32_16x16x32_bf16 a[116:119],v[148:151],v[204:207],a[116:119]
ds_write_b128 v221,v[0:3]
v_accvgpr_read_b32 v41,a9
v_accvgpr_read_b32 v73,a41
v_accvgpr_read_b32 v42,a10
v_accvgpr_read_b32 v74,a42
v_accvgpr_read_b32 v43,a11
v_accvgpr_read_b32 v75,a43
v_accvgpr_read_b32 v44,a12
v_mfma_f32_16x16x32_bf16 a[24:27],v[152:155],v[168:171],a[24:27]
buffer_load_dwordx4 v235,s[48:51],s61 offen nt lds
v_accvgpr_read_b32 v76,a44
v_mfma_f32_16x16x32_bf16 a[24:27],v[156:159],v[192:195],a[24:27]
ds_write_b128 v221,v[8:11] offset:4096
v_accvgpr_read_b32 v45,a13
v_accvgpr_read_b32 v77,a45
v_accvgpr_read_b32 v46,a14
v_accvgpr_read_b32 v78,a46
v_accvgpr_read_b32 v47,a15
v_accvgpr_read_b32 v79,a47
v_mul_f32 v40,s76,v40
v_mul_f32 v72,s76,v72
v_mfma_f32_16x16x32_bf16 a[56:59],v[152:155],v[172:175],a[56:59]
v_mul_f32 v41,s76,v41
v_mul_f32 v73,s76,v73
v_mfma_f32_16x16x32_bf16 a[56:59],v[156:159],v[196:199],a[56:59]
ds_write_b128 v221,v[16:19] offset:8192
v_mul_f32 v42,s76,v42
v_mul_f32 v74,s76,v74
v_mul_f32 v43,s76,v43
v_mul_f32 v75,s76,v75
v_mul_f32 v44,s76,v44
v_mul_f32 v76,s76,v76
v_mfma_f32_16x16x32_bf16 a[88:91],v[152:155],v[176:179],a[88:91]
v_mul_f32 v45,s76,v45
v_mul_f32 v77,s76,v77
v_mfma_f32_16x16x32_bf16 a[88:91],v[156:159],v[200:203],a[88:91]
buffer_load_dwordx4 v236,s[48:51],s61 offen offset:1024 nt lds
ds_write_b128 v221,v[24:27] offset:12288
v_mul_f32 v46,s76,v46
v_mul_f32 v78,s76,v78
v_mul_f32 v47,s76,v47
v_mul_f32 v79,s76,v79
v_cvt_pk_bf16_f32 v40,v40,v41
v_cvt_pk_bf16_f32 v72,v72,v73
v_cvt_pk_bf16_f32 v41,v42,v43
v_mfma_f32_16x16x32_bf16 a[120:123],v[152:155],v[180:183],a[120:123]
v_cvt_pk_bf16_f32 v73,v74,v75
v_cvt_pk_bf16_f32 v42,v44,v45
v_mfma_f32_16x16x32_bf16 a[120:123],v[156:159],v[204:207],a[120:123]
ds_write_b128 v222,v[32:35]
v_cvt_pk_bf16_f32 v74,v76,v77
v_cvt_pk_bf16_f32 v43,v46,v47
v_cvt_pk_bf16_f32 v75,v78,v79
v_permlane16_swap_b32 v40,v42
v_permlane16_swap_b32 v72,v74
v_mfma_f32_16x16x32_bf16 a[28:31],v[160:163],v[168:171],a[28:31]
v_permlane16_swap_b32 v41,v43
v_mfma_f32_16x16x32_bf16 a[28:31],v[164:167],v[192:195],a[28:31]
ds_write_b128 v222,v[64:67] offset:4096
v_permlane16_swap_b32 v73,v75
v_accvgpr_read_b32 v48,a16
v_accvgpr_read_b32 v80,a48
v_accvgpr_read_b32 v49,a17
v_accvgpr_read_b32 v81,a49
v_accvgpr_read_b32 v50,a18
v_mfma_f32_16x16x32_bf16 a[60:63],v[160:163],v[172:175],a[60:63]
buffer_load_dwordx4 v235,s[48:51],s61 offen offset:2048 nt lds
v_accvgpr_read_b32 v82,a50
v_mfma_f32_16x16x32_bf16 a[60:63],v[164:167],v[196:199],a[60:63]
ds_write_b128 v222,v[40:43] offset:1024
v_accvgpr_read_b32 v51,a19
v_accvgpr_read_b32 v83,a51
v_accvgpr_read_b32 v52,a20
v_accvgpr_read_b32 v84,a52
v_accvgpr_read_b32 v53,a21
v_accvgpr_read_b32 v85,a53
v_accvgpr_read_b32 v54,a22
v_mfma_f32_16x16x32_bf16 a[92:95],v[160:163],v[176:179],a[92:95]
v_accvgpr_read_b32 v86,a54
v_accvgpr_read_b32 v55,a23
v_mfma_f32_16x16x32_bf16 a[92:95],v[164:167],v[200:203],a[92:95]
ds_write_b128 v222,v[72:75] offset:5120
v_accvgpr_read_b32 v87,a55
v_mul_f32 v48,s76,v48
v_mul_f32 v80,s76,v80
v_mul_f32 v49,s76,v49
v_mul_f32 v81,s76,v81
v_mul_f32 v50,s76,v50
v_mul_f32 v82,s76,v82
v_mfma_f32_16x16x32_bf16 a[124:127],v[160:163],v[180:183],a[124:127]
v_mul_f32 v51,s76,v51
v_mul_f32 v83,s76,v83
v_mfma_f32_16x16x32_bf16 a[124:127],v[164:167],v[204:207],a[124:127]
buffer_load_dwordx4 v236,s[48:51],s61 offen offset:3072 nt lds
v_mul_f32 v52,s76,v52
v_mul_f32 v84,s76,v84
v_mul_f32 v53,s76,v53
v_mul_f32 v85,s76,v85
v_mul_f32 v54,s76,v54
v_mul_f32 v86,s76,v86
v_mul_f32 v55,s76,v55
v_mul_f32 v87,s76,v87
v_cvt_pk_bf16_f32 v48,v48,v49
v_cvt_pk_bf16_f32 v80,v80,v81
v_cvt_pk_bf16_f32 v49,v50,v51
v_cvt_pk_bf16_f32 v81,v82,v83
v_cvt_pk_bf16_f32 v50,v52,v53
v_cvt_pk_bf16_f32 v82,v84,v85
v_cvt_pk_bf16_f32 v51,v54,v55
v_cvt_pk_bf16_f32 v83,v86,v87
v_permlane16_swap_b32 v48,v50
v_permlane16_swap_b32 v80,v82
s_add_u32 s62,s62,16384
s_xor_b32 s61,s62,s75
v_permlane16_swap_b32 v49,v51
v_permlane16_swap_b32 v81,v83
ds_write_b128 v222,v[48:51] offset:2048
ds_write_b128 v222,v[80:83] offset:6144
s_nop 7
s_waitcnt vmcnt(8) lgkmcnt(0)
s_barrier
.Litem0:
ds_read_b128 v[184:187],v232
ds_read_b128 a[160:163],v252
ds_read_b128 v[188:191],v232 offset:256
ds_read_b128 a[164:167],v252 offset:1024
ds_read_b128 v[192:195],v232 offset:512
ds_read_b128 a[168:171],v252 offset:2048
ds_read_b128 v[196:199],v232 offset:768
ds_read_b128 a[172:175],v252 offset:3072
ds_read_b128 v[200:203],v232 offset:4096
ds_read_b128 v[204:207],v232 offset:4352
ds_read_b128 v[208:211],v232 offset:4608
ds_read_b128 v[212:215],v232 offset:4864
v_add_u32 v255,s74,v239
s_waitcnt lgkmcnt(8)
v_mfma_f32_16x16x32_bf16 v[0:3],v[184:187],a[192:195],0
)A"
R"A(v_accvgpr_read_b32 v112,a24
v_accvgpr_read_b32 v120,a56
v_mfma_f32_16x16x32_bf16 v[0:3],v[188:191],a[196:199],v[0:3]
s_add_u32 m0,s63,32768
v_accvgpr_read_b32 v113,a25
v_accvgpr_read_b32 v121,a57
v_mfma_f32_16x16x32_bf16 v[4:7],v[184:187],a[208:211],0
v_accvgpr_read_b32 v114,a26
v_accvgpr_read_b32 v122,a58
v_mfma_f32_16x16x32_bf16 v[4:7],v[188:191],a[212:215],v[4:7]
buffer_load_dwordx4 v235,s[44:47],s61 offen nt lds
v_accvgpr_read_b32 v115,a27
v_mfma_f32_16x16x32_bf16 v[8:11],v[184:187],a[224:227],0
v_accvgpr_read_b32 v123,a59
v_accvgpr_read_b32 v116,a28
v_mfma_f32_16x16x32_bf16 v[8:11],v[188:191],a[228:231],v[8:11]
v_accvgpr_read_b32 v124,a60
v_accvgpr_read_b32 v117,a29
v_mfma_f32_16x16x32_bf16 v[12:15],v[184:187],a[240:243],0
v_accvgpr_read_b32 v125,a61
v_accvgpr_read_b32 v118,a30
v_mfma_f32_16x16x32_bf16 v[12:15],v[188:191],a[244:247],v[12:15]
v_accvgpr_read_b32 v126,a62
v_accvgpr_read_b32 v119,a31
v_accvgpr_read_b32 v127,a63
v_mul_f32 v112,s76,v112
v_mul_f32 v120,s76,v120
v_mul_f32 v113,s76,v113
v_mfma_f32_16x16x32_bf16 v[64:67],v[184:187],a[160:163],0
v_mul_f32 v121,s76,v121
v_mul_f32 v114,s76,v114
v_mfma_f32_16x16x32_bf16 v[64:67],v[188:191],a[164:167],v[64:67]
s_waitcnt lgkmcnt(4)
v_mul_f32 v122,s76,v122
v_mul_f32 v115,s76,v115
v_mul_f32 v123,s76,v123
v_mul_f32 v116,s76,v116
v_mul_f32 v124,s76,v124
v_mul_f32 v117,s76,v117
v_mul_f32 v125,s76,v125
v_mul_f32 v118,s76,v118
v_mul_f32 v126,s76,v126
v_mul_f32 v119,s76,v119
v_mfma_f32_16x16x32_bf16 v[0:3],v[192:195],a[200:203],v[0:3]
v_mul_f32 v127,s76,v127
v_cvt_pk_bf16_f32 v112,v112,v113
v_mfma_f32_16x16x32_bf16 v[0:3],v[196:199],a[204:207],v[0:3]
v_cvt_pk_bf16_f32 v120,v120,v121
v_cvt_pk_bf16_f32 v113,v114,v115
v_cvt_pk_bf16_f32 v121,v122,v123
v_cvt_pk_bf16_f32 v114,v116,v117
v_cvt_pk_bf16_f32 v122,v124,v125
v_cvt_pk_bf16_f32 v115,v118,v119
v_cvt_pk_bf16_f32 v123,v126,v127
v_permlane16_swap_b32 v112,v114
v_permlane16_swap_b32 v120,v122
v_mfma_f32_16x16x32_bf16 v[4:7],v[192:195],a[216:219],v[4:7]
v_permlane16_swap_b32 v113,v115
v_mfma_f32_16x16x32_bf16 v[4:7],v[196:199],a[220:223],v[4:7]
ds_read_b128 v[184:187],v226
v_permlane16_swap_b32 v121,v123
v_mfma_f32_16x16x32_bf16 v[8:11],v[192:195],a[232:235],v[8:11]
ds_read_b128 v[188:191],v226 offset:256
buffer_load_dwordx4 v235,s[44:47],s61 offen offset:1024 nt lds
v_mfma_f32_16x16x32_bf16 v[8:11],v[196:199],a[236:239],v[8:11]
buffer_load_dwordx4 v237,s[44:47],s61 offen offset:2048 nt lds
ds_write_b128 v255,v[112:115] offset:3072
ds_write_b128 v255,v[120:123] offset:7168
v_accvgpr_read_b32 v128,a64
v_accvgpr_read_b32 v136,a72
v_accvgpr_read_b32 v129,a65
v_accvgpr_read_b32 v137,a73
v_accvgpr_read_b32 v130,a66
v_accvgpr_read_b32 v138,a74
v_accvgpr_read_b32 v131,a67
v_accvgpr_read_b32 v139,a75
v_accvgpr_read_b32 v132,a68
v_accvgpr_read_b32 v140,a76
v_accvgpr_read_b32 v133,a69
v_accvgpr_read_b32 v141,a77
v_accvgpr_read_b32 v134,a70
v_mfma_f32_16x16x32_bf16 v[12:15],v[192:195],a[248:251],v[12:15]
v_accvgpr_read_b32 v142,a78
v_accvgpr_read_b32 v135,a71
v_mfma_f32_16x16x32_bf16 v[12:15],v[196:199],a[252:255],v[12:15]
v_accvgpr_read_b32 v143,a79
v_mul_f32 v128,s76,v128
v_mul_f32 v136,s76,v136
v_mul_f32 v129,s76,v129
v_mul_f32 v137,s76,v137
v_mul_f32 v130,s76,v130
v_mul_f32 v138,s76,v138
v_mul_f32 v131,s76,v131
v_mul_f32 v139,s76,v139
v_mul_f32 v132,s76,v132
v_mfma_f32_16x16x32_bf16 v[64:67],v[192:195],a[168:171],v[64:67]
v_mul_f32 v140,s76,v140
v_mul_f32 v133,s76,v133
v_mfma_f32_16x16x32_bf16 v[64:67],v[196:199],a[172:175],v[64:67]
s_waitcnt lgkmcnt(6)
v_mul_f32 v141,s76,v141
v_mul_f32 v134,s76,v134
v_mul_f32 v142,s76,v142
v_mul_f32 v135,s76,v135
v_mul_f32 v143,s76,v143
v_cvt_pk_bf16_f32 v152,v128,v129
v_cvt_pk_bf16_f32 v156,v136,v137
v_cvt_pk_bf16_f32 v153,v130,v131
v_cvt_pk_bf16_f32 v157,v138,v139
v_cvt_pk_bf16_f32 v154,v132,v133
v_mfma_f32_16x16x32_bf16 v[16:19],v[200:203],a[192:195],0
v_cvt_pk_bf16_f32 v158,v140,v141
v_cvt_pk_bf16_f32 v155,v134,v135
v_mfma_f32_16x16x32_bf16 v[16:19],v[204:207],a[196:199],v[16:19]
ds_read_b128 v[192:195],v226 offset:512
v_cvt_pk_bf16_f32 v159,v142,v143
v_permlane16_swap_b32 v152,v154
v_permlane16_swap_b32 v156,v158
v_permlane16_swap_b32 v153,v155
v_permlane16_swap_b32 v157,v159
v_accvgpr_read_b32 v128,a80
v_accvgpr_read_b32 v136,a88
v_mfma_f32_16x16x32_bf16 v[20:23],v[200:203],a[208:211],0
v_accvgpr_read_b32 v129,a81
v_accvgpr_read_b32 v137,a89
v_mfma_f32_16x16x32_bf16 v[20:23],v[204:207],a[212:215],v[20:23]
v_accvgpr_read_b32 v130,a82
v_accvgpr_read_b32 v138,a90
v_accvgpr_read_b32 v131,a83
v_accvgpr_read_b32 v139,a91
v_accvgpr_read_b32 v132,a84
v_accvgpr_read_b32 v140,a92
v_accvgpr_read_b32 v133,a85
v_accvgpr_read_b32 v141,a93
v_accvgpr_read_b32 v134,a86
v_accvgpr_read_b32 v142,a94
v_mfma_f32_16x16x32_bf16 v[24:27],v[200:203],a[224:227],0
ds_read_b128 v[196:199],v226 offset:768
v_accvgpr_read_b32 v135,a87
v_mfma_f32_16x16x32_bf16 v[24:27],v[204:207],a[228:231],v[24:27]
v_accvgpr_read_b32 v143,a95
v_mul_f32 v128,s76,v128
v_mul_f32 v136,s76,v136
v_mul_f32 v129,s76,v129
v_mul_f32 v137,s76,v137
v_mul_f32 v130,s76,v130
v_mul_f32 v138,s76,v138
v_mul_f32 v131,s76,v131
v_mul_f32 v139,s76,v139
v_mul_f32 v132,s76,v132
v_mul_f32 v140,s76,v140
v_mul_f32 v133,s76,v133
v_mfma_f32_16x16x32_bf16 v[28:31],v[200:203],a[240:243],0
v_mul_f32 v141,s76,v141
v_mul_f32 v134,s76,v134
v_mfma_f32_16x16x32_bf16 v[28:31],v[204:207],a[244:247],v[28:31]
v_mul_f32 v142,s76,v142
v_mul_f32 v135,s76,v135
v_mul_f32 v143,s76,v143
v_cvt_pk_bf16_f32 v160,v128,v129
v_cvt_pk_bf16_f32 v164,v136,v137
v_cvt_pk_bf16_f32 v161,v130,v131
v_cvt_pk_bf16_f32 v165,v138,v139
v_cvt_pk_bf16_f32 v162,v132,v133
v_cvt_pk_bf16_f32 v166,v140,v141
v_cvt_pk_bf16_f32 v163,v134,v135
v_mfma_f32_16x16x32_bf16 v[68:71],v[200:203],a[160:163],0
buffer_load_dwordx4 v237,s[44:47],s61 offen offset:3072 nt lds
v_cvt_pk_bf16_f32 v167,v142,v143
v_mfma_f32_16x16x32_bf16 v[68:71],v[204:207],a[164:167],v[68:71]
s_waitcnt lgkmcnt(6)
v_permlane16_swap_b32 v160,v162
v_permlane16_swap_b32 v164,v166
v_permlane16_swap_b32 v161,v163
v_permlane16_swap_b32 v165,v167
v_accvgpr_read_b32 v128,a96
v_accvgpr_read_b32 v136,a104
v_accvgpr_read_b32 v129,a97
v_mfma_f32_16x16x32_bf16 v[16:19],v[208:211],a[200:203],v[16:19]
s_add_u32 m0,s63,98304
v_accvgpr_read_b32 v137,a105
v_accvgpr_read_b32 v130,a98
v_mfma_f32_16x16x32_bf16 v[16:19],v[212:215],a[204:207],v[16:19]
v_accvgpr_read_b32 v138,a106
v_accvgpr_read_b32 v131,a99
v_accvgpr_read_b32 v139,a107
v_accvgpr_read_b32 v132,a100
v_accvgpr_read_b32 v140,a108
v_accvgpr_read_b32 v133,a101
v_accvgpr_read_b32 v141,a109
v_accvgpr_read_b32 v134,a102
v_accvgpr_read_b32 v142,a110
v_accvgpr_read_b32 v135,a103
v_accvgpr_read_b32 v143,a111
v_mfma_f32_16x16x32_bf16 v[20:23],v[208:211],a[216:219],v[20:23]
v_mul_f32 v128,s76,v128
v_mul_f32 v136,s76,v136
v_mfma_f32_16x16x32_bf16 v[20:23],v[212:215],a[220:223],v[20:23]
ds_read_b128 v[200:203],v226 offset:4096
v_mul_f32 v129,s76,v129
v_mul_f32 v137,s76,v137
v_mul_f32 v130,s76,v130
v_mul_f32 v138,s76,v138
v_mul_f32 v131,s76,v131
v_mul_f32 v139,s76,v139
v_mul_f32 v132,s76,v132
v_mul_f32 v140,s76,v140
v_mul_f32 v133,s76,v133
v_mul_f32 v141,s76,v141
v_mfma_f32_16x16x32_bf16 v[24:27],v[208:211],a[232:235],v[24:27]
ds_read_b128 v[204:207],v226 offset:4352
v_mul_f32 v134,s76,v134
v_mfma_f32_16x16x32_bf16 v[24:27],v[212:215],a[236:239],v[24:27]
v_mul_f32 v142,s76,v142
v_mul_f32 v135,s76,v135
v_mul_f32 v143,s76,v143
v_cvt_pk_bf16_f32 v168,v128,v129
v_cvt_pk_bf16_f32 v172,v136,v137
v_cvt_pk_bf16_f32 v169,v130,v131
v_cvt_pk_bf16_f32 v173,v138,v139
v_cvt_pk_bf16_f32 v170,v132,v133
v_cvt_pk_bf16_f32 v174,v140,v141
v_cvt_pk_bf16_f32 v171,v134,v135
v_cvt_pk_bf16_f32 v175,v142,v143
v_permlane16_swap_b32 v168,v170
v_mfma_f32_16x16x32_bf16 v[28:31],v[208:211],a[248:251],v[28:31]
v_permlane16_swap_b32 v172,v174
v_mfma_f32_16x16x32_bf16 v[28:31],v[212:215],a[252:255],v[28:31]
v_permlane16_swap_b32 v169,v171
v_permlane16_swap_b32 v173,v175
v_accvgpr_read_b32 v128,a112
v_accvgpr_read_b32 v136,a120
v_accvgpr_read_b32 v129,a113
v_accvgpr_read_b32 v137,a121
v_accvgpr_read_b32 v130,a114
v_mfma_f32_16x16x32_bf16 v[68:71],v[208:211],a[168:171],v[68:71]
buffer_load_dwordx4 v235,s[48:51],s61 offen nt lds
v_accvgpr_read_b32 v138,a122
v_mfma_f32_16x16x32_bf16 v[68:71],v[212:215],a[172:175],v[68:71]
s_waitcnt lgkmcnt(6)
v_accvgpr_read_b32 v131,a115
v_accvgpr_read_b32 v139,a123
v_accvgpr_read_b32 v132,a116
v_accvgpr_read_b32 v140,a124
v_accvgpr_read_b32 v133,a117
v_accvgpr_read_b32 v141,a125
v_accvgpr_read_b32 v134,a118
v_accvgpr_read_b32 v142,a126
v_accvgpr_read_b32 v135,a119
v_accvgpr_read_b32 v143,a127
v_mul_f32 v128,s76,v128
v_mul_f32 v136,s76,v136
v_mul_f32 v129,s76,v129
v_mul_f32 v137,s76,v137
v_mul_f32 v130,s76,v130
v_mul_f32 v138,s76,v138
v_mul_f32 v131,s76,v131
v_mul_f32 v139,s76,v139
v_mul_f32 v132,s76,v132
v_mul_f32 v140,s76,v140
v_mul_f32 v133,s76,v133
v_mul_f32 v141,s76,v141
v_mul_f32 v134,s76,v134
v_mul_f32 v142,s76,v142
v_mul_f32 v135,s76,v135
v_mul_f32 v143,s76,v143
v_cvt_pk_bf16_f32 v176,v128,v129
v_cvt_pk_bf16_f32 v180,v136,v137
v_cvt_pk_bf16_f32 v177,v130,v131
v_cvt_pk_bf16_f32 v181,v138,v139
v_cvt_pk_bf16_f32 v178,v132,v133
v_cvt_pk_bf16_f32 v182,v140,v141
v_cvt_pk_bf16_f32 v179,v134,v135
v_cvt_pk_bf16_f32 v183,v142,v143
v_permlane16_swap_b32 v176,v178
v_max3_f32 v72,v0,v1,v2
v_max3_f32 v73,v4,v5,v6
v_max3_f32 v74,v8,v9,v10
v_max3_f32 v75,v12,v13,v14
v_max3_f32 v76,v64,v65,v66
v_max3_f32 v72,v72,v3,v16
v_max3_f32 v73,v73,v7,v20
v_max3_f32 v74,v74,v11,v24
v_max3_f32 v75,v75,v15,v28
v_max3_f32 v76,v76,v67,v68
v_max3_f32 v72,v72,v17,v18
v_max3_f32 v73,v73,v21,v22
v_max3_f32 v74,v74,v25,v26
v_max3_f32 v75,v75,v29,v30
v_max3_f32 v76,v76,v69,v70
v_max_f32 v72,v72,v19
v_max_f32 v73,v73,v23
v_max_f32 v74,v74,v27
v_max_f32 v75,v75,v31
v_max_f32 v76,v76,v71
v_mov_b32 v216,v72
v_mov_b32 v217,v73
v_mov_b32 v218,v74
v_mov_b32 v219,v75
v_mov_b32 v220,v76
v_permlane32_swap_b32 v216,v72
v_permlane32_swap_b32 v217,v73
v_permlane32_swap_b32 v218,v74
v_permlane32_swap_b32 v219,v75
v_permlane32_swap_b32 v220,v76
v_max_f32 v72,v72,v216
v_max_f32 v73,v73,v217
v_max_f32 v74,v74,v218
v_max_f32 v75,v75,v219
v_max_f32 v76,v76,v220
v_mov_b32 v216,v72
v_mov_b32 v217,v73
v_mov_b32 v218,v74
v_mov_b32 v219,v75
v_mov_b32 v220,v76
v_permlane16_swap_b32 v216,v72
v_permlane16_swap_b32 v217,v73
v_permlane16_swap_b32 v218,v74
v_permlane16_swap_b32 v219,v75
v_permlane16_swap_b32 v220,v76
v_max_f32 v72,v72,v216
v_max_f32 v73,v73,v217
v_max_f32 v74,v74,v218
v_max_f32 v75,v75,v219
v_max_f32 v76,v76,v220
v_sub_f32 v80,0,v72
v_sub_f32 v84,0,v73
v_sub_f32 v88,0,v74
v_sub_f32 v92,0,v75
v_sub_f32 v96,0,v76
v_mov_b32 v81,v80
v_mov_b32 v85,v84
v_mov_b32 v89,v88
v_mov_b32 v93,v92
v_mov_b32 v97,v96
v_mov_b32 v82,v80
v_mov_b32 v86,v84
v_mov_b32 v90,v88
v_mov_b32 v94,v92
v_mov_b32 v98,v96
v_mov_b32 v83,v80
v_mov_b32 v87,v84
v_mov_b32 v91,v88
v_mov_b32 v95,v92
v_mov_b32 v99,v96
v_permlane16_swap_b32 v180,v182
v_permlane16_swap_b32 v177,v179
v_permlane16_swap_b32 v181,v183
v_mfma_f32_16x16x32_bf16 v[32:35],v[184:187],a[192:195],v[80:83]
ds_read_b128 v[208:211],v226 offset:4608
ds_read_b128 v[212:215],v226 offset:4864
v_mfma_f32_16x16x32_bf16 v[32:35],v[188:191],a[196:199],v[32:35]
buffer_load_dwordx4 v236,s[48:51],s61 offen offset:1024 nt lds
v_add_f32 v0,v80,v0
v_exp_f32 v216,v0
v_mfma_f32_16x16x32_bf16 v[36:39],v[184:187],a[208:211],v[84:87]
v_mfma_f32_16x16x32_bf16 v[36:39],v[188:191],a[212:215],v[36:39]
s_cmp_eq_u32 s56,32768
s_cselect_b32 s57,0xffff8000,s58
v_add_f32 v4,v84,v4
v_exp_f32 v217,v4
v_mfma_f32_16x16x32_bf16 v[40:43],v[184:187],a[224:227],v[88:91]
buffer_load_dwordx4 v235,s[48:51],s61 offen offset:2048 nt lds
v_mfma_f32_16x16x32_bf16 v[40:43],v[188:191],a[228:231],v[40:43]
v_add_u32 v232,s57,v232
v_add_f32 v1,v80,v1
v_exp_f32 v218,v1
v_mfma_f32_16x16x32_bf16 v[44:47],v[184:187],a[240:243],v[92:95]
v_add_f32 v72,v216,v218
v_cvt_pk_bf16_f32 v112,v216,v218
v_mfma_f32_16x16x32_bf16 v[44:47],v[188:191],a[244:247],v[44:47]
s_waitcnt lgkmcnt(4)
ds_read_b64_tr_b16 v[184:185],v233
ds_read_b64_tr_b16 v[186:187],v233 offset:4096
)A"
R"A(v_add_f32 v5,v84,v5
v_exp_f32 v219,v5
v_add_f32 v2,v80,v2
v_exp_f32 v220,v2
v_add_f32 v6,v84,v6
v_exp_f32 v221,v6
v_mfma_f32_16x16x32_bf16 v[32:35],v[192:195],a[200:203],v[32:35]
v_add_f32 v73,v217,v219
v_cvt_pk_bf16_f32 v116,v217,v219
v_mfma_f32_16x16x32_bf16 v[32:35],v[196:199],a[204:207],v[32:35]
v_add_f32 v72,v220,v72
v_add_f32 v73,v221,v73
v_add_f32 v3,v80,v3
v_exp_f32 v222,v3
v_add_f32 v7,v84,v7
v_exp_f32 v223,v7
v_add_f32 v8,v88,v8
v_exp_f32 v216,v8
v_mfma_f32_16x16x32_bf16 v[36:39],v[192:195],a[216:219],v[36:39]
v_add_f32 v72,v222,v72
v_cvt_pk_bf16_f32 v113,v220,v222
v_mfma_f32_16x16x32_bf16 v[36:39],v[196:199],a[220:223],v[36:39]
v_add_f32 v73,v223,v73
v_cvt_pk_bf16_f32 v117,v221,v223
v_add_f32 v12,v92,v12
v_exp_f32 v217,v12
v_add_f32 v9,v88,v9
v_exp_f32 v218,v9
v_add_f32 v13,v92,v13
v_exp_f32 v219,v13
v_mfma_f32_16x16x32_bf16 v[40:43],v[192:195],a[232:235],v[40:43]
ds_read_b64_tr_b16 v[188:189],v234
ds_read_b64_tr_b16 v[190:191],v234 offset:4096
v_mfma_f32_16x16x32_bf16 v[40:43],v[196:199],a[236:239],v[40:43]
v_add_f32 v74,v216,v218
v_cvt_pk_bf16_f32 v120,v216,v218
v_add_f32 v10,v88,v10
v_exp_f32 v220,v10
v_add_f32 v75,v217,v219
v_cvt_pk_bf16_f32 v124,v217,v219
v_add_f32 v14,v92,v14
v_exp_f32 v221,v14
v_mfma_f32_16x16x32_bf16 v[44:47],v[192:195],a[248:251],v[44:47]
v_add_f32 v74,v220,v74
v_add_f32 v75,v221,v75
v_mfma_f32_16x16x32_bf16 v[44:47],v[196:199],a[252:255],v[44:47]
s_waitcnt lgkmcnt(6)
ds_read_b64_tr_b16 v[192:193],v233 offset:256
v_add_u32 v226,s57,v226
v_add_f32 v11,v88,v11
v_exp_f32 v222,v11
v_add_f32 v15,v92,v15
v_exp_f32 v223,v15
v_add_f32 v64,v96,v64
v_exp_f32 v216,v64
v_mfma_f32_16x16x32_bf16 v[48:51],v[200:203],a[192:195],v[80:83]
v_add_f32 v74,v222,v74
v_cvt_pk_bf16_f32 v121,v220,v222
v_mfma_f32_16x16x32_bf16 v[48:51],v[204:207],a[196:199],v[48:51]
v_add_f32 v75,v223,v75
v_cvt_pk_bf16_f32 v125,v221,v223
v_add_f32 v65,v96,v65
v_exp_f32 v217,v65
v_add_f32 v66,v96,v66
v_exp_f32 v218,v66
v_add_f32 v67,v96,v67
v_exp_f32 v219,v67
v_mfma_f32_16x16x32_bf16 v[52:55],v[200:203],a[208:211],v[84:87]
v_add_f32 v76,v216,v217
v_cvt_pk_bf16_f32 v144,v216,v217
v_mfma_f32_16x16x32_bf16 v[52:55],v[204:207],a[212:215],v[52:55]
ds_read_b64_tr_b16 v[194:195],v233 offset:4352
v_add_f32 v76,v218,v76
v_add_f32 v76,v219,v76
v_add_f32 v16,v80,v16
v_exp_f32 v220,v16
v_cvt_pk_bf16_f32 v145,v218,v219
v_add_f32 v20,v84,v20
v_exp_f32 v221,v20
v_add_f32 v17,v80,v17
v_exp_f32 v222,v17
v_mfma_f32_16x16x32_bf16 v[56:59],v[200:203],a[224:227],v[88:91]
ds_read_b64_tr_b16 v[196:197],v234 offset:256
v_add_f32 v72,v220,v72
v_mfma_f32_16x16x32_bf16 v[56:59],v[204:207],a[228:231],v[56:59]
ds_read_b64_tr_b16 v[198:199],v234 offset:4352
v_add_f32 v73,v221,v73
v_add_f32 v21,v84,v21
v_exp_f32 v223,v21
v_add_f32 v72,v222,v72
v_cvt_pk_bf16_f32 v114,v220,v222
v_add_f32 v18,v80,v18
v_exp_f32 v216,v18
v_mfma_f32_16x16x32_bf16 v[60:63],v[200:203],a[240:243],v[92:95]
v_add_f32 v73,v223,v73
v_cvt_pk_bf16_f32 v118,v221,v223
v_mfma_f32_16x16x32_bf16 v[60:63],v[204:207],a[244:247],v[60:63]
s_waitcnt lgkmcnt(8)
buffer_load_dwordx4 v236,s[48:51],s61 offen offset:3072 nt lds
v_add_f32 v72,v216,v72
v_add_f32 v22,v84,v22
v_exp_f32 v217,v22
v_add_f32 v19,v80,v19
v_exp_f32 v218,v19
v_add_f32 v23,v84,v23
v_exp_f32 v219,v23
v_mfma_f32_16x16x32_bf16 v[48:51],v[208:211],a[200:203],v[48:51]
s_add_u32 s62,s62,16384
s_xor_b32 s61,s62,s75
v_add_f32 v73,v217,v73
v_mfma_f32_16x16x32_bf16 v[48:51],v[212:215],a[204:207],v[48:51]
v_add_f32 v72,v218,v72
v_cvt_pk_bf16_f32 v115,v216,v218
v_add_f32 v24,v88,v24
v_exp_f32 v220,v24
v_add_f32 v73,v219,v73
v_cvt_pk_bf16_f32 v119,v217,v219
v_add_f32 v28,v92,v28
v_exp_f32 v221,v28
v_add_f32 v25,v88,v25
v_exp_f32 v222,v25
v_mfma_f32_16x16x32_bf16 v[52:55],v[208:211],a[216:219],v[52:55]
v_add_f32 v74,v220,v74
v_add_f32 v75,v221,v75
v_mfma_f32_16x16x32_bf16 v[52:55],v[212:215],a[220:223],v[52:55]
ds_read_b64_tr_b16 v[200:201],v233 offset:512
ds_read_b64_tr_b16 v[202:203],v233 offset:4608
v_add_f32 v74,v222,v74
v_cvt_pk_bf16_f32 v122,v220,v222
v_add_f32 v29,v92,v29
v_exp_f32 v223,v29
v_add_f32 v26,v88,v26
v_exp_f32 v216,v26
v_add_f32 v30,v92,v30
v_exp_f32 v217,v30
v_mfma_f32_16x16x32_bf16 v[56:59],v[208:211],a[232:235],v[56:59]
v_add_f32 v75,v223,v75
v_cvt_pk_bf16_f32 v126,v221,v223
v_mfma_f32_16x16x32_bf16 v[56:59],v[212:215],a[236:239],v[56:59]
ds_read_b64_tr_b16 v[204:205],v234 offset:512
ds_read_b64_tr_b16 v[206:207],v234 offset:4608
v_add_f32 v27,v88,v27
v_exp_f32 v218,v27
v_add_f32 v31,v92,v31
v_exp_f32 v219,v31
v_add_f32 v68,v96,v68
v_exp_f32 v220,v68
v_add_f32 v69,v96,v69
v_exp_f32 v221,v69
v_add_f32 v70,v96,v70
v_exp_f32 v222,v70
v_add_f32 v71,v96,v71
v_exp_f32 v223,v71
v_add_f32 v74,v216,v74
v_add_f32 v75,v217,v75
v_mfma_f32_16x16x32_bf16 v[60:63],v[208:211],a[248:251],v[60:63]
v_add_f32 v74,v218,v74
v_cvt_pk_bf16_f32 v123,v216,v218
v_add_f32 v75,v219,v75
v_cvt_pk_bf16_f32 v127,v217,v219
v_add_f32 v76,v220,v76
v_add_f32 v76,v221,v76
v_cvt_pk_bf16_f32 v146,v220,v221
v_add_f32 v76,v222,v76
v_add_f32 v76,v223,v76
v_cvt_pk_bf16_f32 v147,v222,v223
v_mfma_f32_16x16x32_bf16 v[60:63],v[212:215],a[252:255],v[60:63]
ds_read_b64_tr_b16 v[208:209],v233 offset:768
ds_read_b64_tr_b16 v[210:211],v233 offset:4864
ds_read_b64_tr_b16 v[212:213],v234 offset:768
ds_read_b64_tr_b16 v[214:215],v234 offset:4864
s_waitcnt vmcnt(8) lgkmcnt(0)
.Lt0:
v_mfma_f32_16x16x32_bf16 a[0:3],v[184:187],v[112:115],0
s_barrier
v_exp_f32 v216,v32
v_mfma_f32_16x16x32_bf16 a[32:35],v[184:187],v[116:119],0
s_add_u32 m0,s63,114688
v_mov_b32 v104,v72
v_mov_b32 v105,v73
v_mfma_f32_16x16x32_bf16 a[64:67],v[184:187],v[120:123],0
buffer_load_dwordx4 v235,s[48:51],s61 offen nt lds
v_exp_f32 v217,v36
v_mfma_f32_16x16x32_bf16 a[96:99],v[184:187],v[124:127],0
v_exp_f32 v218,v33
v_mfma_f32_16x16x32_bf16 a[128:131],v[184:187],v[144:147],0
ds_read_b128 v[184:187],v232
v_add_f32 v72,v216,v218
v_mfma_f32_16x16x32_bf16 a[4:7],v[188:191],v[112:115],0
buffer_load_dwordx4 v236,s[48:51],s61 offen offset:1024 nt lds
v_cvt_pk_bf16_f32 v128,v216,v218
v_mfma_f32_16x16x32_bf16 a[36:39],v[188:191],v[116:119],0
v_exp_f32 v219,v37
v_mfma_f32_16x16x32_bf16 a[68:71],v[188:191],v[120:123],0
v_add_f32 v73,v217,v219
v_cvt_pk_bf16_f32 v132,v217,v219
v_mfma_f32_16x16x32_bf16 a[100:103],v[188:191],v[124:127],0
v_exp_f32 v220,v34
v_mfma_f32_16x16x32_bf16 a[132:135],v[188:191],v[144:147],0
ds_read_b128 v[188:191],v232 offset:256
v_add_f32 v72,v220,v72
v_mfma_f32_16x16x32_bf16 a[8:11],v[192:195],v[112:115],0
v_exp_f32 v221,v38
v_mfma_f32_16x16x32_bf16 a[40:43],v[192:195],v[116:119],0
v_add_f32 v73,v221,v73
v_mfma_f32_16x16x32_bf16 a[72:75],v[192:195],v[120:123],0
v_exp_f32 v222,v35
v_mfma_f32_16x16x32_bf16 a[104:107],v[192:195],v[124:127],0
v_add_f32 v72,v222,v72
v_cvt_pk_bf16_f32 v129,v220,v222
v_mfma_f32_16x16x32_bf16 a[136:139],v[192:195],v[144:147],0
ds_read_b128 v[192:195],v232 offset:512
buffer_load_dwordx4 v235,s[48:51],s61 offen offset:2048 nt lds
v_mfma_f32_16x16x32_bf16 a[12:15],v[196:199],v[112:115],0
v_exp_f32 v223,v39
v_mfma_f32_16x16x32_bf16 a[44:47],v[196:199],v[116:119],0
v_add_f32 v73,v223,v73
v_cvt_pk_bf16_f32 v133,v221,v223
v_mfma_f32_16x16x32_bf16 a[76:79],v[196:199],v[120:123],0
v_exp_f32 v216,v40
v_mfma_f32_16x16x32_bf16 a[108:111],v[196:199],v[124:127],0
v_mov_b32 v106,v74
v_mov_b32 v107,v75
v_mfma_f32_16x16x32_bf16 a[140:143],v[196:199],v[144:147],0
v_exp_f32 v217,v44
v_mfma_f32_16x16x32_bf16 a[16:19],v[200:203],v[112:115],0
ds_read_b128 v[196:199],v232 offset:768
buffer_load_dwordx4 v236,s[48:51],s61 offen offset:3072 nt lds
v_mfma_f32_16x16x32_bf16 a[48:51],v[200:203],v[116:119],0
v_exp_f32 v218,v41
v_mfma_f32_16x16x32_bf16 a[80:83],v[200:203],v[120:123],0
v_add_f32 v74,v216,v218
v_cvt_pk_bf16_f32 v136,v216,v218
v_mfma_f32_16x16x32_bf16 a[112:115],v[200:203],v[124:127],0
v_exp_f32 v219,v45
v_mfma_f32_16x16x32_bf16 a[144:147],v[200:203],v[144:147],0
v_add_f32 v75,v217,v219
v_cvt_pk_bf16_f32 v140,v217,v219
v_mfma_f32_16x16x32_bf16 a[20:23],v[204:207],v[112:115],0
ds_read_b128 v[200:203],v232 offset:4096
v_exp_f32 v220,v42
v_mfma_f32_16x16x32_bf16 a[52:55],v[204:207],v[116:119],0
s_add_u32 m0,s56,s63
v_add_f32 v74,v220,v74
v_exp_f32 v221,v46
v_mfma_f32_16x16x32_bf16 a[84:87],v[204:207],v[120:123],0
v_add_f32 v75,v221,v75
v_mfma_f32_16x16x32_bf16 a[116:119],v[204:207],v[124:127],0
v_exp_f32 v222,v43
v_mfma_f32_16x16x32_bf16 a[148:151],v[204:207],v[144:147],0
ds_read_b128 v[204:207],v232 offset:4352
v_add_f32 v74,v222,v74
v_mfma_f32_16x16x32_bf16 a[24:27],v[208:211],v[112:115],0
buffer_load_dwordx4 v235,s[44:47],s61 offen nt lds
v_cvt_pk_bf16_f32 v137,v220,v222
v_mfma_f32_16x16x32_bf16 a[56:59],v[208:211],v[116:119],0
v_exp_f32 v223,v47
v_mfma_f32_16x16x32_bf16 a[88:91],v[208:211],v[120:123],0
v_add_f32 v75,v223,v75
v_cvt_pk_bf16_f32 v141,v221,v223
v_mfma_f32_16x16x32_bf16 a[120:123],v[208:211],v[124:127],0
v_exp_f32 v216,v48
v_mfma_f32_16x16x32_bf16 a[152:155],v[208:211],v[144:147],0
ds_read_b128 v[208:211],v232 offset:4608
v_add_f32 v72,v216,v72
v_mfma_f32_16x16x32_bf16 a[28:31],v[212:215],v[112:115],0
v_exp_f32 v217,v52
v_mfma_f32_16x16x32_bf16 a[60:63],v[212:215],v[116:119],0
buffer_load_dwordx4 v235,s[44:47],s61 offen offset:1024 nt lds
v_add_f32 v73,v217,v73
v_mfma_f32_16x16x32_bf16 a[92:95],v[212:215],v[120:123],0
v_exp_f32 v218,v49
v_mfma_f32_16x16x32_bf16 a[124:127],v[212:215],v[124:127],0
v_add_f32 v72,v218,v72
v_cvt_pk_bf16_f32 v130,v216,v218
v_mfma_f32_16x16x32_bf16 a[156:159],v[212:215],v[144:147],0
s_waitcnt lgkmcnt(5)
ds_read_b128 v[212:215],v232 offset:4864
v_exp_f32 v219,v53
v_mfma_f32_16x16x32_bf16 v[0:3],v[184:187],a[192:195],v[80:83]
v_add_f32 v73,v219,v73
v_cvt_pk_bf16_f32 v134,v217,v219
v_mfma_f32_16x16x32_bf16 v[0:3],v[188:191],a[196:199],v[0:3]
v_exp_f32 v220,v50
v_mfma_f32_16x16x32_bf16 v[4:7],v[184:187],a[208:211],v[84:87]
v_add_f32 v72,v220,v72
v_mfma_f32_16x16x32_bf16 v[4:7],v[188:191],a[212:215],v[4:7]
v_exp_f32 v221,v54
v_mfma_f32_16x16x32_bf16 v[8:11],v[184:187],a[224:227],v[88:91]
buffer_load_dwordx4 v237,s[44:47],s61 offen offset:2048 nt lds
v_add_f32 v73,v221,v73
v_mfma_f32_16x16x32_bf16 v[8:11],v[188:191],a[228:231],v[8:11]
v_exp_f32 v222,v51
v_mfma_f32_16x16x32_bf16 v[12:15],v[184:187],a[240:243],v[92:95]
v_add_f32 v72,v222,v72
v_cvt_pk_bf16_f32 v131,v220,v222
v_mfma_f32_16x16x32_bf16 v[12:15],v[188:191],a[244:247],v[12:15]
v_exp_f32 v223,v55
v_mfma_f32_16x16x32_bf16 v[64:67],v[184:187],a[160:163],v[96:99]
ds_read_b64_tr_b16 v[184:185],v230
ds_read_b64_tr_b16 v[186:187],v230 offset:4096
v_mfma_f32_16x16x32_bf16 v[64:67],v[188:191],a[164:167],v[64:67]
s_waitcnt lgkmcnt(6)
v_add_f32 v73,v223,v73
v_cvt_pk_bf16_f32 v135,v221,v223
v_mfma_f32_16x16x32_bf16 v[0:3],v[192:195],a[200:203],v[0:3]
ds_read_b64_tr_b16 v[188:189],v231
ds_read_b64_tr_b16 v[190:191],v231 offset:4096
v_mfma_f32_16x16x32_bf16 v[0:3],v[196:199],a[204:207],v[0:3]
v_exp_f32 v216,v56
v_mfma_f32_16x16x32_bf16 v[4:7],v[192:195],a[216:219],v[4:7]
v_max_f32 v228,v72,v73
v_add_f32 v74,v216,v74
v_mfma_f32_16x16x32_bf16 v[4:7],v[196:199],a[220:223],v[4:7]
v_exp_f32 v217,v60
v_mfma_f32_16x16x32_bf16 v[8:11],v[192:195],a[232:235],v[8:11]
buffer_load_dwordx4 v237,s[44:47],s61 offen offset:3072 nt lds
v_add_f32 v75,v217,v75
v_mfma_f32_16x16x32_bf16 v[8:11],v[196:199],a[236:239],v[8:11]
v_exp_f32 v218,v57
v_mfma_f32_16x16x32_bf16 v[12:15],v[192:195],a[248:251],v[12:15]
v_add_f32 v74,v218,v74
v_cvt_pk_bf16_f32 v138,v216,v218
v_mfma_f32_16x16x32_bf16 v[12:15],v[196:199],a[252:255],v[12:15]
v_exp_f32 v219,v61
v_mfma_f32_16x16x32_bf16 v[64:67],v[192:195],a[168:171],v[64:67]
s_add_u32 s62,s62,16384
v_add_f32 v75,v219,v75
v_cvt_pk_bf16_f32 v142,v217,v219
v_mfma_f32_16x16x32_bf16 v[64:67],v[196:199],a[172:175],v[64:67]
s_waitcnt lgkmcnt(6)
v_exp_f32 v220,v58
v_mfma_f32_16x16x32_bf16 v[16:19],v[200:203],a[192:195],v[80:83]
s_xor_b32 s61,s62,s75
ds_read_b64_tr_b16 v[192:193],v230 offset:256
ds_read_b64_tr_b16 v[194:195],v230 offset:4352
v_mfma_f32_16x16x32_bf16 v[16:19],v[204:207],a[196:199],v[16:19]
s_add_u32 s56,s56,s57
ds_read_b64_tr_b16 v[196:197],v231 offset:256
)A"
R"A(v_add_f32 v74,v220,v74
v_mfma_f32_16x16x32_bf16 v[20:23],v[200:203],a[208:211],v[84:87]
s_mov_b32 s72,s29
ds_read_b64_tr_b16 v[198:199],v231 offset:4352
buffer_store_dwordx4 v[152:155],v239,s[36:39],s72 offen sc1
v_mfma_f32_16x16x32_bf16 v[20:23],v[204:207],a[212:215],v[20:23]
v_exp_f32 v221,v62
v_mfma_f32_16x16x32_bf16 v[24:27],v[200:203],a[224:227],v[88:91]
v_add_f32 v75,v221,v75
v_mfma_f32_16x16x32_bf16 v[24:27],v[204:207],a[228:231],v[24:27]
v_exp_f32 v222,v59
v_mfma_f32_16x16x32_bf16 v[28:31],v[200:203],a[240:243],v[92:95]
v_exp_f32 v223,v63
v_mfma_f32_16x16x32_bf16 v[28:31],v[204:207],a[244:247],v[28:31]
v_add_f32 v74,v222,v74
v_add_f32 v75,v223,v75
v_max3_f32 v228,v228,v74,v75
v_cmp_nge_f32 vcc,0x46800000,v228
v_cvt_pk_bf16_f32 v139,v220,v222
v_mfma_f32_16x16x32_bf16 v[68:71],v[200:203],a[160:163],v[96:99]
ds_read_b64_tr_b16 v[200:201],v230 offset:512
v_cvt_pk_bf16_f32 v143,v221,v223
v_mfma_f32_16x16x32_bf16 v[68:71],v[204:207],a[164:167],v[68:71]
s_waitcnt lgkmcnt(9)
s_cbranch_vccnz .Lst1
.Lbk1:
ds_read_b64_tr_b16 v[202:203],v230 offset:4608
v_mfma_f32_16x16x32_bf16 v[16:19],v[208:211],a[200:203],v[16:19]
ds_read_b64_tr_b16 v[204:205],v231 offset:512
ds_read_b64_tr_b16 v[206:207],v231 offset:4608
v_mfma_f32_16x16x32_bf16 v[16:19],v[212:215],a[204:207],v[16:19]
v_exp_f32 v216,v0
v_mfma_f32_16x16x32_bf16 v[20:23],v[208:211],a[216:219],v[20:23]
v_add_f32 v104,v72,v104
v_add_f32 v105,v73,v105
v_mfma_f32_16x16x32_bf16 v[20:23],v[212:215],a[220:223],v[20:23]
v_exp_f32 v217,v4
v_mfma_f32_16x16x32_bf16 v[24:27],v[208:211],a[232:235],v[24:27]
buffer_store_dwordx4 v[168:171],v239,s[36:39],s72 offen offset:1024 sc1
v_mfma_f32_16x16x32_bf16 v[24:27],v[212:215],a[236:239],v[24:27]
s_add_u32 s72,s29,6144
v_exp_f32 v218,v1
v_mfma_f32_16x16x32_bf16 v[28:31],v[208:211],a[248:251],v[28:31]
v_add_f32 v72,v216,v218
v_cvt_pk_bf16_f32 v112,v216,v218
v_mfma_f32_16x16x32_bf16 v[28:31],v[212:215],a[252:255],v[28:31]
v_exp_f32 v219,v5
v_mfma_f32_16x16x32_bf16 v[68:71],v[208:211],a[168:171],v[68:71]
v_add_f32 v73,v217,v219
v_cvt_pk_bf16_f32 v116,v217,v219
v_mfma_f32_16x16x32_bf16 v[68:71],v[212:215],a[172:175],v[68:71]
s_waitcnt lgkmcnt(8)
v_exp_f32 v220,v2
v_mfma_f32_16x16x32_bf16 a[0:3],v[184:187],v[128:131],a[0:3]
ds_read_b64_tr_b16 v[208:209],v230 offset:768
ds_read_b64_tr_b16 v[210:211],v230 offset:4864
v_mfma_f32_16x16x32_bf16 a[32:35],v[184:187],v[132:135],a[32:35]
v_exp_f32 v221,v6
v_mfma_f32_16x16x32_bf16 a[64:67],v[184:187],v[136:139],a[64:67]
ds_read_b64_tr_b16 v[212:213],v231 offset:768
v_add_f32 v72,v220,v72
v_mfma_f32_16x16x32_bf16 a[96:99],v[184:187],v[140:143],a[96:99]
ds_read_b64_tr_b16 v[214:215],v231 offset:4864
v_add_f32 v73,v221,v73
v_mfma_f32_16x16x32_bf16 a[4:7],v[188:191],v[128:131],a[4:7]
v_exp_f32 v222,v3
v_mfma_f32_16x16x32_bf16 a[36:39],v[188:191],v[132:135],a[36:39]
buffer_store_dwordx4 v[156:159],v239,s[36:39],s72 offen sc1
v_add_f32 v72,v222,v72
v_mfma_f32_16x16x32_bf16 a[68:71],v[188:191],v[136:139],a[68:71]
ds_read_b128 v[184:187],v226
v_cvt_pk_bf16_f32 v113,v220,v222
v_mfma_f32_16x16x32_bf16 a[100:103],v[188:191],v[140:143],a[100:103]
s_waitcnt lgkmcnt(9)
v_exp_f32 v223,v7
v_mfma_f32_16x16x32_bf16 a[8:11],v[192:195],v[128:131],a[8:11]
v_add_f32 v73,v223,v73
v_cvt_pk_bf16_f32 v117,v221,v223
v_mfma_f32_16x16x32_bf16 a[40:43],v[192:195],v[132:135],a[40:43]
v_exp_f32 v216,v8
v_add_f32 v106,v74,v106
v_mfma_f32_16x16x32_bf16 a[72:75],v[192:195],v[136:139],a[72:75]
v_exp_f32 v217,v12
v_add_f32 v107,v75,v107
v_mfma_f32_16x16x32_bf16 a[104:107],v[192:195],v[140:143],a[104:107]
ds_read_b128 v[188:191],v226 offset:256
buffer_store_dwordx4 v[172:175],v239,s[36:39],s72 offen offset:1024 sc1
v_mfma_f32_16x16x32_bf16 a[12:15],v[196:199],v[128:131],a[12:15]
s_add_u32 s72,s29,12288
v_exp_f32 v218,v9
v_mfma_f32_16x16x32_bf16 a[44:47],v[196:199],v[132:135],a[44:47]
v_add_f32 v74,v216,v218
v_cvt_pk_bf16_f32 v120,v216,v218
v_mfma_f32_16x16x32_bf16 a[76:79],v[196:199],v[136:139],a[76:79]
v_exp_f32 v219,v13
v_mfma_f32_16x16x32_bf16 a[108:111],v[196:199],v[140:143],a[108:111]
s_waitcnt lgkmcnt(6)
v_add_f32 v75,v217,v219
v_cvt_pk_bf16_f32 v124,v217,v219
v_mfma_f32_16x16x32_bf16 a[16:19],v[200:203],v[128:131],a[16:19]
ds_read_b128 v[192:195],v226 offset:512
ds_read_b128 v[196:199],v226 offset:768
v_exp_f32 v220,v10
v_mfma_f32_16x16x32_bf16 a[48:51],v[200:203],v[132:135],a[48:51]
v_exp_f32 v221,v14
v_add_f32 v74,v220,v74
v_mfma_f32_16x16x32_bf16 a[80:83],v[200:203],v[136:139],a[80:83]
buffer_store_dwordx4 v[160:163],v239,s[36:39],s72 offen sc1
v_add_f32 v75,v221,v75
v_mfma_f32_16x16x32_bf16 a[112:115],v[200:203],v[140:143],a[112:115]
v_exp_f32 v222,v11
v_mfma_f32_16x16x32_bf16 a[20:23],v[204:207],v[128:131],a[20:23]
v_add_f32 v74,v222,v74
v_cvt_pk_bf16_f32 v121,v220,v222
v_mfma_f32_16x16x32_bf16 a[52:55],v[204:207],v[132:135],a[52:55]
v_exp_f32 v223,v15
v_mfma_f32_16x16x32_bf16 a[84:87],v[204:207],v[136:139],a[84:87]
v_add_f32 v75,v223,v75
v_cvt_pk_bf16_f32 v125,v221,v223
v_mfma_f32_16x16x32_bf16 a[116:119],v[204:207],v[140:143],a[116:119]
s_waitcnt lgkmcnt(4)
v_exp_f32 v216,v64
v_mov_b32 v108,v76
v_mfma_f32_16x16x32_bf16 a[24:27],v[208:211],v[128:131],a[24:27]
ds_read_b128 v[200:203],v226 offset:4096
ds_read_b128 v[204:207],v226 offset:4352
v_exp_f32 v217,v65
v_mfma_f32_16x16x32_bf16 a[56:59],v[208:211],v[132:135],a[56:59]
v_add_f32 v76,v216,v217
v_cvt_pk_bf16_f32 v144,v216,v217
v_mfma_f32_16x16x32_bf16 a[88:91],v[208:211],v[136:139],a[88:91]
v_exp_f32 v218,v66
v_mfma_f32_16x16x32_bf16 a[120:123],v[208:211],v[140:143],a[120:123]
ds_read_b128 v[208:211],v226 offset:4608
v_add_f32 v76,v218,v76
v_mfma_f32_16x16x32_bf16 a[28:31],v[212:215],v[128:131],a[28:31]
v_exp_f32 v219,v67
v_mfma_f32_16x16x32_bf16 a[60:63],v[212:215],v[132:135],a[60:63]
v_add_f32 v76,v219,v76
v_cvt_pk_bf16_f32 v145,v218,v219
v_mfma_f32_16x16x32_bf16 a[92:95],v[212:215],v[136:139],a[92:95]
v_exp_f32 v220,v16
v_mfma_f32_16x16x32_bf16 a[124:127],v[212:215],v[140:143],a[124:127]
s_waitcnt lgkmcnt(5)
v_exp_f32 v221,v20
v_add_f32 v72,v220,v72
v_mfma_f32_16x16x32_bf16 v[32:35],v[184:187],a[192:195],v[80:83]
buffer_store_dwordx4 v[176:179],v239,s[36:39],s72 offen offset:1024 sc1
v_add_f32 v73,v221,v73
v_mfma_f32_16x16x32_bf16 v[32:35],v[188:191],a[196:199],v[32:35]
s_add_u32 s72,s29,18432
v_exp_f32 v222,v17
v_mfma_f32_16x16x32_bf16 v[36:39],v[184:187],a[208:211],v[84:87]
v_add_f32 v72,v222,v72
v_cvt_pk_bf16_f32 v114,v220,v222
v_mfma_f32_16x16x32_bf16 v[36:39],v[188:191],a[212:215],v[36:39]
v_exp_f32 v223,v21
v_mfma_f32_16x16x32_bf16 v[40:43],v[184:187],a[224:227],v[88:91]
ds_read_b128 v[212:215],v226 offset:4864
v_add_f32 v73,v223,v73
v_mfma_f32_16x16x32_bf16 v[40:43],v[188:191],a[228:231],v[40:43]
buffer_store_dwordx4 v[164:167],v239,s[36:39],s72 offen sc1
v_cvt_pk_bf16_f32 v118,v221,v223
v_exp_f32 v216,v18
v_mfma_f32_16x16x32_bf16 v[44:47],v[184:187],a[240:243],v[92:95]
v_exp_f32 v217,v22
v_mfma_f32_16x16x32_bf16 v[44:47],v[188:191],a[244:247],v[44:47]
s_waitcnt lgkmcnt(4)
v_add_f32 v72,v216,v72
v_add_f32 v73,v217,v73
v_mfma_f32_16x16x32_bf16 v[32:35],v[192:195],a[200:203],v[32:35]
v_exp_f32 v218,v19
v_mfma_f32_16x16x32_bf16 v[32:35],v[196:199],a[204:207],v[32:35]
v_add_f32 v72,v218,v72
v_cvt_pk_bf16_f32 v115,v216,v218
v_mfma_f32_16x16x32_bf16 v[36:39],v[192:195],a[216:219],v[36:39]
v_exp_f32 v219,v23
v_mfma_f32_16x16x32_bf16 v[36:39],v[196:199],a[220:223],v[36:39]
ds_read_b64_tr_b16 v[184:185],v233 offset:16384
ds_read_b64_tr_b16 v[186:187],v233 offset:20480
v_add_f32 v73,v219,v73
v_cvt_pk_bf16_f32 v119,v217,v219
v_mfma_f32_16x16x32_bf16 v[40:43],v[192:195],a[232:235],v[40:43]
ds_read_b64_tr_b16 v[188:189],v234 offset:16384
ds_read_b64_tr_b16 v[190:191],v234 offset:20480
v_mfma_f32_16x16x32_bf16 v[40:43],v[196:199],a[236:239],v[40:43]
s_cmp_eq_u32 s56,32768
s_cselect_b32 s57,0xffff8000,s58
v_exp_f32 v220,v24
v_max_f32 v228,v72,v73
v_exp_f32 v221,v28
v_mfma_f32_16x16x32_bf16 v[44:47],v[192:195],a[248:251],v[44:47]
v_add_f32 v74,v220,v74
v_add_f32 v75,v221,v75
v_mfma_f32_16x16x32_bf16 v[44:47],v[196:199],a[252:255],v[44:47]
s_waitcnt lgkmcnt(6)
v_exp_f32 v222,v25
v_add_u32 v232,s57,v232
v_mfma_f32_16x16x32_bf16 v[48:51],v[200:203],a[192:195],v[80:83]
v_add_f32 v74,v222,v74
v_cvt_pk_bf16_f32 v122,v220,v222
v_mfma_f32_16x16x32_bf16 v[48:51],v[204:207],a[196:199],v[48:51]
v_exp_f32 v223,v29
v_add_u32 v226,s57,v226
v_mfma_f32_16x16x32_bf16 v[52:55],v[200:203],a[208:211],v[84:87]
v_add_f32 v75,v223,v75
v_cvt_pk_bf16_f32 v126,v221,v223
v_mfma_f32_16x16x32_bf16 v[52:55],v[204:207],a[212:215],v[52:55]
buffer_store_dwordx4 v[180:183],v239,s[36:39],s72 offen offset:1024 sc1
ds_read_b64_tr_b16 v[192:193],v233 offset:16640
ds_read_b64_tr_b16 v[194:195],v233 offset:20736
v_exp_f32 v216,v26
v_mfma_f32_16x16x32_bf16 v[56:59],v[200:203],a[224:227],v[88:91]
ds_read_b64_tr_b16 v[196:197],v234 offset:16640
ds_read_b64_tr_b16 v[198:199],v234 offset:20736
v_mfma_f32_16x16x32_bf16 v[56:59],v[204:207],a[228:231],v[56:59]
v_exp_f32 v217,v30
v_add_f32 v74,v216,v74
v_mfma_f32_16x16x32_bf16 v[60:63],v[200:203],a[240:243],v[92:95]
ds_read_b64_tr_b16 v[200:201],v233 offset:16896
v_add_f32 v75,v217,v75
v_mfma_f32_16x16x32_bf16 v[60:63],v[204:207],a[244:247],v[60:63]
s_waitcnt lgkmcnt(9)
v_exp_f32 v218,v27
v_mfma_f32_16x16x32_bf16 v[48:51],v[208:211],a[200:203],v[48:51]
v_add_f32 v74,v218,v74
v_cvt_pk_bf16_f32 v123,v216,v218
v_mfma_f32_16x16x32_bf16 v[48:51],v[212:215],a[204:207],v[48:51]
v_exp_f32 v219,v31
v_mfma_f32_16x16x32_bf16 v[52:55],v[208:211],a[216:219],v[52:55]
v_add_f32 v75,v219,v75
v_cvt_pk_bf16_f32 v127,v217,v219
v_mfma_f32_16x16x32_bf16 v[52:55],v[212:215],a[220:223],v[52:55]
ds_read_b64_tr_b16 v[202:203],v233 offset:20992
v_exp_f32 v220,v68
v_max3_f32 v228,v228,v74,v75
v_exp_f32 v221,v69
v_mfma_f32_16x16x32_bf16 v[56:59],v[208:211],a[232:235],v[56:59]
v_exp_f32 v222,v70
v_exp_f32 v223,v71
v_mfma_f32_16x16x32_bf16 v[56:59],v[212:215],a[236:239],v[56:59]
ds_read_b64_tr_b16 v[204:205],v234 offset:16896
ds_read_b64_tr_b16 v[206:207],v234 offset:20992
v_add_f32 v76,v220,v76
v_add_f32 v76,v221,v76
v_cvt_pk_bf16_f32 v146,v220,v221
v_add_f32 v76,v222,v76
v_add_f32 v76,v223,v76
v_max_f32 v228,v228,v76
v_cmp_nge_f32 vcc,0x46800000,v228
v_mfma_f32_16x16x32_bf16 v[60:63],v[208:211],a[248:251],v[60:63]
ds_read_b64_tr_b16 v[208:209],v233 offset:17152
v_cvt_pk_bf16_f32 v147,v222,v223
v_mfma_f32_16x16x32_bf16 v[60:63],v[212:215],a[252:255],v[60:63]
s_waitcnt lgkmcnt(9)
s_cbranch_vccnz .Lst2
.Lbk2:
ds_read_b64_tr_b16 v[210:211],v233 offset:21248
v_mfma_f32_16x16x32_bf16 a[0:3],v[184:187],v[112:115],a[0:3]
s_waitcnt vmcnt(26)
s_cmp_lg_u32 s65,0
s_cbranch_scc1 .Ldw1
s_waitcnt vmcnt(16)
.Ldw1:
s_barrier
v_mfma_f32_16x16x32_bf16 a[32:35],v[184:187],v[116:119],a[32:35]
s_add_u32 m0,s63,65536
ds_read_b64_tr_b16 v[212:213],v234 offset:17152
ds_read_b64_tr_b16 v[214:215],v234 offset:21248
v_mfma_f32_16x16x32_bf16 a[64:67],v[184:187],v[120:123],a[64:67]
v_exp_f32 v216,v32
v_mfma_f32_16x16x32_bf16 a[96:99],v[184:187],v[124:127],a[96:99]
buffer_load_dwordx4 v235,s[48:51],s61 offen nt lds
v_add_f32 v104,v72,v104
v_mfma_f32_16x16x32_bf16 a[128:131],v[184:187],v[144:147],a[128:131]
v_exp_f32 v217,v36
v_mfma_f32_16x16x32_bf16 a[4:7],v[188:191],v[112:115],a[4:7]
ds_read_b128 v[184:187],v232
v_add_f32 v105,v73,v105
v_mfma_f32_16x16x32_bf16 a[36:39],v[188:191],v[116:119],a[36:39]
v_exp_f32 v218,v33
v_mfma_f32_16x16x32_bf16 a[68:71],v[188:191],v[120:123],a[68:71]
v_add_f32 v72,v216,v218
v_cvt_pk_bf16_f32 v128,v216,v218
v_mfma_f32_16x16x32_bf16 a[100:103],v[188:191],v[124:127],a[100:103]
v_exp_f32 v219,v37
v_mfma_f32_16x16x32_bf16 a[132:135],v[188:191],v[144:147],a[132:135]
s_waitcnt lgkmcnt(9)
v_add_f32 v73,v217,v219
v_cvt_pk_bf16_f32 v132,v217,v219
v_mfma_f32_16x16x32_bf16 a[8:11],v[192:195],v[112:115],a[8:11]
v_exp_f32 v220,v34
v_mfma_f32_16x16x32_bf16 a[40:43],v[192:195],v[116:119],a[40:43]
ds_read_b128 v[188:191],v232 offset:256
v_add_f32 v72,v220,v72
v_mfma_f32_16x16x32_bf16 a[72:75],v[192:195],v[120:123],a[72:75]
buffer_load_dwordx4 v236,s[48:51],s61 offen offset:1024 nt lds
)A"
R"A(buffer_load_dwordx4 v235,s[48:51],s61 offen offset:2048 nt lds
v_mfma_f32_16x16x32_bf16 a[104:107],v[192:195],v[124:127],a[104:107]
v_exp_f32 v221,v38
v_mfma_f32_16x16x32_bf16 a[136:139],v[192:195],v[144:147],a[136:139]
ds_read_b128 v[192:195],v232 offset:512
v_add_f32 v73,v221,v73
v_mfma_f32_16x16x32_bf16 a[12:15],v[196:199],v[112:115],a[12:15]
v_exp_f32 v222,v35
v_mfma_f32_16x16x32_bf16 a[44:47],v[196:199],v[116:119],a[44:47]
v_add_f32 v72,v222,v72
v_cvt_pk_bf16_f32 v129,v220,v222
v_mfma_f32_16x16x32_bf16 a[76:79],v[196:199],v[120:123],a[76:79]
v_exp_f32 v223,v39
v_mfma_f32_16x16x32_bf16 a[108:111],v[196:199],v[124:127],a[108:111]
v_add_f32 v73,v223,v73
v_cvt_pk_bf16_f32 v133,v221,v223
v_mfma_f32_16x16x32_bf16 a[140:143],v[196:199],v[144:147],a[140:143]
s_waitcnt lgkmcnt(7)
v_exp_f32 v216,v40
v_mfma_f32_16x16x32_bf16 a[16:19],v[200:203],v[112:115],a[16:19]
v_add_f32 v106,v74,v106
v_add_f32 v107,v75,v107
v_mfma_f32_16x16x32_bf16 a[48:51],v[200:203],v[116:119],a[48:51]
v_exp_f32 v217,v44
v_mfma_f32_16x16x32_bf16 a[80:83],v[200:203],v[120:123],a[80:83]
ds_read_b128 v[196:199],v232 offset:768
buffer_load_dwordx4 v236,s[48:51],s61 offen offset:3072 nt lds
v_mfma_f32_16x16x32_bf16 a[112:115],v[200:203],v[124:127],a[112:115]
v_exp_f32 v218,v41
v_mfma_f32_16x16x32_bf16 a[144:147],v[200:203],v[144:147],a[144:147]
v_add_f32 v74,v216,v218
v_cvt_pk_bf16_f32 v136,v216,v218
v_mfma_f32_16x16x32_bf16 a[20:23],v[204:207],v[112:115],a[20:23]
v_exp_f32 v219,v45
v_mfma_f32_16x16x32_bf16 a[52:55],v[204:207],v[116:119],a[52:55]
s_add_u32 m0,s56,s63
v_add_f32 v75,v217,v219
v_cvt_pk_bf16_f32 v140,v217,v219
v_mfma_f32_16x16x32_bf16 a[84:87],v[204:207],v[120:123],a[84:87]
ds_read_b128 v[200:203],v232 offset:4096
v_exp_f32 v220,v42
v_mfma_f32_16x16x32_bf16 a[116:119],v[204:207],v[124:127],a[116:119]
buffer_load_dwordx4 v235,s[44:47],s61 offen nt lds
v_add_f32 v74,v220,v74
v_mfma_f32_16x16x32_bf16 a[148:151],v[204:207],v[144:147],a[148:151]
s_waitcnt lgkmcnt(5)
v_exp_f32 v221,v46
v_mfma_f32_16x16x32_bf16 a[24:27],v[208:211],v[112:115],a[24:27]
ds_read_b128 v[204:207],v232 offset:4352
v_add_f32 v75,v221,v75
v_mfma_f32_16x16x32_bf16 a[56:59],v[208:211],v[116:119],a[56:59]
v_exp_f32 v222,v43
v_mfma_f32_16x16x32_bf16 a[88:91],v[208:211],v[120:123],a[88:91]
v_add_f32 v74,v222,v74
v_cvt_pk_bf16_f32 v137,v220,v222
v_mfma_f32_16x16x32_bf16 a[120:123],v[208:211],v[124:127],a[120:123]
v_exp_f32 v223,v47
v_mfma_f32_16x16x32_bf16 a[152:155],v[208:211],v[144:147],a[152:155]
v_add_f32 v75,v223,v75
v_cvt_pk_bf16_f32 v141,v221,v223
v_mfma_f32_16x16x32_bf16 a[28:31],v[212:215],v[112:115],a[28:31]
v_exp_f32 v216,v48
v_mfma_f32_16x16x32_bf16 a[60:63],v[212:215],v[116:119],a[60:63]
ds_read_b128 v[208:211],v232 offset:4608
v_add_f32 v72,v216,v72
v_mfma_f32_16x16x32_bf16 a[92:95],v[212:215],v[120:123],a[92:95]
v_exp_f32 v217,v52
v_mfma_f32_16x16x32_bf16 a[124:127],v[212:215],v[124:127],a[124:127]
buffer_load_dwordx4 v235,s[44:47],s61 offen offset:1024 nt lds
v_add_f32 v73,v217,v73
v_mfma_f32_16x16x32_bf16 a[156:159],v[212:215],v[144:147],a[156:159]
s_waitcnt lgkmcnt(5)
v_exp_f32 v218,v49
v_mfma_f32_16x16x32_bf16 v[0:3],v[184:187],a[192:195],v[80:83]
v_add_f32 v72,v218,v72
v_cvt_pk_bf16_f32 v130,v216,v218
v_mfma_f32_16x16x32_bf16 v[0:3],v[188:191],a[196:199],v[0:3]
ds_read_b128 v[212:215],v232 offset:4864
v_exp_f32 v219,v53
v_mfma_f32_16x16x32_bf16 v[4:7],v[184:187],a[208:211],v[84:87]
v_add_f32 v73,v219,v73
v_cvt_pk_bf16_f32 v134,v217,v219
v_mfma_f32_16x16x32_bf16 v[4:7],v[188:191],a[212:215],v[4:7]
v_exp_f32 v220,v50
v_mfma_f32_16x16x32_bf16 v[8:11],v[184:187],a[224:227],v[88:91]
buffer_load_dwordx4 v237,s[44:47],s61 offen offset:2048 nt lds
v_add_f32 v72,v220,v72
v_mfma_f32_16x16x32_bf16 v[8:11],v[188:191],a[228:231],v[8:11]
v_exp_f32 v221,v54
v_mfma_f32_16x16x32_bf16 v[12:15],v[184:187],a[240:243],v[92:95]
v_add_f32 v73,v221,v73
v_mfma_f32_16x16x32_bf16 v[12:15],v[188:191],a[244:247],v[12:15]
v_exp_f32 v222,v51
v_mfma_f32_16x16x32_bf16 v[64:67],v[184:187],a[160:163],v[96:99]
v_add_f32 v72,v222,v72
v_cvt_pk_bf16_f32 v131,v220,v222
v_mfma_f32_16x16x32_bf16 v[64:67],v[188:191],a[164:167],v[64:67]
s_waitcnt lgkmcnt(4)
v_exp_f32 v223,v55
v_mfma_f32_16x16x32_bf16 v[0:3],v[192:195],a[200:203],v[0:3]
ds_read_b64_tr_b16 v[184:185],v230 offset:16384
ds_read_b64_tr_b16 v[186:187],v230 offset:20480
v_mfma_f32_16x16x32_bf16 v[0:3],v[196:199],a[204:207],v[0:3]
v_add_f32 v73,v223,v73
v_cvt_pk_bf16_f32 v135,v221,v223
v_mfma_f32_16x16x32_bf16 v[4:7],v[192:195],a[216:219],v[4:7]
ds_read_b64_tr_b16 v[188:189],v231 offset:16384
ds_read_b64_tr_b16 v[190:191],v231 offset:20480
v_mfma_f32_16x16x32_bf16 v[4:7],v[196:199],a[220:223],v[4:7]
v_exp_f32 v216,v56
v_mfma_f32_16x16x32_bf16 v[8:11],v[192:195],a[232:235],v[8:11]
v_max_f32 v228,v72,v73
v_add_f32 v74,v216,v74
v_mfma_f32_16x16x32_bf16 v[8:11],v[196:199],a[236:239],v[8:11]
v_exp_f32 v217,v60
v_mfma_f32_16x16x32_bf16 v[12:15],v[192:195],a[248:251],v[12:15]
buffer_load_dwordx4 v237,s[44:47],s61 offen offset:3072 nt lds
v_add_f32 v75,v217,v75
v_mfma_f32_16x16x32_bf16 v[12:15],v[196:199],a[252:255],v[12:15]
v_exp_f32 v218,v57
v_mfma_f32_16x16x32_bf16 v[64:67],v[192:195],a[168:171],v[64:67]
s_add_u32 s62,s62,16384
v_add_f32 v74,v218,v74
v_cvt_pk_bf16_f32 v138,v216,v218
v_mfma_f32_16x16x32_bf16 v[64:67],v[196:199],a[172:175],v[64:67]
s_waitcnt lgkmcnt(6)
v_exp_f32 v219,v61
v_mfma_f32_16x16x32_bf16 v[16:19],v[200:203],a[192:195],v[80:83]
s_xor_b32 s61,s62,s75
v_add_f32 v75,v219,v75
v_cvt_pk_bf16_f32 v142,v217,v219
v_mfma_f32_16x16x32_bf16 v[16:19],v[204:207],a[196:199],v[16:19]
s_add_u32 s56,s56,s57
v_exp_f32 v220,v58
v_mfma_f32_16x16x32_bf16 v[20:23],v[200:203],a[208:211],v[84:87]
ds_read_b64_tr_b16 v[192:193],v230 offset:16640
ds_read_b64_tr_b16 v[194:195],v230 offset:20736
v_mfma_f32_16x16x32_bf16 v[20:23],v[204:207],a[212:215],v[20:23]
ds_read_b64_tr_b16 v[196:197],v231 offset:16640
v_add_f32 v74,v220,v74
v_mfma_f32_16x16x32_bf16 v[24:27],v[200:203],a[224:227],v[88:91]
ds_read_b64_tr_b16 v[198:199],v231 offset:20736
v_mfma_f32_16x16x32_bf16 v[24:27],v[204:207],a[228:231],v[24:27]
v_exp_f32 v221,v62
v_mfma_f32_16x16x32_bf16 v[28:31],v[200:203],a[240:243],v[92:95]
v_exp_f32 v222,v59
v_exp_f32 v223,v63
v_mfma_f32_16x16x32_bf16 v[28:31],v[204:207],a[244:247],v[28:31]
v_add_f32 v75,v221,v75
v_add_f32 v74,v222,v74
v_add_f32 v75,v223,v75
v_max3_f32 v228,v228,v74,v75
v_cmp_nge_f32 vcc,0x46800000,v228
v_cvt_pk_bf16_f32 v139,v220,v222
v_mfma_f32_16x16x32_bf16 v[68:71],v[200:203],a[160:163],v[96:99]
ds_read_b64_tr_b16 v[200:201],v230 offset:16896
v_cvt_pk_bf16_f32 v143,v221,v223
v_mfma_f32_16x16x32_bf16 v[68:71],v[204:207],a[164:167],v[68:71]
s_waitcnt lgkmcnt(9)
s_cbranch_vccnz .Lst3
.Lbk3:
ds_read_b64_tr_b16 v[202:203],v230 offset:20992
v_mfma_f32_16x16x32_bf16 v[16:19],v[208:211],a[200:203],v[16:19]
ds_read_b64_tr_b16 v[204:205],v231 offset:16896
ds_read_b64_tr_b16 v[206:207],v231 offset:20992
v_mfma_f32_16x16x32_bf16 v[16:19],v[212:215],a[204:207],v[16:19]
v_exp_f32 v216,v0
v_mfma_f32_16x16x32_bf16 v[20:23],v[208:211],a[216:219],v[20:23]
v_add_f32 v104,v72,v104
v_add_f32 v105,v73,v105
v_mfma_f32_16x16x32_bf16 v[20:23],v[212:215],a[220:223],v[20:23]
v_exp_f32 v217,v4
v_mfma_f32_16x16x32_bf16 v[24:27],v[208:211],a[232:235],v[24:27]
v_exp_f32 v218,v1
v_mfma_f32_16x16x32_bf16 v[24:27],v[212:215],a[236:239],v[24:27]
v_add_f32 v72,v216,v218
v_cvt_pk_bf16_f32 v112,v216,v218
v_mfma_f32_16x16x32_bf16 v[28:31],v[208:211],a[248:251],v[28:31]
v_exp_f32 v219,v5
v_mfma_f32_16x16x32_bf16 v[28:31],v[212:215],a[252:255],v[28:31]
v_add_f32 v73,v217,v219
v_cvt_pk_bf16_f32 v116,v217,v219
v_mfma_f32_16x16x32_bf16 v[68:71],v[208:211],a[168:171],v[68:71]
v_exp_f32 v220,v2
v_mfma_f32_16x16x32_bf16 v[68:71],v[212:215],a[172:175],v[68:71]
s_waitcnt lgkmcnt(8)
ds_read_b64_tr_b16 v[208:209],v230 offset:17152
ds_read_b64_tr_b16 v[210:211],v230 offset:21248
v_mfma_f32_16x16x32_bf16 a[0:3],v[184:187],v[128:131],a[0:3]
v_exp_f32 v221,v6
v_mfma_f32_16x16x32_bf16 a[32:35],v[184:187],v[132:135],a[32:35]
ds_read_b64_tr_b16 v[212:213],v231 offset:17152
v_add_f32 v72,v220,v72
v_mfma_f32_16x16x32_bf16 a[64:67],v[184:187],v[136:139],a[64:67]
ds_read_b64_tr_b16 v[214:215],v231 offset:21248
v_add_f32 v73,v221,v73
v_mfma_f32_16x16x32_bf16 a[96:99],v[184:187],v[140:143],a[96:99]
v_exp_f32 v222,v3
v_mfma_f32_16x16x32_bf16 a[4:7],v[188:191],v[128:131],a[4:7]
v_add_f32 v72,v222,v72
v_cvt_pk_bf16_f32 v113,v220,v222
v_mfma_f32_16x16x32_bf16 a[36:39],v[188:191],v[132:135],a[36:39]
v_exp_f32 v223,v7
v_mfma_f32_16x16x32_bf16 a[68:71],v[188:191],v[136:139],a[68:71]
ds_read_b128 v[184:187],v226
v_add_f32 v73,v223,v73
v_mfma_f32_16x16x32_bf16 a[100:103],v[188:191],v[140:143],a[100:103]
s_waitcnt lgkmcnt(9)
v_cvt_pk_bf16_f32 v117,v221,v223
v_add_f32 v106,v74,v106
v_mfma_f32_16x16x32_bf16 a[8:11],v[192:195],v[128:131],a[8:11]
v_exp_f32 v216,v8
v_mfma_f32_16x16x32_bf16 a[40:43],v[192:195],v[132:135],a[40:43]
v_exp_f32 v217,v12
v_mfma_f32_16x16x32_bf16 a[72:75],v[192:195],v[136:139],a[72:75]
ds_read_b128 v[188:191],v226 offset:256
v_add_f32 v107,v75,v107
v_mfma_f32_16x16x32_bf16 a[104:107],v[192:195],v[140:143],a[104:107]
v_exp_f32 v218,v9
v_mfma_f32_16x16x32_bf16 a[12:15],v[196:199],v[128:131],a[12:15]
v_add_f32 v74,v216,v218
v_cvt_pk_bf16_f32 v120,v216,v218
v_mfma_f32_16x16x32_bf16 a[44:47],v[196:199],v[132:135],a[44:47]
v_exp_f32 v219,v13
v_mfma_f32_16x16x32_bf16 a[76:79],v[196:199],v[136:139],a[76:79]
v_add_f32 v75,v217,v219
v_cvt_pk_bf16_f32 v124,v217,v219
v_mfma_f32_16x16x32_bf16 a[108:111],v[196:199],v[140:143],a[108:111]
s_waitcnt lgkmcnt(6)
ds_read_b128 v[192:195],v226 offset:512
ds_read_b128 v[196:199],v226 offset:768
v_mfma_f32_16x16x32_bf16 a[16:19],v[200:203],v[128:131],a[16:19]
v_exp_f32 v220,v10
v_mfma_f32_16x16x32_bf16 a[48:51],v[200:203],v[132:135],a[48:51]
v_exp_f32 v221,v14
v_add_f32 v74,v220,v74
v_mfma_f32_16x16x32_bf16 a[80:83],v[200:203],v[136:139],a[80:83]
v_add_f32 v75,v221,v75
v_exp_f32 v222,v11
v_mfma_f32_16x16x32_bf16 a[112:115],v[200:203],v[140:143],a[112:115]
v_add_f32 v74,v222,v74
v_cvt_pk_bf16_f32 v121,v220,v222
v_mfma_f32_16x16x32_bf16 a[20:23],v[204:207],v[128:131],a[20:23]
v_exp_f32 v223,v15
v_mfma_f32_16x16x32_bf16 a[52:55],v[204:207],v[132:135],a[52:55]
v_add_f32 v75,v223,v75
v_cvt_pk_bf16_f32 v125,v221,v223
v_mfma_f32_16x16x32_bf16 a[84:87],v[204:207],v[136:139],a[84:87]
v_exp_f32 v216,v64
v_mfma_f32_16x16x32_bf16 a[116:119],v[204:207],v[140:143],a[116:119]
s_waitcnt lgkmcnt(4)
ds_read_b128 v[200:203],v226 offset:4096
v_add_f32 v108,v76,v108
v_mfma_f32_16x16x32_bf16 a[24:27],v[208:211],v[128:131],a[24:27]
v_exp_f32 v217,v65
v_mfma_f32_16x16x32_bf16 a[56:59],v[208:211],v[132:135],a[56:59]
v_add_f32 v76,v216,v217
v_cvt_pk_bf16_f32 v144,v216,v217
v_mfma_f32_16x16x32_bf16 a[88:91],v[208:211],v[136:139],a[88:91]
v_exp_f32 v218,v66
v_mfma_f32_16x16x32_bf16 a[120:123],v[208:211],v[140:143],a[120:123]
ds_read_b128 v[204:207],v226 offset:4352
v_add_f32 v76,v218,v76
v_mfma_f32_16x16x32_bf16 a[28:31],v[212:215],v[128:131],a[28:31]
v_exp_f32 v219,v67
v_mfma_f32_16x16x32_bf16 a[60:63],v[212:215],v[132:135],a[60:63]
v_add_f32 v76,v219,v76
v_cvt_pk_bf16_f32 v145,v218,v219
v_mfma_f32_16x16x32_bf16 a[92:95],v[212:215],v[136:139],a[92:95]
v_exp_f32 v220,v16
v_mfma_f32_16x16x32_bf16 a[124:127],v[212:215],v[140:143],a[124:127]
s_waitcnt lgkmcnt(4)
v_exp_f32 v221,v20
v_add_f32 v72,v220,v72
v_mfma_f32_16x16x32_bf16 v[32:35],v[184:187],a[192:195],v[80:83]
ds_read_b128 v[208:211],v226 offset:4608
v_add_f32 v73,v221,v73
v_mfma_f32_16x16x32_bf16 v[32:35],v[188:191],a[196:199],v[32:35]
v_exp_f32 v222,v17
v_mfma_f32_16x16x32_bf16 v[36:39],v[184:187],a[208:211],v[84:87]
v_add_f32 v72,v222,v72
v_cvt_pk_bf16_f32 v114,v220,v222
v_mfma_f32_16x16x32_bf16 v[36:39],v[188:191],a[212:215],v[36:39]
v_exp_f32 v223,v21
v_mfma_f32_16x16x32_bf16 v[40:43],v[184:187],a[224:227],v[88:91]
ds_read_b128 v[212:215],v226 offset:4864
v_add_f32 v73,v223,v73
v_mfma_f32_16x16x32_bf16 v[40:43],v[188:191],a[228:231],v[40:43]
s_cmp_eq_u32 s56,32768
s_cselect_b32 s57,0xffff8000,s58
)A"
R"A(v_cvt_pk_bf16_f32 v118,v221,v223
v_exp_f32 v216,v18
v_mfma_f32_16x16x32_bf16 v[44:47],v[184:187],a[240:243],v[92:95]
v_exp_f32 v217,v22
v_mfma_f32_16x16x32_bf16 v[44:47],v[188:191],a[244:247],v[44:47]
s_waitcnt lgkmcnt(4)
v_add_f32 v72,v216,v72
v_add_f32 v73,v217,v73
v_mfma_f32_16x16x32_bf16 v[32:35],v[192:195],a[200:203],v[32:35]
v_exp_f32 v218,v19
v_mfma_f32_16x16x32_bf16 v[32:35],v[196:199],a[204:207],v[32:35]
v_add_f32 v72,v218,v72
v_cvt_pk_bf16_f32 v115,v216,v218
v_mfma_f32_16x16x32_bf16 v[36:39],v[192:195],a[216:219],v[36:39]
v_exp_f32 v219,v23
v_mfma_f32_16x16x32_bf16 v[36:39],v[196:199],a[220:223],v[36:39]
ds_read_b64_tr_b16 v[184:185],v233 offset:32768
ds_read_b64_tr_b16 v[186:187],v233 offset:36864
v_add_f32 v73,v219,v73
v_cvt_pk_bf16_f32 v119,v217,v219
v_mfma_f32_16x16x32_bf16 v[40:43],v[192:195],a[232:235],v[40:43]
ds_read_b64_tr_b16 v[188:189],v234 offset:32768
ds_read_b64_tr_b16 v[190:191],v234 offset:36864
v_mfma_f32_16x16x32_bf16 v[40:43],v[196:199],a[236:239],v[40:43]
v_exp_f32 v220,v24
v_max_f32 v228,v72,v73
v_exp_f32 v221,v28
v_mfma_f32_16x16x32_bf16 v[44:47],v[192:195],a[248:251],v[44:47]
v_add_f32 v74,v220,v74
v_add_f32 v75,v221,v75
v_mfma_f32_16x16x32_bf16 v[44:47],v[196:199],a[252:255],v[44:47]
s_waitcnt lgkmcnt(6)
v_exp_f32 v222,v25
v_add_u32 v232,s57,v232
v_mfma_f32_16x16x32_bf16 v[48:51],v[200:203],a[192:195],v[80:83]
v_add_f32 v74,v222,v74
v_cvt_pk_bf16_f32 v122,v220,v222
v_mfma_f32_16x16x32_bf16 v[48:51],v[204:207],a[196:199],v[48:51]
v_exp_f32 v223,v29
v_add_u32 v226,s57,v226
v_mfma_f32_16x16x32_bf16 v[52:55],v[200:203],a[208:211],v[84:87]
v_add_f32 v75,v223,v75
v_cvt_pk_bf16_f32 v126,v221,v223
v_mfma_f32_16x16x32_bf16 v[52:55],v[204:207],a[212:215],v[52:55]
ds_read_b64_tr_b16 v[192:193],v233 offset:33024
ds_read_b64_tr_b16 v[194:195],v233 offset:37120
v_exp_f32 v216,v26
v_mfma_f32_16x16x32_bf16 v[56:59],v[200:203],a[224:227],v[88:91]
ds_read_b64_tr_b16 v[196:197],v234 offset:33024
ds_read_b64_tr_b16 v[198:199],v234 offset:37120
v_mfma_f32_16x16x32_bf16 v[56:59],v[204:207],a[228:231],v[56:59]
v_exp_f32 v217,v30
v_add_f32 v74,v216,v74
v_mfma_f32_16x16x32_bf16 v[60:63],v[200:203],a[240:243],v[92:95]
ds_read_b64_tr_b16 v[200:201],v233 offset:33280
v_add_f32 v75,v217,v75
v_mfma_f32_16x16x32_bf16 v[60:63],v[204:207],a[244:247],v[60:63]
s_waitcnt lgkmcnt(9)
v_exp_f32 v218,v27
v_mfma_f32_16x16x32_bf16 v[48:51],v[208:211],a[200:203],v[48:51]
v_add_f32 v74,v218,v74
v_cvt_pk_bf16_f32 v123,v216,v218
v_mfma_f32_16x16x32_bf16 v[48:51],v[212:215],a[204:207],v[48:51]
v_exp_f32 v219,v31
v_mfma_f32_16x16x32_bf16 v[52:55],v[208:211],a[216:219],v[52:55]
v_add_f32 v75,v219,v75
v_cvt_pk_bf16_f32 v127,v217,v219
v_mfma_f32_16x16x32_bf16 v[52:55],v[212:215],a[220:223],v[52:55]
ds_read_b64_tr_b16 v[202:203],v233 offset:37376
v_exp_f32 v220,v68
v_max3_f32 v228,v228,v74,v75
v_exp_f32 v221,v69
v_mfma_f32_16x16x32_bf16 v[56:59],v[208:211],a[232:235],v[56:59]
v_exp_f32 v222,v70
v_exp_f32 v223,v71
v_mfma_f32_16x16x32_bf16 v[56:59],v[212:215],a[236:239],v[56:59]
ds_read_b64_tr_b16 v[204:205],v234 offset:33280
ds_read_b64_tr_b16 v[206:207],v234 offset:37376
v_add_f32 v76,v220,v76
v_add_f32 v76,v221,v76
v_cvt_pk_bf16_f32 v146,v220,v221
v_add_f32 v76,v222,v76
v_add_f32 v76,v223,v76
v_max_f32 v228,v228,v76
v_cmp_nge_f32 vcc,0x46800000,v228
v_mfma_f32_16x16x32_bf16 v[60:63],v[208:211],a[248:251],v[60:63]
ds_read_b64_tr_b16 v[208:209],v233 offset:33536
v_cvt_pk_bf16_f32 v147,v222,v223
v_mfma_f32_16x16x32_bf16 v[60:63],v[212:215],a[252:255],v[60:63]
s_waitcnt lgkmcnt(9)
s_cbranch_vccnz .Lst4
.Lbk4:
ds_read_b64_tr_b16 v[210:211],v233 offset:37632
s_mov_b32 s64,3
.La:
v_mfma_f32_16x16x32_bf16 a[0:3],v[184:187],v[112:115],a[0:3]
s_waitcnt vmcnt(16)
s_cmp_eq_u32 s64,3
s_cbranch_scc1 .Ldw2
s_waitcnt vmcnt(8)
.Ldw2:
s_barrier
v_mfma_f32_16x16x32_bf16 a[32:35],v[184:187],v[116:119],a[32:35]
s_add_u32 m0,s63,81920
ds_read_b64_tr_b16 v[212:213],v234 offset:33536
ds_read_b64_tr_b16 v[214:215],v234 offset:37632
v_mfma_f32_16x16x32_bf16 a[64:67],v[184:187],v[120:123],a[64:67]
v_exp_f32 v216,v32
v_mfma_f32_16x16x32_bf16 a[96:99],v[184:187],v[124:127],a[96:99]
buffer_load_dwordx4 v235,s[48:51],s61 offen nt lds
v_add_f32 v104,v72,v104
v_mfma_f32_16x16x32_bf16 a[128:131],v[184:187],v[144:147],a[128:131]
v_exp_f32 v217,v36
v_mfma_f32_16x16x32_bf16 a[4:7],v[188:191],v[112:115],a[4:7]
ds_read_b128 v[184:187],v232
v_add_f32 v105,v73,v105
v_mfma_f32_16x16x32_bf16 a[36:39],v[188:191],v[116:119],a[36:39]
v_exp_f32 v218,v33
v_mfma_f32_16x16x32_bf16 a[68:71],v[188:191],v[120:123],a[68:71]
v_add_f32 v72,v216,v218
v_cvt_pk_bf16_f32 v128,v216,v218
v_mfma_f32_16x16x32_bf16 a[100:103],v[188:191],v[124:127],a[100:103]
v_exp_f32 v219,v37
v_mfma_f32_16x16x32_bf16 a[132:135],v[188:191],v[144:147],a[132:135]
s_waitcnt lgkmcnt(9)
v_add_f32 v73,v217,v219
v_cvt_pk_bf16_f32 v132,v217,v219
v_mfma_f32_16x16x32_bf16 a[8:11],v[192:195],v[112:115],a[8:11]
v_exp_f32 v220,v34
v_mfma_f32_16x16x32_bf16 a[40:43],v[192:195],v[116:119],a[40:43]
ds_read_b128 v[188:191],v232 offset:256
v_add_f32 v72,v220,v72
v_mfma_f32_16x16x32_bf16 a[72:75],v[192:195],v[120:123],a[72:75]
buffer_load_dwordx4 v236,s[48:51],s61 offen offset:1024 nt lds
buffer_load_dwordx4 v235,s[48:51],s61 offen offset:2048 nt lds
v_mfma_f32_16x16x32_bf16 a[104:107],v[192:195],v[124:127],a[104:107]
v_exp_f32 v221,v38
v_mfma_f32_16x16x32_bf16 a[136:139],v[192:195],v[144:147],a[136:139]
ds_read_b128 v[192:195],v232 offset:512
v_add_f32 v73,v221,v73
v_mfma_f32_16x16x32_bf16 a[12:15],v[196:199],v[112:115],a[12:15]
v_exp_f32 v222,v35
v_mfma_f32_16x16x32_bf16 a[44:47],v[196:199],v[116:119],a[44:47]
v_add_f32 v72,v222,v72
v_cvt_pk_bf16_f32 v129,v220,v222
v_mfma_f32_16x16x32_bf16 a[76:79],v[196:199],v[120:123],a[76:79]
v_exp_f32 v223,v39
v_mfma_f32_16x16x32_bf16 a[108:111],v[196:199],v[124:127],a[108:111]
v_add_f32 v73,v223,v73
v_cvt_pk_bf16_f32 v133,v221,v223
v_mfma_f32_16x16x32_bf16 a[140:143],v[196:199],v[144:147],a[140:143]
s_waitcnt lgkmcnt(7)
v_exp_f32 v216,v40
v_mfma_f32_16x16x32_bf16 a[16:19],v[200:203],v[112:115],a[16:19]
v_add_f32 v106,v74,v106
v_add_f32 v107,v75,v107
v_mfma_f32_16x16x32_bf16 a[48:51],v[200:203],v[116:119],a[48:51]
v_exp_f32 v217,v44
v_mfma_f32_16x16x32_bf16 a[80:83],v[200:203],v[120:123],a[80:83]
ds_read_b128 v[196:199],v232 offset:768
buffer_load_dwordx4 v236,s[48:51],s61 offen offset:3072 nt lds
v_mfma_f32_16x16x32_bf16 a[112:115],v[200:203],v[124:127],a[112:115]
v_exp_f32 v218,v41
v_mfma_f32_16x16x32_bf16 a[144:147],v[200:203],v[144:147],a[144:147]
v_add_f32 v74,v216,v218
v_cvt_pk_bf16_f32 v136,v216,v218
v_mfma_f32_16x16x32_bf16 a[20:23],v[204:207],v[112:115],a[20:23]
v_exp_f32 v219,v45
v_mfma_f32_16x16x32_bf16 a[52:55],v[204:207],v[116:119],a[52:55]
s_add_u32 m0,s56,s63
v_add_f32 v75,v217,v219
v_cvt_pk_bf16_f32 v140,v217,v219
v_mfma_f32_16x16x32_bf16 a[84:87],v[204:207],v[120:123],a[84:87]
ds_read_b128 v[200:203],v232 offset:4096
v_exp_f32 v220,v42
v_mfma_f32_16x16x32_bf16 a[116:119],v[204:207],v[124:127],a[116:119]
buffer_load_dwordx4 v235,s[44:47],s61 offen nt lds
v_add_f32 v74,v220,v74
v_mfma_f32_16x16x32_bf16 a[148:151],v[204:207],v[144:147],a[148:151]
s_waitcnt lgkmcnt(5)
v_exp_f32 v221,v46
v_mfma_f32_16x16x32_bf16 a[24:27],v[208:211],v[112:115],a[24:27]
ds_read_b128 v[204:207],v232 offset:4352
v_add_f32 v75,v221,v75
v_mfma_f32_16x16x32_bf16 a[56:59],v[208:211],v[116:119],a[56:59]
v_exp_f32 v222,v43
v_mfma_f32_16x16x32_bf16 a[88:91],v[208:211],v[120:123],a[88:91]
v_add_f32 v74,v222,v74
v_cvt_pk_bf16_f32 v137,v220,v222
v_mfma_f32_16x16x32_bf16 a[120:123],v[208:211],v[124:127],a[120:123]
v_exp_f32 v223,v47
v_mfma_f32_16x16x32_bf16 a[152:155],v[208:211],v[144:147],a[152:155]
v_add_f32 v75,v223,v75
v_cvt_pk_bf16_f32 v141,v221,v223
v_mfma_f32_16x16x32_bf16 a[28:31],v[212:215],v[112:115],a[28:31]
v_exp_f32 v216,v48
v_mfma_f32_16x16x32_bf16 a[60:63],v[212:215],v[116:119],a[60:63]
ds_read_b128 v[208:211],v232 offset:4608
v_add_f32 v72,v216,v72
v_mfma_f32_16x16x32_bf16 a[92:95],v[212:215],v[120:123],a[92:95]
v_exp_f32 v217,v52
v_mfma_f32_16x16x32_bf16 a[124:127],v[212:215],v[124:127],a[124:127]
buffer_load_dwordx4 v235,s[44:47],s61 offen offset:1024 nt lds
v_add_f32 v73,v217,v73
v_mfma_f32_16x16x32_bf16 a[156:159],v[212:215],v[144:147],a[156:159]
s_waitcnt lgkmcnt(5)
v_exp_f32 v218,v49
v_mfma_f32_16x16x32_bf16 v[0:3],v[184:187],a[192:195],v[80:83]
v_add_f32 v72,v218,v72
v_cvt_pk_bf16_f32 v130,v216,v218
v_mfma_f32_16x16x32_bf16 v[0:3],v[188:191],a[196:199],v[0:3]
ds_read_b128 v[212:215],v232 offset:4864
v_exp_f32 v219,v53
v_mfma_f32_16x16x32_bf16 v[4:7],v[184:187],a[208:211],v[84:87]
v_add_f32 v73,v219,v73
v_cvt_pk_bf16_f32 v134,v217,v219
v_mfma_f32_16x16x32_bf16 v[4:7],v[188:191],a[212:215],v[4:7]
v_exp_f32 v220,v50
v_mfma_f32_16x16x32_bf16 v[8:11],v[184:187],a[224:227],v[88:91]
buffer_load_dwordx4 v237,s[44:47],s61 offen offset:2048 nt lds
v_add_f32 v72,v220,v72
v_mfma_f32_16x16x32_bf16 v[8:11],v[188:191],a[228:231],v[8:11]
v_exp_f32 v221,v54
v_mfma_f32_16x16x32_bf16 v[12:15],v[184:187],a[240:243],v[92:95]
v_add_f32 v73,v221,v73
v_mfma_f32_16x16x32_bf16 v[12:15],v[188:191],a[244:247],v[12:15]
v_exp_f32 v222,v51
v_mfma_f32_16x16x32_bf16 v[64:67],v[184:187],a[160:163],v[96:99]
v_add_f32 v72,v222,v72
v_cvt_pk_bf16_f32 v131,v220,v222
v_mfma_f32_16x16x32_bf16 v[64:67],v[188:191],a[164:167],v[64:67]
s_waitcnt lgkmcnt(4)
v_exp_f32 v223,v55
v_mfma_f32_16x16x32_bf16 v[0:3],v[192:195],a[200:203],v[0:3]
ds_read_b64_tr_b16 v[184:185],v230 offset:32768
ds_read_b64_tr_b16 v[186:187],v230 offset:36864
v_mfma_f32_16x16x32_bf16 v[0:3],v[196:199],a[204:207],v[0:3]
v_add_f32 v73,v223,v73
v_cvt_pk_bf16_f32 v135,v221,v223
v_mfma_f32_16x16x32_bf16 v[4:7],v[192:195],a[216:219],v[4:7]
ds_read_b64_tr_b16 v[188:189],v231 offset:32768
ds_read_b64_tr_b16 v[190:191],v231 offset:36864
v_mfma_f32_16x16x32_bf16 v[4:7],v[196:199],a[220:223],v[4:7]
v_exp_f32 v216,v56
v_mfma_f32_16x16x32_bf16 v[8:11],v[192:195],a[232:235],v[8:11]
v_max_f32 v228,v72,v73
v_add_f32 v74,v216,v74
v_mfma_f32_16x16x32_bf16 v[8:11],v[196:199],a[236:239],v[8:11]
v_exp_f32 v217,v60
v_mfma_f32_16x16x32_bf16 v[12:15],v[192:195],a[248:251],v[12:15]
buffer_load_dwordx4 v237,s[44:47],s61 offen offset:3072 nt lds
v_add_f32 v75,v217,v75
v_mfma_f32_16x16x32_bf16 v[12:15],v[196:199],a[252:255],v[12:15]
v_exp_f32 v218,v57
v_mfma_f32_16x16x32_bf16 v[64:67],v[192:195],a[168:171],v[64:67]
s_add_u32 s62,s62,16384
v_add_f32 v74,v218,v74
v_cvt_pk_bf16_f32 v138,v216,v218
v_mfma_f32_16x16x32_bf16 v[64:67],v[196:199],a[172:175],v[64:67]
s_waitcnt lgkmcnt(6)
v_exp_f32 v219,v61
v_mfma_f32_16x16x32_bf16 v[16:19],v[200:203],a[192:195],v[80:83]
s_xor_b32 s61,s62,s75
v_add_f32 v75,v219,v75
v_cvt_pk_bf16_f32 v142,v217,v219
v_mfma_f32_16x16x32_bf16 v[16:19],v[204:207],a[196:199],v[16:19]
s_add_u32 s56,s56,s57
v_exp_f32 v220,v58
v_mfma_f32_16x16x32_bf16 v[20:23],v[200:203],a[208:211],v[84:87]
ds_read_b64_tr_b16 v[192:193],v230 offset:33024
ds_read_b64_tr_b16 v[194:195],v230 offset:37120
v_mfma_f32_16x16x32_bf16 v[20:23],v[204:207],a[212:215],v[20:23]
ds_read_b64_tr_b16 v[196:197],v231 offset:33024
v_add_f32 v74,v220,v74
v_mfma_f32_16x16x32_bf16 v[24:27],v[200:203],a[224:227],v[88:91]
ds_read_b64_tr_b16 v[198:199],v231 offset:37120
v_mfma_f32_16x16x32_bf16 v[24:27],v[204:207],a[228:231],v[24:27]
v_exp_f32 v221,v62
v_mfma_f32_16x16x32_bf16 v[28:31],v[200:203],a[240:243],v[92:95]
v_exp_f32 v222,v59
v_exp_f32 v223,v63
v_mfma_f32_16x16x32_bf16 v[28:31],v[204:207],a[244:247],v[28:31]
v_add_f32 v75,v221,v75
v_add_f32 v74,v222,v74
v_add_f32 v75,v223,v75
v_max3_f32 v228,v228,v74,v75
v_cmp_nge_f32 vcc,0x46800000,v228
v_cvt_pk_bf16_f32 v139,v220,v222
v_mfma_f32_16x16x32_bf16 v[68:71],v[200:203],a[160:163],v[96:99]
ds_read_b64_tr_b16 v[200:201],v230 offset:33280
v_cvt_pk_bf16_f32 v143,v221,v223
v_mfma_f32_16x16x32_bf16 v[68:71],v[204:207],a[164:167],v[68:71]
s_waitcnt lgkmcnt(9)
s_cbranch_vccnz .Lst5
.Lbk5:
ds_read_b64_tr_b16 v[202:203],v230 offset:37376
v_mfma_f32_16x16x32_bf16 v[16:19],v[208:211],a[200:203],v[16:19]
ds_read_b64_tr_b16 v[204:205],v231 offset:33280
)A"
R"A(ds_read_b64_tr_b16 v[206:207],v231 offset:37376
v_mfma_f32_16x16x32_bf16 v[16:19],v[212:215],a[204:207],v[16:19]
v_exp_f32 v216,v0
v_mfma_f32_16x16x32_bf16 v[20:23],v[208:211],a[216:219],v[20:23]
v_add_f32 v104,v72,v104
v_add_f32 v105,v73,v105
v_mfma_f32_16x16x32_bf16 v[20:23],v[212:215],a[220:223],v[20:23]
v_exp_f32 v217,v4
v_mfma_f32_16x16x32_bf16 v[24:27],v[208:211],a[232:235],v[24:27]
v_exp_f32 v218,v1
v_mfma_f32_16x16x32_bf16 v[24:27],v[212:215],a[236:239],v[24:27]
v_add_f32 v72,v216,v218
v_cvt_pk_bf16_f32 v112,v216,v218
v_mfma_f32_16x16x32_bf16 v[28:31],v[208:211],a[248:251],v[28:31]
v_exp_f32 v219,v5
v_mfma_f32_16x16x32_bf16 v[28:31],v[212:215],a[252:255],v[28:31]
v_add_f32 v73,v217,v219
v_cvt_pk_bf16_f32 v116,v217,v219
v_mfma_f32_16x16x32_bf16 v[68:71],v[208:211],a[168:171],v[68:71]
v_exp_f32 v220,v2
v_mfma_f32_16x16x32_bf16 v[68:71],v[212:215],a[172:175],v[68:71]
s_waitcnt lgkmcnt(8)
ds_read_b64_tr_b16 v[208:209],v230 offset:33536
ds_read_b64_tr_b16 v[210:211],v230 offset:37632
v_mfma_f32_16x16x32_bf16 a[0:3],v[184:187],v[128:131],a[0:3]
v_exp_f32 v221,v6
v_mfma_f32_16x16x32_bf16 a[32:35],v[184:187],v[132:135],a[32:35]
ds_read_b64_tr_b16 v[212:213],v231 offset:33536
v_add_f32 v72,v220,v72
v_mfma_f32_16x16x32_bf16 a[64:67],v[184:187],v[136:139],a[64:67]
ds_read_b64_tr_b16 v[214:215],v231 offset:37632
v_add_f32 v73,v221,v73
v_mfma_f32_16x16x32_bf16 a[96:99],v[184:187],v[140:143],a[96:99]
v_exp_f32 v222,v3
v_mfma_f32_16x16x32_bf16 a[4:7],v[188:191],v[128:131],a[4:7]
v_add_f32 v72,v222,v72
v_cvt_pk_bf16_f32 v113,v220,v222
v_mfma_f32_16x16x32_bf16 a[36:39],v[188:191],v[132:135],a[36:39]
v_exp_f32 v223,v7
v_mfma_f32_16x16x32_bf16 a[68:71],v[188:191],v[136:139],a[68:71]
ds_read_b128 v[184:187],v226
v_add_f32 v73,v223,v73
v_mfma_f32_16x16x32_bf16 a[100:103],v[188:191],v[140:143],a[100:103]
s_waitcnt lgkmcnt(9)
v_cvt_pk_bf16_f32 v117,v221,v223
v_add_f32 v106,v74,v106
v_mfma_f32_16x16x32_bf16 a[8:11],v[192:195],v[128:131],a[8:11]
v_exp_f32 v216,v8
v_mfma_f32_16x16x32_bf16 a[40:43],v[192:195],v[132:135],a[40:43]
v_exp_f32 v217,v12
v_mfma_f32_16x16x32_bf16 a[72:75],v[192:195],v[136:139],a[72:75]
ds_read_b128 v[188:191],v226 offset:256
v_add_f32 v107,v75,v107
v_mfma_f32_16x16x32_bf16 a[104:107],v[192:195],v[140:143],a[104:107]
v_exp_f32 v218,v9
v_mfma_f32_16x16x32_bf16 a[12:15],v[196:199],v[128:131],a[12:15]
v_add_f32 v74,v216,v218
v_cvt_pk_bf16_f32 v120,v216,v218
v_mfma_f32_16x16x32_bf16 a[44:47],v[196:199],v[132:135],a[44:47]
v_exp_f32 v219,v13
v_mfma_f32_16x16x32_bf16 a[76:79],v[196:199],v[136:139],a[76:79]
v_add_f32 v75,v217,v219
v_cvt_pk_bf16_f32 v124,v217,v219
v_mfma_f32_16x16x32_bf16 a[108:111],v[196:199],v[140:143],a[108:111]
s_waitcnt lgkmcnt(6)
ds_read_b128 v[192:195],v226 offset:512
ds_read_b128 v[196:199],v226 offset:768
v_mfma_f32_16x16x32_bf16 a[16:19],v[200:203],v[128:131],a[16:19]
v_exp_f32 v220,v10
v_mfma_f32_16x16x32_bf16 a[48:51],v[200:203],v[132:135],a[48:51]
v_exp_f32 v221,v14
v_add_f32 v74,v220,v74
v_mfma_f32_16x16x32_bf16 a[80:83],v[200:203],v[136:139],a[80:83]
v_add_f32 v75,v221,v75
v_exp_f32 v222,v11
v_mfma_f32_16x16x32_bf16 a[112:115],v[200:203],v[140:143],a[112:115]
v_add_f32 v74,v222,v74
v_cvt_pk_bf16_f32 v121,v220,v222
v_mfma_f32_16x16x32_bf16 a[20:23],v[204:207],v[128:131],a[20:23]
v_exp_f32 v223,v15
v_mfma_f32_16x16x32_bf16 a[52:55],v[204:207],v[132:135],a[52:55]
v_add_f32 v75,v223,v75
v_cvt_pk_bf16_f32 v125,v221,v223
v_mfma_f32_16x16x32_bf16 a[84:87],v[204:207],v[136:139],a[84:87]
v_exp_f32 v216,v64
v_mfma_f32_16x16x32_bf16 a[116:119],v[204:207],v[140:143],a[116:119]
s_waitcnt lgkmcnt(4)
ds_read_b128 v[200:203],v226 offset:4096
v_add_f32 v108,v76,v108
v_mfma_f32_16x16x32_bf16 a[24:27],v[208:211],v[128:131],a[24:27]
v_exp_f32 v217,v65
v_mfma_f32_16x16x32_bf16 a[56:59],v[208:211],v[132:135],a[56:59]
v_add_f32 v76,v216,v217
v_cvt_pk_bf16_f32 v144,v216,v217
v_mfma_f32_16x16x32_bf16 a[88:91],v[208:211],v[136:139],a[88:91]
v_exp_f32 v218,v66
v_mfma_f32_16x16x32_bf16 a[120:123],v[208:211],v[140:143],a[120:123]
ds_read_b128 v[204:207],v226 offset:4352
v_add_f32 v76,v218,v76
v_mfma_f32_16x16x32_bf16 a[28:31],v[212:215],v[128:131],a[28:31]
v_exp_f32 v219,v67
v_mfma_f32_16x16x32_bf16 a[60:63],v[212:215],v[132:135],a[60:63]
v_add_f32 v76,v219,v76
v_cvt_pk_bf16_f32 v145,v218,v219
v_mfma_f32_16x16x32_bf16 a[92:95],v[212:215],v[136:139],a[92:95]
v_exp_f32 v220,v16
v_mfma_f32_16x16x32_bf16 a[124:127],v[212:215],v[140:143],a[124:127]
s_waitcnt lgkmcnt(4)
v_exp_f32 v221,v20
v_add_f32 v72,v220,v72
v_mfma_f32_16x16x32_bf16 v[32:35],v[184:187],a[192:195],v[80:83]
ds_read_b128 v[208:211],v226 offset:4608
v_add_f32 v73,v221,v73
v_mfma_f32_16x16x32_bf16 v[32:35],v[188:191],a[196:199],v[32:35]
v_exp_f32 v222,v17
v_mfma_f32_16x16x32_bf16 v[36:39],v[184:187],a[208:211],v[84:87]
v_add_f32 v72,v222,v72
v_cvt_pk_bf16_f32 v114,v220,v222
v_mfma_f32_16x16x32_bf16 v[36:39],v[188:191],a[212:215],v[36:39]
v_exp_f32 v223,v21
v_mfma_f32_16x16x32_bf16 v[40:43],v[184:187],a[224:227],v[88:91]
ds_read_b128 v[212:215],v226 offset:4864
v_add_f32 v73,v223,v73
v_mfma_f32_16x16x32_bf16 v[40:43],v[188:191],a[228:231],v[40:43]
s_cmp_eq_u32 s56,32768
s_cselect_b32 s57,0xffff8000,s58
v_cvt_pk_bf16_f32 v118,v221,v223
v_exp_f32 v216,v18
v_mfma_f32_16x16x32_bf16 v[44:47],v[184:187],a[240:243],v[92:95]
v_exp_f32 v217,v22
v_mfma_f32_16x16x32_bf16 v[44:47],v[188:191],a[244:247],v[44:47]
s_waitcnt lgkmcnt(4)
v_add_f32 v72,v216,v72
v_add_f32 v73,v217,v73
v_mfma_f32_16x16x32_bf16 v[32:35],v[192:195],a[200:203],v[32:35]
v_exp_f32 v218,v19
v_mfma_f32_16x16x32_bf16 v[32:35],v[196:199],a[204:207],v[32:35]
v_add_f32 v72,v218,v72
v_cvt_pk_bf16_f32 v115,v216,v218
v_mfma_f32_16x16x32_bf16 v[36:39],v[192:195],a[216:219],v[36:39]
v_exp_f32 v219,v23
v_mfma_f32_16x16x32_bf16 v[36:39],v[196:199],a[220:223],v[36:39]
ds_read_b64_tr_b16 v[184:185],v233 offset:49152
ds_read_b64_tr_b16 v[186:187],v233 offset:53248
v_add_f32 v73,v219,v73
v_cvt_pk_bf16_f32 v119,v217,v219
v_mfma_f32_16x16x32_bf16 v[40:43],v[192:195],a[232:235],v[40:43]
ds_read_b64_tr_b16 v[188:189],v234 offset:49152
ds_read_b64_tr_b16 v[190:191],v234 offset:53248
v_mfma_f32_16x16x32_bf16 v[40:43],v[196:199],a[236:239],v[40:43]
v_exp_f32 v220,v24
v_max_f32 v228,v72,v73
v_exp_f32 v221,v28
v_mfma_f32_16x16x32_bf16 v[44:47],v[192:195],a[248:251],v[44:47]
v_add_f32 v74,v220,v74
v_add_f32 v75,v221,v75
v_mfma_f32_16x16x32_bf16 v[44:47],v[196:199],a[252:255],v[44:47]
s_waitcnt lgkmcnt(6)
v_exp_f32 v222,v25
v_add_u32 v232,s57,v232
v_mfma_f32_16x16x32_bf16 v[48:51],v[200:203],a[192:195],v[80:83]
v_add_f32 v74,v222,v74
v_cvt_pk_bf16_f32 v122,v220,v222
v_mfma_f32_16x16x32_bf16 v[48:51],v[204:207],a[196:199],v[48:51]
v_exp_f32 v223,v29
v_add_u32 v226,s57,v226
v_mfma_f32_16x16x32_bf16 v[52:55],v[200:203],a[208:211],v[84:87]
v_add_f32 v75,v223,v75
v_cvt_pk_bf16_f32 v126,v221,v223
v_mfma_f32_16x16x32_bf16 v[52:55],v[204:207],a[212:215],v[52:55]
ds_read_b64_tr_b16 v[192:193],v233 offset:49408
ds_read_b64_tr_b16 v[194:195],v233 offset:53504
v_exp_f32 v216,v26
v_mfma_f32_16x16x32_bf16 v[56:59],v[200:203],a[224:227],v[88:91]
ds_read_b64_tr_b16 v[196:197],v234 offset:49408
ds_read_b64_tr_b16 v[198:199],v234 offset:53504
v_mfma_f32_16x16x32_bf16 v[56:59],v[204:207],a[228:231],v[56:59]
v_exp_f32 v217,v30
v_add_f32 v74,v216,v74
v_mfma_f32_16x16x32_bf16 v[60:63],v[200:203],a[240:243],v[92:95]
ds_read_b64_tr_b16 v[200:201],v233 offset:49664
v_add_f32 v75,v217,v75
v_mfma_f32_16x16x32_bf16 v[60:63],v[204:207],a[244:247],v[60:63]
s_waitcnt lgkmcnt(9)
v_exp_f32 v218,v27
v_mfma_f32_16x16x32_bf16 v[48:51],v[208:211],a[200:203],v[48:51]
v_add_f32 v74,v218,v74
v_cvt_pk_bf16_f32 v123,v216,v218
v_mfma_f32_16x16x32_bf16 v[48:51],v[212:215],a[204:207],v[48:51]
v_exp_f32 v219,v31
v_mfma_f32_16x16x32_bf16 v[52:55],v[208:211],a[216:219],v[52:55]
v_add_f32 v75,v219,v75
v_cvt_pk_bf16_f32 v127,v217,v219
v_mfma_f32_16x16x32_bf16 v[52:55],v[212:215],a[220:223],v[52:55]
ds_read_b64_tr_b16 v[202:203],v233 offset:53760
v_exp_f32 v220,v68
v_max3_f32 v228,v228,v74,v75
v_exp_f32 v221,v69
v_mfma_f32_16x16x32_bf16 v[56:59],v[208:211],a[232:235],v[56:59]
v_exp_f32 v222,v70
v_exp_f32 v223,v71
v_mfma_f32_16x16x32_bf16 v[56:59],v[212:215],a[236:239],v[56:59]
ds_read_b64_tr_b16 v[204:205],v234 offset:49664
ds_read_b64_tr_b16 v[206:207],v234 offset:53760
v_add_f32 v76,v220,v76
v_add_f32 v76,v221,v76
v_cvt_pk_bf16_f32 v146,v220,v221
v_add_f32 v76,v222,v76
v_add_f32 v76,v223,v76
v_max_f32 v228,v228,v76
v_cmp_nge_f32 vcc,0x46800000,v228
v_mfma_f32_16x16x32_bf16 v[60:63],v[208:211],a[248:251],v[60:63]
ds_read_b64_tr_b16 v[208:209],v233 offset:49920
v_cvt_pk_bf16_f32 v147,v222,v223
v_mfma_f32_16x16x32_bf16 v[60:63],v[212:215],a[252:255],v[60:63]
s_waitcnt lgkmcnt(9)
s_cbranch_vccnz .Lst6
.Lbk6:
ds_read_b64_tr_b16 v[210:211],v233 offset:54016
v_mfma_f32_16x16x32_bf16 a[0:3],v[184:187],v[112:115],a[0:3]
s_waitcnt vmcnt(8)
s_barrier
ds_read_b64_tr_b16 v[212:213],v234 offset:49920
v_mfma_f32_16x16x32_bf16 a[32:35],v[184:187],v[116:119],a[32:35]
s_add_u32 m0,s63,98304
ds_read_b64_tr_b16 v[214:215],v234 offset:54016
v_add_f32 v104,v72,v104
v_mfma_f32_16x16x32_bf16 a[64:67],v[184:187],v[120:123],a[64:67]
v_exp_f32 v216,v32
v_mfma_f32_16x16x32_bf16 a[96:99],v[184:187],v[124:127],a[96:99]
buffer_load_dwordx4 v235,s[48:51],s61 offen nt lds
v_add_f32 v105,v73,v105
v_mfma_f32_16x16x32_bf16 a[128:131],v[184:187],v[144:147],a[128:131]
v_exp_f32 v217,v36
v_mfma_f32_16x16x32_bf16 a[4:7],v[188:191],v[112:115],a[4:7]
ds_read_b128 v[184:187],v232
buffer_load_dwordx4 v236,s[48:51],s61 offen offset:1024 nt lds
v_mfma_f32_16x16x32_bf16 a[36:39],v[188:191],v[116:119],a[36:39]
v_exp_f32 v218,v33
v_mfma_f32_16x16x32_bf16 a[68:71],v[188:191],v[120:123],a[68:71]
v_add_f32 v72,v216,v218
v_cvt_pk_bf16_f32 v128,v216,v218
v_mfma_f32_16x16x32_bf16 a[100:103],v[188:191],v[124:127],a[100:103]
v_exp_f32 v219,v37
v_mfma_f32_16x16x32_bf16 a[132:135],v[188:191],v[144:147],a[132:135]
s_waitcnt lgkmcnt(9)
v_add_f32 v73,v217,v219
v_cvt_pk_bf16_f32 v132,v217,v219
v_mfma_f32_16x16x32_bf16 a[8:11],v[192:195],v[112:115],a[8:11]
v_exp_f32 v220,v34
v_mfma_f32_16x16x32_bf16 a[40:43],v[192:195],v[116:119],a[40:43]
ds_read_b128 v[188:191],v232 offset:256
v_add_f32 v72,v220,v72
v_mfma_f32_16x16x32_bf16 a[72:75],v[192:195],v[120:123],a[72:75]
v_exp_f32 v221,v38
v_mfma_f32_16x16x32_bf16 a[104:107],v[192:195],v[124:127],a[104:107]
buffer_load_dwordx4 v235,s[48:51],s61 offen offset:2048 nt lds
v_add_f32 v73,v221,v73
v_mfma_f32_16x16x32_bf16 a[136:139],v[192:195],v[144:147],a[136:139]
v_exp_f32 v222,v35
v_mfma_f32_16x16x32_bf16 a[12:15],v[196:199],v[112:115],a[12:15]
v_add_f32 v72,v222,v72
v_cvt_pk_bf16_f32 v129,v220,v222
v_mfma_f32_16x16x32_bf16 a[44:47],v[196:199],v[116:119],a[44:47]
ds_read_b128 v[192:195],v232 offset:512
v_exp_f32 v223,v39
v_mfma_f32_16x16x32_bf16 a[76:79],v[196:199],v[120:123],a[76:79]
v_add_f32 v73,v223,v73
v_cvt_pk_bf16_f32 v133,v221,v223
v_mfma_f32_16x16x32_bf16 a[108:111],v[196:199],v[124:127],a[108:111]
v_exp_f32 v216,v40
v_mfma_f32_16x16x32_bf16 a[140:143],v[196:199],v[144:147],a[140:143]
s_waitcnt lgkmcnt(7)
v_add_f32 v106,v74,v106
v_add_f32 v107,v75,v107
v_mfma_f32_16x16x32_bf16 a[16:19],v[200:203],v[112:115],a[16:19]
v_exp_f32 v217,v44
v_mfma_f32_16x16x32_bf16 a[48:51],v[200:203],v[116:119],a[48:51]
ds_read_b128 v[196:199],v232 offset:768
buffer_load_dwordx4 v236,s[48:51],s61 offen offset:3072 nt lds
v_mfma_f32_16x16x32_bf16 a[80:83],v[200:203],v[120:123],a[80:83]
v_exp_f32 v218,v41
v_mfma_f32_16x16x32_bf16 a[112:115],v[200:203],v[124:127],a[112:115]
v_add_f32 v74,v216,v218
v_cvt_pk_bf16_f32 v136,v216,v218
v_mfma_f32_16x16x32_bf16 a[144:147],v[200:203],v[144:147],a[144:147]
v_exp_f32 v219,v45
v_mfma_f32_16x16x32_bf16 a[20:23],v[204:207],v[112:115],a[20:23]
v_add_f32 v75,v217,v219
v_cvt_pk_bf16_f32 v140,v217,v219
v_mfma_f32_16x16x32_bf16 a[52:55],v[204:207],v[116:119],a[52:55]
s_add_u32 m0,s56,s63
ds_read_b128 v[200:203],v232 offset:4096
v_exp_f32 v220,v42
v_mfma_f32_16x16x32_bf16 a[84:87],v[204:207],v[120:123],a[84:87]
v_add_f32 v74,v220,v74
v_exp_f32 v221,v46
)A"
R"A(v_mfma_f32_16x16x32_bf16 a[116:119],v[204:207],v[124:127],a[116:119]
buffer_load_dwordx4 v235,s[44:47],s61 offen nt lds
v_add_f32 v75,v221,v75
v_mfma_f32_16x16x32_bf16 a[148:151],v[204:207],v[144:147],a[148:151]
s_waitcnt lgkmcnt(5)
v_exp_f32 v222,v43
v_mfma_f32_16x16x32_bf16 a[24:27],v[208:211],v[112:115],a[24:27]
ds_read_b128 v[204:207],v232 offset:4352
v_add_f32 v74,v222,v74
v_mfma_f32_16x16x32_bf16 a[56:59],v[208:211],v[116:119],a[56:59]
v_cvt_pk_bf16_f32 v137,v220,v222
v_exp_f32 v223,v47
v_mfma_f32_16x16x32_bf16 a[88:91],v[208:211],v[120:123],a[88:91]
v_add_f32 v75,v223,v75
v_cvt_pk_bf16_f32 v141,v221,v223
v_mfma_f32_16x16x32_bf16 a[120:123],v[208:211],v[124:127],a[120:123]
v_exp_f32 v216,v48
v_mfma_f32_16x16x32_bf16 a[152:155],v[208:211],v[144:147],a[152:155]
ds_read_b128 v[208:211],v232 offset:4608
v_add_f32 v72,v216,v72
v_mfma_f32_16x16x32_bf16 a[28:31],v[212:215],v[112:115],a[28:31]
v_exp_f32 v217,v52
v_mfma_f32_16x16x32_bf16 a[60:63],v[212:215],v[116:119],a[60:63]
buffer_load_dwordx4 v235,s[44:47],s61 offen offset:1024 nt lds
v_add_f32 v73,v217,v73
v_mfma_f32_16x16x32_bf16 a[92:95],v[212:215],v[120:123],a[92:95]
v_exp_f32 v218,v49
v_mfma_f32_16x16x32_bf16 a[124:127],v[212:215],v[124:127],a[124:127]
v_add_f32 v72,v218,v72
v_cvt_pk_bf16_f32 v130,v216,v218
v_mfma_f32_16x16x32_bf16 a[156:159],v[212:215],v[144:147],a[156:159]
s_waitcnt lgkmcnt(5)
ds_read_b128 v[212:215],v232 offset:4864
v_exp_f32 v219,v53
v_mfma_f32_16x16x32_bf16 v[0:3],v[184:187],a[192:195],v[80:83]
v_add_f32 v73,v219,v73
v_cvt_pk_bf16_f32 v134,v217,v219
v_mfma_f32_16x16x32_bf16 v[0:3],v[188:191],a[196:199],v[0:3]
v_exp_f32 v220,v50
v_mfma_f32_16x16x32_bf16 v[4:7],v[184:187],a[208:211],v[84:87]
v_add_f32 v72,v220,v72
v_mfma_f32_16x16x32_bf16 v[4:7],v[188:191],a[212:215],v[4:7]
v_exp_f32 v221,v54
v_mfma_f32_16x16x32_bf16 v[8:11],v[184:187],a[224:227],v[88:91]
buffer_load_dwordx4 v237,s[44:47],s61 offen offset:2048 nt lds
v_add_f32 v73,v221,v73
v_mfma_f32_16x16x32_bf16 v[8:11],v[188:191],a[228:231],v[8:11]
v_exp_f32 v222,v51
v_mfma_f32_16x16x32_bf16 v[12:15],v[184:187],a[240:243],v[92:95]
v_add_f32 v72,v222,v72
v_cvt_pk_bf16_f32 v131,v220,v222
v_mfma_f32_16x16x32_bf16 v[12:15],v[188:191],a[244:247],v[12:15]
v_exp_f32 v223,v55
v_mfma_f32_16x16x32_bf16 v[64:67],v[184:187],a[160:163],v[96:99]
ds_read_b64_tr_b16 v[184:185],v230 offset:49152
ds_read_b64_tr_b16 v[186:187],v230 offset:53248
v_mfma_f32_16x16x32_bf16 v[64:67],v[188:191],a[164:167],v[64:67]
s_waitcnt lgkmcnt(6)
v_add_f32 v73,v223,v73
v_cvt_pk_bf16_f32 v135,v221,v223
v_mfma_f32_16x16x32_bf16 v[0:3],v[192:195],a[200:203],v[0:3]
ds_read_b64_tr_b16 v[188:189],v231 offset:49152
ds_read_b64_tr_b16 v[190:191],v231 offset:53248
v_mfma_f32_16x16x32_bf16 v[0:3],v[196:199],a[204:207],v[0:3]
v_exp_f32 v216,v56
v_mfma_f32_16x16x32_bf16 v[4:7],v[192:195],a[216:219],v[4:7]
v_max_f32 v228,v72,v73
v_add_f32 v74,v216,v74
v_mfma_f32_16x16x32_bf16 v[4:7],v[196:199],a[220:223],v[4:7]
v_exp_f32 v217,v60
v_mfma_f32_16x16x32_bf16 v[8:11],v[192:195],a[232:235],v[8:11]
buffer_load_dwordx4 v237,s[44:47],s61 offen offset:3072 nt lds
v_add_f32 v75,v217,v75
v_mfma_f32_16x16x32_bf16 v[8:11],v[196:199],a[236:239],v[8:11]
v_exp_f32 v218,v57
v_mfma_f32_16x16x32_bf16 v[12:15],v[192:195],a[248:251],v[12:15]
v_add_f32 v74,v218,v74
v_cvt_pk_bf16_f32 v138,v216,v218
v_mfma_f32_16x16x32_bf16 v[12:15],v[196:199],a[252:255],v[12:15]
v_exp_f32 v219,v61
v_mfma_f32_16x16x32_bf16 v[64:67],v[192:195],a[168:171],v[64:67]
s_add_u32 s62,s62,16384
v_add_f32 v75,v219,v75
v_cvt_pk_bf16_f32 v142,v217,v219
v_mfma_f32_16x16x32_bf16 v[64:67],v[196:199],a[172:175],v[64:67]
s_waitcnt lgkmcnt(6)
v_exp_f32 v220,v58
v_mfma_f32_16x16x32_bf16 v[16:19],v[200:203],a[192:195],v[80:83]
s_xor_b32 s61,s62,s75
ds_read_b64_tr_b16 v[192:193],v230 offset:49408
ds_read_b64_tr_b16 v[194:195],v230 offset:53504
v_mfma_f32_16x16x32_bf16 v[16:19],v[204:207],a[196:199],v[16:19]
s_add_u32 s56,s56,s57
ds_read_b64_tr_b16 v[196:197],v231 offset:49408
v_add_f32 v74,v220,v74
v_mfma_f32_16x16x32_bf16 v[20:23],v[200:203],a[208:211],v[84:87]
ds_read_b64_tr_b16 v[198:199],v231 offset:53504
buffer_store_dword v225,v224,s[40:43],s68 offen sc1
v_mfma_f32_16x16x32_bf16 v[20:23],v[204:207],a[212:215],v[20:23]
v_exp_f32 v221,v62
v_mfma_f32_16x16x32_bf16 v[24:27],v[200:203],a[224:227],v[88:91]
v_add_f32 v75,v221,v75
v_mfma_f32_16x16x32_bf16 v[24:27],v[204:207],a[228:231],v[24:27]
v_exp_f32 v222,v59
v_mfma_f32_16x16x32_bf16 v[28:31],v[200:203],a[240:243],v[92:95]
v_exp_f32 v223,v63
v_mfma_f32_16x16x32_bf16 v[28:31],v[204:207],a[244:247],v[28:31]
v_add_f32 v74,v222,v74
v_add_f32 v75,v223,v75
v_max3_f32 v228,v228,v74,v75
v_cmp_nge_f32 vcc,0x46800000,v228
v_cvt_pk_bf16_f32 v139,v220,v222
v_mfma_f32_16x16x32_bf16 v[68:71],v[200:203],a[160:163],v[96:99]
ds_read_b64_tr_b16 v[200:201],v230 offset:49664
v_cvt_pk_bf16_f32 v143,v221,v223
v_mfma_f32_16x16x32_bf16 v[68:71],v[204:207],a[164:167],v[68:71]
s_waitcnt lgkmcnt(9)
s_cbranch_vccnz .Lst7
.Lbk7:
ds_read_b64_tr_b16 v[202:203],v230 offset:53760
v_mfma_f32_16x16x32_bf16 v[16:19],v[208:211],a[200:203],v[16:19]
ds_read_b64_tr_b16 v[204:205],v231 offset:49664
ds_read_b64_tr_b16 v[206:207],v231 offset:53760
v_mfma_f32_16x16x32_bf16 v[16:19],v[212:215],a[204:207],v[16:19]
v_exp_f32 v216,v0
v_mfma_f32_16x16x32_bf16 v[20:23],v[208:211],a[216:219],v[20:23]
v_add_f32 v104,v72,v104
v_add_f32 v105,v73,v105
v_mfma_f32_16x16x32_bf16 v[20:23],v[212:215],a[220:223],v[20:23]
v_exp_f32 v217,v4
v_mfma_f32_16x16x32_bf16 v[24:27],v[208:211],a[232:235],v[24:27]
v_exp_f32 v218,v1
v_mfma_f32_16x16x32_bf16 v[24:27],v[212:215],a[236:239],v[24:27]
v_add_f32 v72,v216,v218
v_cvt_pk_bf16_f32 v112,v216,v218
v_mfma_f32_16x16x32_bf16 v[28:31],v[208:211],a[248:251],v[28:31]
v_exp_f32 v219,v5
v_mfma_f32_16x16x32_bf16 v[28:31],v[212:215],a[252:255],v[28:31]
v_add_f32 v73,v217,v219
v_cvt_pk_bf16_f32 v116,v217,v219
v_mfma_f32_16x16x32_bf16 v[68:71],v[208:211],a[168:171],v[68:71]
v_exp_f32 v220,v2
v_mfma_f32_16x16x32_bf16 v[68:71],v[212:215],a[172:175],v[68:71]
s_waitcnt lgkmcnt(8)
ds_read_b64_tr_b16 v[208:209],v230 offset:49920
ds_read_b64_tr_b16 v[210:211],v230 offset:54016
v_mfma_f32_16x16x32_bf16 a[0:3],v[184:187],v[128:131],a[0:3]
v_exp_f32 v221,v6
v_mfma_f32_16x16x32_bf16 a[32:35],v[184:187],v[132:135],a[32:35]
ds_read_b64_tr_b16 v[212:213],v231 offset:49920
v_add_f32 v72,v220,v72
v_mfma_f32_16x16x32_bf16 a[64:67],v[184:187],v[136:139],a[64:67]
ds_read_b64_tr_b16 v[214:215],v231 offset:54016
v_add_f32 v73,v221,v73
v_mfma_f32_16x16x32_bf16 a[96:99],v[184:187],v[140:143],a[96:99]
v_exp_f32 v222,v3
v_mfma_f32_16x16x32_bf16 a[4:7],v[188:191],v[128:131],a[4:7]
v_add_f32 v72,v222,v72
v_cvt_pk_bf16_f32 v113,v220,v222
v_mfma_f32_16x16x32_bf16 a[36:39],v[188:191],v[132:135],a[36:39]
v_exp_f32 v223,v7
v_mfma_f32_16x16x32_bf16 a[68:71],v[188:191],v[136:139],a[68:71]
ds_read_b128 v[184:187],v226
v_add_f32 v73,v223,v73
v_mfma_f32_16x16x32_bf16 a[100:103],v[188:191],v[140:143],a[100:103]
s_waitcnt lgkmcnt(9)
v_cvt_pk_bf16_f32 v117,v221,v223
v_add_f32 v106,v74,v106
v_mfma_f32_16x16x32_bf16 a[8:11],v[192:195],v[128:131],a[8:11]
v_exp_f32 v216,v8
v_mfma_f32_16x16x32_bf16 a[40:43],v[192:195],v[132:135],a[40:43]
v_exp_f32 v217,v12
v_mfma_f32_16x16x32_bf16 a[72:75],v[192:195],v[136:139],a[72:75]
ds_read_b128 v[188:191],v226 offset:256
v_add_f32 v107,v75,v107
v_mfma_f32_16x16x32_bf16 a[104:107],v[192:195],v[140:143],a[104:107]
v_exp_f32 v218,v9
v_mfma_f32_16x16x32_bf16 a[12:15],v[196:199],v[128:131],a[12:15]
v_add_f32 v74,v216,v218
v_cvt_pk_bf16_f32 v120,v216,v218
v_mfma_f32_16x16x32_bf16 a[44:47],v[196:199],v[132:135],a[44:47]
v_exp_f32 v219,v13
v_mfma_f32_16x16x32_bf16 a[76:79],v[196:199],v[136:139],a[76:79]
v_add_f32 v75,v217,v219
v_cvt_pk_bf16_f32 v124,v217,v219
v_mfma_f32_16x16x32_bf16 a[108:111],v[196:199],v[140:143],a[108:111]
s_waitcnt lgkmcnt(6)
ds_read_b128 v[192:195],v226 offset:512
ds_read_b128 v[196:199],v226 offset:768
v_mfma_f32_16x16x32_bf16 a[16:19],v[200:203],v[128:131],a[16:19]
v_exp_f32 v220,v10
v_mfma_f32_16x16x32_bf16 a[48:51],v[200:203],v[132:135],a[48:51]
v_exp_f32 v221,v14
v_add_f32 v74,v220,v74
v_mfma_f32_16x16x32_bf16 a[80:83],v[200:203],v[136:139],a[80:83]
v_add_f32 v75,v221,v75
v_exp_f32 v222,v11
v_mfma_f32_16x16x32_bf16 a[112:115],v[200:203],v[140:143],a[112:115]
v_add_f32 v74,v222,v74
v_cvt_pk_bf16_f32 v121,v220,v222
v_mfma_f32_16x16x32_bf16 a[20:23],v[204:207],v[128:131],a[20:23]
v_exp_f32 v223,v15
v_mfma_f32_16x16x32_bf16 a[52:55],v[204:207],v[132:135],a[52:55]
v_add_f32 v75,v223,v75
v_cvt_pk_bf16_f32 v125,v221,v223
v_mfma_f32_16x16x32_bf16 a[84:87],v[204:207],v[136:139],a[84:87]
v_exp_f32 v216,v64
v_mfma_f32_16x16x32_bf16 a[116:119],v[204:207],v[140:143],a[116:119]
s_waitcnt lgkmcnt(4)
ds_read_b128 v[200:203],v226 offset:4096
v_add_f32 v108,v76,v108
v_mfma_f32_16x16x32_bf16 a[24:27],v[208:211],v[128:131],a[24:27]
v_exp_f32 v217,v65
v_mfma_f32_16x16x32_bf16 a[56:59],v[208:211],v[132:135],a[56:59]
v_add_f32 v76,v216,v217
v_cvt_pk_bf16_f32 v144,v216,v217
v_mfma_f32_16x16x32_bf16 a[88:91],v[208:211],v[136:139],a[88:91]
v_exp_f32 v218,v66
v_mfma_f32_16x16x32_bf16 a[120:123],v[208:211],v[140:143],a[120:123]
ds_read_b128 v[204:207],v226 offset:4352
v_add_f32 v76,v218,v76
v_mfma_f32_16x16x32_bf16 a[28:31],v[212:215],v[128:131],a[28:31]
v_exp_f32 v219,v67
v_mfma_f32_16x16x32_bf16 a[60:63],v[212:215],v[132:135],a[60:63]
v_add_f32 v76,v219,v76
v_cvt_pk_bf16_f32 v145,v218,v219
v_mfma_f32_16x16x32_bf16 a[92:95],v[212:215],v[136:139],a[92:95]
v_exp_f32 v220,v16
v_mfma_f32_16x16x32_bf16 a[124:127],v[212:215],v[140:143],a[124:127]
s_waitcnt lgkmcnt(4)
v_exp_f32 v221,v20
v_add_f32 v72,v220,v72
v_mfma_f32_16x16x32_bf16 v[32:35],v[184:187],a[192:195],v[80:83]
ds_read_b128 v[208:211],v226 offset:4608
v_add_f32 v73,v221,v73
v_mfma_f32_16x16x32_bf16 v[32:35],v[188:191],a[196:199],v[32:35]
v_exp_f32 v222,v17
v_mfma_f32_16x16x32_bf16 v[36:39],v[184:187],a[208:211],v[84:87]
v_add_f32 v72,v222,v72
v_cvt_pk_bf16_f32 v114,v220,v222
v_mfma_f32_16x16x32_bf16 v[36:39],v[188:191],a[212:215],v[36:39]
v_exp_f32 v223,v21
v_mfma_f32_16x16x32_bf16 v[40:43],v[184:187],a[224:227],v[88:91]
ds_read_b128 v[212:215],v226 offset:4864
v_add_f32 v73,v223,v73
v_mfma_f32_16x16x32_bf16 v[40:43],v[188:191],a[228:231],v[40:43]
s_cmp_eq_u32 s56,32768
s_cselect_b32 s57,0xffff8000,s58
v_cvt_pk_bf16_f32 v118,v221,v223
v_exp_f32 v216,v18
v_mfma_f32_16x16x32_bf16 v[44:47],v[184:187],a[240:243],v[92:95]
v_exp_f32 v217,v22
v_mfma_f32_16x16x32_bf16 v[44:47],v[188:191],a[244:247],v[44:47]
s_waitcnt lgkmcnt(4)
v_add_f32 v72,v216,v72
v_add_f32 v73,v217,v73
v_mfma_f32_16x16x32_bf16 v[32:35],v[192:195],a[200:203],v[32:35]
v_exp_f32 v218,v19
v_mfma_f32_16x16x32_bf16 v[32:35],v[196:199],a[204:207],v[32:35]
v_add_f32 v72,v218,v72
v_cvt_pk_bf16_f32 v115,v216,v218
v_mfma_f32_16x16x32_bf16 v[36:39],v[192:195],a[216:219],v[36:39]
v_exp_f32 v219,v23
v_mfma_f32_16x16x32_bf16 v[36:39],v[196:199],a[220:223],v[36:39]
ds_read_b64_tr_b16 v[184:185],v233
ds_read_b64_tr_b16 v[186:187],v233 offset:4096
v_add_f32 v73,v219,v73
v_cvt_pk_bf16_f32 v119,v217,v219
v_mfma_f32_16x16x32_bf16 v[40:43],v[192:195],a[232:235],v[40:43]
ds_read_b64_tr_b16 v[188:189],v234
ds_read_b64_tr_b16 v[190:191],v234 offset:4096
v_mfma_f32_16x16x32_bf16 v[40:43],v[196:199],a[236:239],v[40:43]
v_exp_f32 v220,v24
v_max_f32 v228,v72,v73
v_exp_f32 v221,v28
v_mfma_f32_16x16x32_bf16 v[44:47],v[192:195],a[248:251],v[44:47]
v_add_f32 v74,v220,v74
v_add_f32 v75,v221,v75
v_mfma_f32_16x16x32_bf16 v[44:47],v[196:199],a[252:255],v[44:47]
s_waitcnt lgkmcnt(6)
v_exp_f32 v222,v25
v_add_u32 v232,s57,v232
v_mfma_f32_16x16x32_bf16 v[48:51],v[200:203],a[192:195],v[80:83]
v_add_f32 v74,v222,v74
v_cvt_pk_bf16_f32 v122,v220,v222
v_mfma_f32_16x16x32_bf16 v[48:51],v[204:207],a[196:199],v[48:51]
v_exp_f32 v223,v29
v_add_u32 v226,s57,v226
v_mfma_f32_16x16x32_bf16 v[52:55],v[200:203],a[208:211],v[84:87]
v_add_f32 v75,v223,v75
v_cvt_pk_bf16_f32 v126,v221,v223
v_mfma_f32_16x16x32_bf16 v[52:55],v[204:207],a[212:215],v[52:55]
ds_read_b64_tr_b16 v[192:193],v233 offset:256
)A"
R"A(ds_read_b64_tr_b16 v[194:195],v233 offset:4352
v_exp_f32 v216,v26
v_mfma_f32_16x16x32_bf16 v[56:59],v[200:203],a[224:227],v[88:91]
ds_read_b64_tr_b16 v[196:197],v234 offset:256
ds_read_b64_tr_b16 v[198:199],v234 offset:4352
v_mfma_f32_16x16x32_bf16 v[56:59],v[204:207],a[228:231],v[56:59]
v_exp_f32 v217,v30
v_add_f32 v74,v216,v74
v_mfma_f32_16x16x32_bf16 v[60:63],v[200:203],a[240:243],v[92:95]
ds_read_b64_tr_b16 v[200:201],v233 offset:512
v_add_f32 v75,v217,v75
v_mfma_f32_16x16x32_bf16 v[60:63],v[204:207],a[244:247],v[60:63]
s_waitcnt lgkmcnt(9)
v_exp_f32 v218,v27
v_mfma_f32_16x16x32_bf16 v[48:51],v[208:211],a[200:203],v[48:51]
v_add_f32 v74,v218,v74
v_cvt_pk_bf16_f32 v123,v216,v218
v_mfma_f32_16x16x32_bf16 v[48:51],v[212:215],a[204:207],v[48:51]
v_exp_f32 v219,v31
v_mfma_f32_16x16x32_bf16 v[52:55],v[208:211],a[216:219],v[52:55]
v_add_f32 v75,v219,v75
v_cvt_pk_bf16_f32 v127,v217,v219
v_mfma_f32_16x16x32_bf16 v[52:55],v[212:215],a[220:223],v[52:55]
ds_read_b64_tr_b16 v[202:203],v233 offset:4608
v_exp_f32 v220,v68
v_max3_f32 v228,v228,v74,v75
v_exp_f32 v221,v69
v_mfma_f32_16x16x32_bf16 v[56:59],v[208:211],a[232:235],v[56:59]
v_exp_f32 v222,v70
v_exp_f32 v223,v71
v_mfma_f32_16x16x32_bf16 v[56:59],v[212:215],a[236:239],v[56:59]
ds_read_b64_tr_b16 v[204:205],v234 offset:512
ds_read_b64_tr_b16 v[206:207],v234 offset:4608
v_add_f32 v76,v220,v76
v_add_f32 v76,v221,v76
v_cvt_pk_bf16_f32 v146,v220,v221
v_add_f32 v76,v222,v76
v_add_f32 v76,v223,v76
v_max_f32 v228,v228,v76
v_cmp_nge_f32 vcc,0x46800000,v228
v_mfma_f32_16x16x32_bf16 v[60:63],v[208:211],a[248:251],v[60:63]
ds_read_b64_tr_b16 v[208:209],v233 offset:768
v_cvt_pk_bf16_f32 v147,v222,v223
v_mfma_f32_16x16x32_bf16 v[60:63],v[212:215],a[252:255],v[60:63]
s_waitcnt lgkmcnt(9)
s_cbranch_vccnz .Lst8
.Lbk8:
ds_read_b64_tr_b16 v[210:211],v233 offset:4864
v_mfma_f32_16x16x32_bf16 a[0:3],v[184:187],v[112:115],a[0:3]
s_waitcnt vmcnt(9)
s_barrier
ds_read_b64_tr_b16 v[212:213],v234 offset:768
v_mfma_f32_16x16x32_bf16 a[32:35],v[184:187],v[116:119],a[32:35]
s_add_u32 m0,s63,114688
ds_read_b64_tr_b16 v[214:215],v234 offset:4864
v_add_f32 v104,v72,v104
v_mfma_f32_16x16x32_bf16 a[64:67],v[184:187],v[120:123],a[64:67]
v_exp_f32 v216,v32
v_mfma_f32_16x16x32_bf16 a[96:99],v[184:187],v[124:127],a[96:99]
buffer_load_dwordx4 v235,s[48:51],s61 offen nt lds
v_add_f32 v105,v73,v105
v_mfma_f32_16x16x32_bf16 a[128:131],v[184:187],v[144:147],a[128:131]
v_exp_f32 v217,v36
v_mfma_f32_16x16x32_bf16 a[4:7],v[188:191],v[112:115],a[4:7]
ds_read_b128 v[184:187],v232
buffer_load_dwordx4 v236,s[48:51],s61 offen offset:1024 nt lds
v_mfma_f32_16x16x32_bf16 a[36:39],v[188:191],v[116:119],a[36:39]
v_exp_f32 v218,v33
v_mfma_f32_16x16x32_bf16 a[68:71],v[188:191],v[120:123],a[68:71]
v_add_f32 v72,v216,v218
v_cvt_pk_bf16_f32 v128,v216,v218
v_mfma_f32_16x16x32_bf16 a[100:103],v[188:191],v[124:127],a[100:103]
v_exp_f32 v219,v37
v_mfma_f32_16x16x32_bf16 a[132:135],v[188:191],v[144:147],a[132:135]
s_waitcnt lgkmcnt(9)
v_add_f32 v73,v217,v219
v_cvt_pk_bf16_f32 v132,v217,v219
v_mfma_f32_16x16x32_bf16 a[8:11],v[192:195],v[112:115],a[8:11]
v_exp_f32 v220,v34
v_mfma_f32_16x16x32_bf16 a[40:43],v[192:195],v[116:119],a[40:43]
ds_read_b128 v[188:191],v232 offset:256
v_add_f32 v72,v220,v72
v_mfma_f32_16x16x32_bf16 a[72:75],v[192:195],v[120:123],a[72:75]
v_exp_f32 v221,v38
v_mfma_f32_16x16x32_bf16 a[104:107],v[192:195],v[124:127],a[104:107]
buffer_load_dwordx4 v235,s[48:51],s61 offen offset:2048 nt lds
v_add_f32 v73,v221,v73
v_mfma_f32_16x16x32_bf16 a[136:139],v[192:195],v[144:147],a[136:139]
v_exp_f32 v222,v35
v_mfma_f32_16x16x32_bf16 a[12:15],v[196:199],v[112:115],a[12:15]
v_add_f32 v72,v222,v72
v_cvt_pk_bf16_f32 v129,v220,v222
v_mfma_f32_16x16x32_bf16 a[44:47],v[196:199],v[116:119],a[44:47]
ds_read_b128 v[192:195],v232 offset:512
v_exp_f32 v223,v39
v_mfma_f32_16x16x32_bf16 a[76:79],v[196:199],v[120:123],a[76:79]
v_add_f32 v73,v223,v73
v_cvt_pk_bf16_f32 v133,v221,v223
v_mfma_f32_16x16x32_bf16 a[108:111],v[196:199],v[124:127],a[108:111]
v_exp_f32 v216,v40
v_mfma_f32_16x16x32_bf16 a[140:143],v[196:199],v[144:147],a[140:143]
s_waitcnt lgkmcnt(7)
v_add_f32 v106,v74,v106
v_add_f32 v107,v75,v107
v_mfma_f32_16x16x32_bf16 a[16:19],v[200:203],v[112:115],a[16:19]
v_exp_f32 v217,v44
v_mfma_f32_16x16x32_bf16 a[48:51],v[200:203],v[116:119],a[48:51]
ds_read_b128 v[196:199],v232 offset:768
buffer_load_dwordx4 v236,s[48:51],s61 offen offset:3072 nt lds
v_mfma_f32_16x16x32_bf16 a[80:83],v[200:203],v[120:123],a[80:83]
v_exp_f32 v218,v41
v_mfma_f32_16x16x32_bf16 a[112:115],v[200:203],v[124:127],a[112:115]
v_add_f32 v74,v216,v218
v_cvt_pk_bf16_f32 v136,v216,v218
v_mfma_f32_16x16x32_bf16 a[144:147],v[200:203],v[144:147],a[144:147]
v_exp_f32 v219,v45
v_mfma_f32_16x16x32_bf16 a[20:23],v[204:207],v[112:115],a[20:23]
v_add_f32 v75,v217,v219
v_cvt_pk_bf16_f32 v140,v217,v219
v_mfma_f32_16x16x32_bf16 a[52:55],v[204:207],v[116:119],a[52:55]
s_add_u32 m0,s56,s63
ds_read_b128 v[200:203],v232 offset:4096
v_exp_f32 v220,v42
v_mfma_f32_16x16x32_bf16 a[84:87],v[204:207],v[120:123],a[84:87]
v_add_f32 v74,v220,v74
v_exp_f32 v221,v46
v_mfma_f32_16x16x32_bf16 a[116:119],v[204:207],v[124:127],a[116:119]
buffer_load_dwordx4 v235,s[44:47],s61 offen nt lds
v_add_f32 v75,v221,v75
v_mfma_f32_16x16x32_bf16 a[148:151],v[204:207],v[144:147],a[148:151]
s_waitcnt lgkmcnt(5)
v_exp_f32 v222,v43
v_mfma_f32_16x16x32_bf16 a[24:27],v[208:211],v[112:115],a[24:27]
ds_read_b128 v[204:207],v232 offset:4352
v_add_f32 v74,v222,v74
v_mfma_f32_16x16x32_bf16 a[56:59],v[208:211],v[116:119],a[56:59]
v_cvt_pk_bf16_f32 v137,v220,v222
v_exp_f32 v223,v47
v_mfma_f32_16x16x32_bf16 a[88:91],v[208:211],v[120:123],a[88:91]
v_add_f32 v75,v223,v75
v_cvt_pk_bf16_f32 v141,v221,v223
v_mfma_f32_16x16x32_bf16 a[120:123],v[208:211],v[124:127],a[120:123]
v_exp_f32 v216,v48
v_mfma_f32_16x16x32_bf16 a[152:155],v[208:211],v[144:147],a[152:155]
ds_read_b128 v[208:211],v232 offset:4608
v_add_f32 v72,v216,v72
v_mfma_f32_16x16x32_bf16 a[28:31],v[212:215],v[112:115],a[28:31]
v_exp_f32 v217,v52
v_mfma_f32_16x16x32_bf16 a[60:63],v[212:215],v[116:119],a[60:63]
buffer_load_dwordx4 v235,s[44:47],s61 offen offset:1024 nt lds
v_add_f32 v73,v217,v73
v_mfma_f32_16x16x32_bf16 a[92:95],v[212:215],v[120:123],a[92:95]
v_exp_f32 v218,v49
v_mfma_f32_16x16x32_bf16 a[124:127],v[212:215],v[124:127],a[124:127]
v_add_f32 v72,v218,v72
v_cvt_pk_bf16_f32 v130,v216,v218
v_mfma_f32_16x16x32_bf16 a[156:159],v[212:215],v[144:147],a[156:159]
s_waitcnt lgkmcnt(5)
ds_read_b128 v[212:215],v232 offset:4864
v_exp_f32 v219,v53
v_mfma_f32_16x16x32_bf16 v[0:3],v[184:187],a[192:195],v[80:83]
v_add_f32 v73,v219,v73
v_cvt_pk_bf16_f32 v134,v217,v219
v_mfma_f32_16x16x32_bf16 v[0:3],v[188:191],a[196:199],v[0:3]
v_exp_f32 v220,v50
v_mfma_f32_16x16x32_bf16 v[4:7],v[184:187],a[208:211],v[84:87]
v_add_f32 v72,v220,v72
v_mfma_f32_16x16x32_bf16 v[4:7],v[188:191],a[212:215],v[4:7]
v_exp_f32 v221,v54
v_mfma_f32_16x16x32_bf16 v[8:11],v[184:187],a[224:227],v[88:91]
buffer_load_dwordx4 v237,s[44:47],s61 offen offset:2048 nt lds
v_add_f32 v73,v221,v73
v_mfma_f32_16x16x32_bf16 v[8:11],v[188:191],a[228:231],v[8:11]
v_exp_f32 v222,v51
v_mfma_f32_16x16x32_bf16 v[12:15],v[184:187],a[240:243],v[92:95]
v_add_f32 v72,v222,v72
v_cvt_pk_bf16_f32 v131,v220,v222
v_mfma_f32_16x16x32_bf16 v[12:15],v[188:191],a[244:247],v[12:15]
v_exp_f32 v223,v55
v_mfma_f32_16x16x32_bf16 v[64:67],v[184:187],a[160:163],v[96:99]
ds_read_b64_tr_b16 v[184:185],v230
ds_read_b64_tr_b16 v[186:187],v230 offset:4096
v_mfma_f32_16x16x32_bf16 v[64:67],v[188:191],a[164:167],v[64:67]
s_waitcnt lgkmcnt(6)
v_add_f32 v73,v223,v73
v_cvt_pk_bf16_f32 v135,v221,v223
v_mfma_f32_16x16x32_bf16 v[0:3],v[192:195],a[200:203],v[0:3]
ds_read_b64_tr_b16 v[188:189],v231
ds_read_b64_tr_b16 v[190:191],v231 offset:4096
v_mfma_f32_16x16x32_bf16 v[0:3],v[196:199],a[204:207],v[0:3]
v_exp_f32 v216,v56
v_mfma_f32_16x16x32_bf16 v[4:7],v[192:195],a[216:219],v[4:7]
v_max_f32 v228,v72,v73
v_add_f32 v74,v216,v74
v_mfma_f32_16x16x32_bf16 v[4:7],v[196:199],a[220:223],v[4:7]
v_exp_f32 v217,v60
v_mfma_f32_16x16x32_bf16 v[8:11],v[192:195],a[232:235],v[8:11]
buffer_load_dwordx4 v237,s[44:47],s61 offen offset:3072 nt lds
v_add_f32 v75,v217,v75
v_mfma_f32_16x16x32_bf16 v[8:11],v[196:199],a[236:239],v[8:11]
v_exp_f32 v218,v57
v_mfma_f32_16x16x32_bf16 v[12:15],v[192:195],a[248:251],v[12:15]
v_add_f32 v74,v218,v74
v_cvt_pk_bf16_f32 v138,v216,v218
v_mfma_f32_16x16x32_bf16 v[12:15],v[196:199],a[252:255],v[12:15]
v_exp_f32 v219,v61
v_mfma_f32_16x16x32_bf16 v[64:67],v[192:195],a[168:171],v[64:67]
s_add_u32 s62,s62,16384
v_add_f32 v75,v219,v75
v_cvt_pk_bf16_f32 v142,v217,v219
v_mfma_f32_16x16x32_bf16 v[64:67],v[196:199],a[172:175],v[64:67]
s_waitcnt lgkmcnt(6)
v_exp_f32 v220,v58
v_mfma_f32_16x16x32_bf16 v[16:19],v[200:203],a[192:195],v[80:83]
s_xor_b32 s61,s62,s75
ds_read_b64_tr_b16 v[192:193],v230 offset:256
ds_read_b64_tr_b16 v[194:195],v230 offset:4352
v_mfma_f32_16x16x32_bf16 v[16:19],v[204:207],a[196:199],v[16:19]
s_add_u32 s56,s56,s57
ds_read_b64_tr_b16 v[196:197],v231 offset:256
v_add_f32 v74,v220,v74
v_mfma_f32_16x16x32_bf16 v[20:23],v[200:203],a[208:211],v[84:87]
ds_read_b64_tr_b16 v[198:199],v231 offset:4352
v_mfma_f32_16x16x32_bf16 v[20:23],v[204:207],a[212:215],v[20:23]
v_exp_f32 v221,v62
v_mfma_f32_16x16x32_bf16 v[24:27],v[200:203],a[224:227],v[88:91]
v_add_f32 v75,v221,v75
v_mfma_f32_16x16x32_bf16 v[24:27],v[204:207],a[228:231],v[24:27]
v_exp_f32 v222,v59
v_mfma_f32_16x16x32_bf16 v[28:31],v[200:203],a[240:243],v[92:95]
v_exp_f32 v223,v63
v_mfma_f32_16x16x32_bf16 v[28:31],v[204:207],a[244:247],v[28:31]
v_add_f32 v74,v222,v74
v_add_f32 v75,v223,v75
v_max3_f32 v228,v228,v74,v75
v_cmp_nge_f32 vcc,0x46800000,v228
v_cvt_pk_bf16_f32 v139,v220,v222
v_mfma_f32_16x16x32_bf16 v[68:71],v[200:203],a[160:163],v[96:99]
ds_read_b64_tr_b16 v[200:201],v230 offset:512
v_cvt_pk_bf16_f32 v143,v221,v223
v_mfma_f32_16x16x32_bf16 v[68:71],v[204:207],a[164:167],v[68:71]
s_waitcnt lgkmcnt(9)
s_cbranch_vccnz .Lst9
.Lbk9:
ds_read_b64_tr_b16 v[202:203],v230 offset:4608
v_mfma_f32_16x16x32_bf16 v[16:19],v[208:211],a[200:203],v[16:19]
ds_read_b64_tr_b16 v[204:205],v231 offset:512
ds_read_b64_tr_b16 v[206:207],v231 offset:4608
v_mfma_f32_16x16x32_bf16 v[16:19],v[212:215],a[204:207],v[16:19]
v_exp_f32 v216,v0
v_mfma_f32_16x16x32_bf16 v[20:23],v[208:211],a[216:219],v[20:23]
v_add_f32 v104,v72,v104
v_add_f32 v105,v73,v105
v_mfma_f32_16x16x32_bf16 v[20:23],v[212:215],a[220:223],v[20:23]
v_exp_f32 v217,v4
v_mfma_f32_16x16x32_bf16 v[24:27],v[208:211],a[232:235],v[24:27]
v_exp_f32 v218,v1
v_mfma_f32_16x16x32_bf16 v[24:27],v[212:215],a[236:239],v[24:27]
v_add_f32 v72,v216,v218
v_cvt_pk_bf16_f32 v112,v216,v218
v_mfma_f32_16x16x32_bf16 v[28:31],v[208:211],a[248:251],v[28:31]
v_exp_f32 v219,v5
v_mfma_f32_16x16x32_bf16 v[28:31],v[212:215],a[252:255],v[28:31]
v_add_f32 v73,v217,v219
v_cvt_pk_bf16_f32 v116,v217,v219
v_mfma_f32_16x16x32_bf16 v[68:71],v[208:211],a[168:171],v[68:71]
v_exp_f32 v220,v2
v_mfma_f32_16x16x32_bf16 v[68:71],v[212:215],a[172:175],v[68:71]
s_waitcnt lgkmcnt(8)
ds_read_b64_tr_b16 v[208:209],v230 offset:768
ds_read_b64_tr_b16 v[210:211],v230 offset:4864
v_mfma_f32_16x16x32_bf16 a[0:3],v[184:187],v[128:131],a[0:3]
v_exp_f32 v221,v6
v_mfma_f32_16x16x32_bf16 a[32:35],v[184:187],v[132:135],a[32:35]
ds_read_b64_tr_b16 v[212:213],v231 offset:768
v_add_f32 v72,v220,v72
v_mfma_f32_16x16x32_bf16 a[64:67],v[184:187],v[136:139],a[64:67]
ds_read_b64_tr_b16 v[214:215],v231 offset:4864
v_add_f32 v73,v221,v73
v_mfma_f32_16x16x32_bf16 a[96:99],v[184:187],v[140:143],a[96:99]
v_exp_f32 v222,v3
v_mfma_f32_16x16x32_bf16 a[4:7],v[188:191],v[128:131],a[4:7]
v_add_f32 v72,v222,v72
v_cvt_pk_bf16_f32 v113,v220,v222
v_mfma_f32_16x16x32_bf16 a[36:39],v[188:191],v[132:135],a[36:39]
v_exp_f32 v223,v7
v_mfma_f32_16x16x32_bf16 a[68:71],v[188:191],v[136:139],a[68:71]
ds_read_b128 v[184:187],v226
v_add_f32 v73,v223,v73
v_mfma_f32_16x16x32_bf16 a[100:103],v[188:191],v[140:143],a[100:103]
s_waitcnt lgkmcnt(9)
v_cvt_pk_bf16_f32 v117,v221,v223
v_add_f32 v106,v74,v106
)A"
R"A(v_mfma_f32_16x16x32_bf16 a[8:11],v[192:195],v[128:131],a[8:11]
v_exp_f32 v216,v8
v_mfma_f32_16x16x32_bf16 a[40:43],v[192:195],v[132:135],a[40:43]
v_exp_f32 v217,v12
v_mfma_f32_16x16x32_bf16 a[72:75],v[192:195],v[136:139],a[72:75]
ds_read_b128 v[188:191],v226 offset:256
v_add_f32 v107,v75,v107
v_mfma_f32_16x16x32_bf16 a[104:107],v[192:195],v[140:143],a[104:107]
v_exp_f32 v218,v9
v_mfma_f32_16x16x32_bf16 a[12:15],v[196:199],v[128:131],a[12:15]
v_add_f32 v74,v216,v218
v_cvt_pk_bf16_f32 v120,v216,v218
v_mfma_f32_16x16x32_bf16 a[44:47],v[196:199],v[132:135],a[44:47]
v_exp_f32 v219,v13
v_mfma_f32_16x16x32_bf16 a[76:79],v[196:199],v[136:139],a[76:79]
v_add_f32 v75,v217,v219
v_cvt_pk_bf16_f32 v124,v217,v219
v_mfma_f32_16x16x32_bf16 a[108:111],v[196:199],v[140:143],a[108:111]
s_waitcnt lgkmcnt(6)
ds_read_b128 v[192:195],v226 offset:512
ds_read_b128 v[196:199],v226 offset:768
v_mfma_f32_16x16x32_bf16 a[16:19],v[200:203],v[128:131],a[16:19]
v_exp_f32 v220,v10
v_mfma_f32_16x16x32_bf16 a[48:51],v[200:203],v[132:135],a[48:51]
v_exp_f32 v221,v14
v_add_f32 v74,v220,v74
v_mfma_f32_16x16x32_bf16 a[80:83],v[200:203],v[136:139],a[80:83]
v_add_f32 v75,v221,v75
v_exp_f32 v222,v11
v_mfma_f32_16x16x32_bf16 a[112:115],v[200:203],v[140:143],a[112:115]
v_add_f32 v74,v222,v74
v_cvt_pk_bf16_f32 v121,v220,v222
v_mfma_f32_16x16x32_bf16 a[20:23],v[204:207],v[128:131],a[20:23]
v_exp_f32 v223,v15
v_mfma_f32_16x16x32_bf16 a[52:55],v[204:207],v[132:135],a[52:55]
v_add_f32 v75,v223,v75
v_cvt_pk_bf16_f32 v125,v221,v223
v_mfma_f32_16x16x32_bf16 a[84:87],v[204:207],v[136:139],a[84:87]
v_exp_f32 v216,v64
v_mfma_f32_16x16x32_bf16 a[116:119],v[204:207],v[140:143],a[116:119]
s_waitcnt lgkmcnt(4)
ds_read_b128 v[200:203],v226 offset:4096
v_add_f32 v108,v76,v108
v_mfma_f32_16x16x32_bf16 a[24:27],v[208:211],v[128:131],a[24:27]
v_exp_f32 v217,v65
v_mfma_f32_16x16x32_bf16 a[56:59],v[208:211],v[132:135],a[56:59]
v_add_f32 v76,v216,v217
v_cvt_pk_bf16_f32 v144,v216,v217
v_mfma_f32_16x16x32_bf16 a[88:91],v[208:211],v[136:139],a[88:91]
v_exp_f32 v218,v66
v_mfma_f32_16x16x32_bf16 a[120:123],v[208:211],v[140:143],a[120:123]
ds_read_b128 v[204:207],v226 offset:4352
v_add_f32 v76,v218,v76
v_mfma_f32_16x16x32_bf16 a[28:31],v[212:215],v[128:131],a[28:31]
v_exp_f32 v219,v67
v_mfma_f32_16x16x32_bf16 a[60:63],v[212:215],v[132:135],a[60:63]
v_add_f32 v76,v219,v76
v_cvt_pk_bf16_f32 v145,v218,v219
v_mfma_f32_16x16x32_bf16 a[92:95],v[212:215],v[136:139],a[92:95]
v_exp_f32 v220,v16
v_mfma_f32_16x16x32_bf16 a[124:127],v[212:215],v[140:143],a[124:127]
s_waitcnt lgkmcnt(4)
v_exp_f32 v221,v20
v_add_f32 v72,v220,v72
v_mfma_f32_16x16x32_bf16 v[32:35],v[184:187],a[192:195],v[80:83]
ds_read_b128 v[208:211],v226 offset:4608
v_add_f32 v73,v221,v73
v_mfma_f32_16x16x32_bf16 v[32:35],v[188:191],a[196:199],v[32:35]
v_exp_f32 v222,v17
v_mfma_f32_16x16x32_bf16 v[36:39],v[184:187],a[208:211],v[84:87]
v_add_f32 v72,v222,v72
v_cvt_pk_bf16_f32 v114,v220,v222
v_mfma_f32_16x16x32_bf16 v[36:39],v[188:191],a[212:215],v[36:39]
v_exp_f32 v223,v21
v_mfma_f32_16x16x32_bf16 v[40:43],v[184:187],a[224:227],v[88:91]
ds_read_b128 v[212:215],v226 offset:4864
v_add_f32 v73,v223,v73
v_mfma_f32_16x16x32_bf16 v[40:43],v[188:191],a[228:231],v[40:43]
s_cmp_eq_u32 s56,32768
s_cselect_b32 s57,0xffff8000,s58
v_cvt_pk_bf16_f32 v118,v221,v223
v_exp_f32 v216,v18
v_mfma_f32_16x16x32_bf16 v[44:47],v[184:187],a[240:243],v[92:95]
v_exp_f32 v217,v22
v_mfma_f32_16x16x32_bf16 v[44:47],v[188:191],a[244:247],v[44:47]
s_waitcnt lgkmcnt(4)
v_add_f32 v72,v216,v72
v_add_f32 v73,v217,v73
v_mfma_f32_16x16x32_bf16 v[32:35],v[192:195],a[200:203],v[32:35]
v_exp_f32 v218,v19
v_mfma_f32_16x16x32_bf16 v[32:35],v[196:199],a[204:207],v[32:35]
v_add_f32 v72,v218,v72
v_cvt_pk_bf16_f32 v115,v216,v218
v_mfma_f32_16x16x32_bf16 v[36:39],v[192:195],a[216:219],v[36:39]
v_exp_f32 v219,v23
v_mfma_f32_16x16x32_bf16 v[36:39],v[196:199],a[220:223],v[36:39]
ds_read_b64_tr_b16 v[184:185],v233 offset:16384
ds_read_b64_tr_b16 v[186:187],v233 offset:20480
v_add_f32 v73,v219,v73
v_cvt_pk_bf16_f32 v119,v217,v219
v_mfma_f32_16x16x32_bf16 v[40:43],v[192:195],a[232:235],v[40:43]
ds_read_b64_tr_b16 v[188:189],v234 offset:16384
ds_read_b64_tr_b16 v[190:191],v234 offset:20480
v_mfma_f32_16x16x32_bf16 v[40:43],v[196:199],a[236:239],v[40:43]
v_exp_f32 v220,v24
v_max_f32 v228,v72,v73
v_exp_f32 v221,v28
v_mfma_f32_16x16x32_bf16 v[44:47],v[192:195],a[248:251],v[44:47]
v_add_f32 v74,v220,v74
v_add_f32 v75,v221,v75
v_mfma_f32_16x16x32_bf16 v[44:47],v[196:199],a[252:255],v[44:47]
s_waitcnt lgkmcnt(6)
v_exp_f32 v222,v25
v_add_u32 v232,s57,v232
v_mfma_f32_16x16x32_bf16 v[48:51],v[200:203],a[192:195],v[80:83]
v_add_f32 v74,v222,v74
v_cvt_pk_bf16_f32 v122,v220,v222
v_mfma_f32_16x16x32_bf16 v[48:51],v[204:207],a[196:199],v[48:51]
v_exp_f32 v223,v29
v_add_u32 v226,s57,v226
v_mfma_f32_16x16x32_bf16 v[52:55],v[200:203],a[208:211],v[84:87]
v_add_f32 v75,v223,v75
v_cvt_pk_bf16_f32 v126,v221,v223
v_mfma_f32_16x16x32_bf16 v[52:55],v[204:207],a[212:215],v[52:55]
ds_read_b64_tr_b16 v[192:193],v233 offset:16640
ds_read_b64_tr_b16 v[194:195],v233 offset:20736
v_exp_f32 v216,v26
v_mfma_f32_16x16x32_bf16 v[56:59],v[200:203],a[224:227],v[88:91]
ds_read_b64_tr_b16 v[196:197],v234 offset:16640
ds_read_b64_tr_b16 v[198:199],v234 offset:20736
v_mfma_f32_16x16x32_bf16 v[56:59],v[204:207],a[228:231],v[56:59]
v_exp_f32 v217,v30
v_add_f32 v74,v216,v74
v_mfma_f32_16x16x32_bf16 v[60:63],v[200:203],a[240:243],v[92:95]
ds_read_b64_tr_b16 v[200:201],v233 offset:16896
v_add_f32 v75,v217,v75
v_mfma_f32_16x16x32_bf16 v[60:63],v[204:207],a[244:247],v[60:63]
s_waitcnt lgkmcnt(9)
v_exp_f32 v218,v27
v_mfma_f32_16x16x32_bf16 v[48:51],v[208:211],a[200:203],v[48:51]
v_add_f32 v74,v218,v74
v_cvt_pk_bf16_f32 v123,v216,v218
v_mfma_f32_16x16x32_bf16 v[48:51],v[212:215],a[204:207],v[48:51]
v_exp_f32 v219,v31
v_mfma_f32_16x16x32_bf16 v[52:55],v[208:211],a[216:219],v[52:55]
v_add_f32 v75,v219,v75
v_cvt_pk_bf16_f32 v127,v217,v219
v_mfma_f32_16x16x32_bf16 v[52:55],v[212:215],a[220:223],v[52:55]
ds_read_b64_tr_b16 v[202:203],v233 offset:20992
v_exp_f32 v220,v68
v_max3_f32 v228,v228,v74,v75
v_exp_f32 v221,v69
v_mfma_f32_16x16x32_bf16 v[56:59],v[208:211],a[232:235],v[56:59]
v_exp_f32 v222,v70
v_exp_f32 v223,v71
v_mfma_f32_16x16x32_bf16 v[56:59],v[212:215],a[236:239],v[56:59]
ds_read_b64_tr_b16 v[204:205],v234 offset:16896
ds_read_b64_tr_b16 v[206:207],v234 offset:20992
v_add_f32 v76,v220,v76
v_add_f32 v76,v221,v76
v_cvt_pk_bf16_f32 v146,v220,v221
v_add_f32 v76,v222,v76
v_add_f32 v76,v223,v76
v_max_f32 v228,v228,v76
v_cmp_nge_f32 vcc,0x46800000,v228
v_mfma_f32_16x16x32_bf16 v[60:63],v[208:211],a[248:251],v[60:63]
ds_read_b64_tr_b16 v[208:209],v233 offset:17152
v_cvt_pk_bf16_f32 v147,v222,v223
v_mfma_f32_16x16x32_bf16 v[60:63],v[212:215],a[252:255],v[60:63]
s_waitcnt lgkmcnt(9)
s_cbranch_vccnz .Lst10
.Lbk10:
ds_read_b64_tr_b16 v[210:211],v233 offset:21248
v_mfma_f32_16x16x32_bf16 a[0:3],v[184:187],v[112:115],a[0:3]
s_waitcnt vmcnt(8)
s_barrier
ds_read_b64_tr_b16 v[212:213],v234 offset:17152
v_mfma_f32_16x16x32_bf16 a[32:35],v[184:187],v[116:119],a[32:35]
s_add_u32 m0,s63,65536
ds_read_b64_tr_b16 v[214:215],v234 offset:21248
v_add_f32 v104,v72,v104
v_mfma_f32_16x16x32_bf16 a[64:67],v[184:187],v[120:123],a[64:67]
v_exp_f32 v216,v32
v_mfma_f32_16x16x32_bf16 a[96:99],v[184:187],v[124:127],a[96:99]
buffer_load_dwordx4 v235,s[48:51],s61 offen nt lds
v_add_f32 v105,v73,v105
v_mfma_f32_16x16x32_bf16 a[128:131],v[184:187],v[144:147],a[128:131]
v_exp_f32 v217,v36
v_mfma_f32_16x16x32_bf16 a[4:7],v[188:191],v[112:115],a[4:7]
ds_read_b128 v[184:187],v232
buffer_load_dwordx4 v236,s[48:51],s61 offen offset:1024 nt lds
v_mfma_f32_16x16x32_bf16 a[36:39],v[188:191],v[116:119],a[36:39]
v_exp_f32 v218,v33
v_mfma_f32_16x16x32_bf16 a[68:71],v[188:191],v[120:123],a[68:71]
v_add_f32 v72,v216,v218
v_cvt_pk_bf16_f32 v128,v216,v218
v_mfma_f32_16x16x32_bf16 a[100:103],v[188:191],v[124:127],a[100:103]
v_exp_f32 v219,v37
v_mfma_f32_16x16x32_bf16 a[132:135],v[188:191],v[144:147],a[132:135]
s_waitcnt lgkmcnt(9)
v_add_f32 v73,v217,v219
v_cvt_pk_bf16_f32 v132,v217,v219
v_mfma_f32_16x16x32_bf16 a[8:11],v[192:195],v[112:115],a[8:11]
v_exp_f32 v220,v34
v_mfma_f32_16x16x32_bf16 a[40:43],v[192:195],v[116:119],a[40:43]
ds_read_b128 v[188:191],v232 offset:256
v_add_f32 v72,v220,v72
v_mfma_f32_16x16x32_bf16 a[72:75],v[192:195],v[120:123],a[72:75]
v_exp_f32 v221,v38
v_mfma_f32_16x16x32_bf16 a[104:107],v[192:195],v[124:127],a[104:107]
buffer_load_dwordx4 v235,s[48:51],s61 offen offset:2048 nt lds
v_add_f32 v73,v221,v73
v_mfma_f32_16x16x32_bf16 a[136:139],v[192:195],v[144:147],a[136:139]
v_exp_f32 v222,v35
v_mfma_f32_16x16x32_bf16 a[12:15],v[196:199],v[112:115],a[12:15]
v_add_f32 v72,v222,v72
v_cvt_pk_bf16_f32 v129,v220,v222
v_mfma_f32_16x16x32_bf16 a[44:47],v[196:199],v[116:119],a[44:47]
ds_read_b128 v[192:195],v232 offset:512
v_exp_f32 v223,v39
v_mfma_f32_16x16x32_bf16 a[76:79],v[196:199],v[120:123],a[76:79]
v_add_f32 v73,v223,v73
v_cvt_pk_bf16_f32 v133,v221,v223
v_mfma_f32_16x16x32_bf16 a[108:111],v[196:199],v[124:127],a[108:111]
v_exp_f32 v216,v40
v_mfma_f32_16x16x32_bf16 a[140:143],v[196:199],v[144:147],a[140:143]
s_waitcnt lgkmcnt(7)
v_add_f32 v106,v74,v106
v_add_f32 v107,v75,v107
v_mfma_f32_16x16x32_bf16 a[16:19],v[200:203],v[112:115],a[16:19]
v_exp_f32 v217,v44
v_mfma_f32_16x16x32_bf16 a[48:51],v[200:203],v[116:119],a[48:51]
ds_read_b128 v[196:199],v232 offset:768
buffer_load_dwordx4 v236,s[48:51],s61 offen offset:3072 nt lds
v_mfma_f32_16x16x32_bf16 a[80:83],v[200:203],v[120:123],a[80:83]
v_exp_f32 v218,v41
v_mfma_f32_16x16x32_bf16 a[112:115],v[200:203],v[124:127],a[112:115]
v_add_f32 v74,v216,v218
v_cvt_pk_bf16_f32 v136,v216,v218
v_mfma_f32_16x16x32_bf16 a[144:147],v[200:203],v[144:147],a[144:147]
v_exp_f32 v219,v45
v_mfma_f32_16x16x32_bf16 a[20:23],v[204:207],v[112:115],a[20:23]
v_add_f32 v75,v217,v219
v_cvt_pk_bf16_f32 v140,v217,v219
v_mfma_f32_16x16x32_bf16 a[52:55],v[204:207],v[116:119],a[52:55]
s_add_u32 m0,s56,s63
ds_read_b128 v[200:203],v232 offset:4096
v_exp_f32 v220,v42
v_mfma_f32_16x16x32_bf16 a[84:87],v[204:207],v[120:123],a[84:87]
v_add_f32 v74,v220,v74
v_exp_f32 v221,v46
v_mfma_f32_16x16x32_bf16 a[116:119],v[204:207],v[124:127],a[116:119]
buffer_load_dwordx4 v235,s[44:47],s61 offen nt lds
v_add_f32 v75,v221,v75
v_mfma_f32_16x16x32_bf16 a[148:151],v[204:207],v[144:147],a[148:151]
s_waitcnt lgkmcnt(5)
v_exp_f32 v222,v43
v_mfma_f32_16x16x32_bf16 a[24:27],v[208:211],v[112:115],a[24:27]
ds_read_b128 v[204:207],v232 offset:4352
v_add_f32 v74,v222,v74
v_mfma_f32_16x16x32_bf16 a[56:59],v[208:211],v[116:119],a[56:59]
v_cvt_pk_bf16_f32 v137,v220,v222
v_exp_f32 v223,v47
v_mfma_f32_16x16x32_bf16 a[88:91],v[208:211],v[120:123],a[88:91]
v_add_f32 v75,v223,v75
v_cvt_pk_bf16_f32 v141,v221,v223
v_mfma_f32_16x16x32_bf16 a[120:123],v[208:211],v[124:127],a[120:123]
v_exp_f32 v216,v48
v_mfma_f32_16x16x32_bf16 a[152:155],v[208:211],v[144:147],a[152:155]
ds_read_b128 v[208:211],v232 offset:4608
v_add_f32 v72,v216,v72
v_mfma_f32_16x16x32_bf16 a[28:31],v[212:215],v[112:115],a[28:31]
v_exp_f32 v217,v52
v_mfma_f32_16x16x32_bf16 a[60:63],v[212:215],v[116:119],a[60:63]
buffer_load_dwordx4 v235,s[44:47],s61 offen offset:1024 nt lds
v_add_f32 v73,v217,v73
v_mfma_f32_16x16x32_bf16 a[92:95],v[212:215],v[120:123],a[92:95]
v_exp_f32 v218,v49
v_mfma_f32_16x16x32_bf16 a[124:127],v[212:215],v[124:127],a[124:127]
v_add_f32 v72,v218,v72
v_cvt_pk_bf16_f32 v130,v216,v218
v_mfma_f32_16x16x32_bf16 a[156:159],v[212:215],v[144:147],a[156:159]
s_waitcnt lgkmcnt(5)
ds_read_b128 v[212:215],v232 offset:4864
v_exp_f32 v219,v53
v_mfma_f32_16x16x32_bf16 v[0:3],v[184:187],a[192:195],v[80:83]
v_add_f32 v73,v219,v73
v_cvt_pk_bf16_f32 v134,v217,v219
v_mfma_f32_16x16x32_bf16 v[0:3],v[188:191],a[196:199],v[0:3]
v_exp_f32 v220,v50
v_mfma_f32_16x16x32_bf16 v[4:7],v[184:187],a[208:211],v[84:87]
v_add_f32 v72,v220,v72
v_mfma_f32_16x16x32_bf16 v[4:7],v[188:191],a[212:215],v[4:7]
v_exp_f32 v221,v54
v_mfma_f32_16x16x32_bf16 v[8:11],v[184:187],a[224:227],v[88:91]
buffer_load_dwordx4 v237,s[44:47],s61 offen offset:2048 nt lds
)A"
R"A(v_add_f32 v73,v221,v73
v_mfma_f32_16x16x32_bf16 v[8:11],v[188:191],a[228:231],v[8:11]
v_exp_f32 v222,v51
v_mfma_f32_16x16x32_bf16 v[12:15],v[184:187],a[240:243],v[92:95]
v_add_f32 v72,v222,v72
v_cvt_pk_bf16_f32 v131,v220,v222
v_mfma_f32_16x16x32_bf16 v[12:15],v[188:191],a[244:247],v[12:15]
v_exp_f32 v223,v55
v_mfma_f32_16x16x32_bf16 v[64:67],v[184:187],a[160:163],v[96:99]
ds_read_b64_tr_b16 v[184:185],v230 offset:16384
ds_read_b64_tr_b16 v[186:187],v230 offset:20480
v_mfma_f32_16x16x32_bf16 v[64:67],v[188:191],a[164:167],v[64:67]
s_waitcnt lgkmcnt(6)
v_add_f32 v73,v223,v73
v_cvt_pk_bf16_f32 v135,v221,v223
v_mfma_f32_16x16x32_bf16 v[0:3],v[192:195],a[200:203],v[0:3]
ds_read_b64_tr_b16 v[188:189],v231 offset:16384
ds_read_b64_tr_b16 v[190:191],v231 offset:20480
v_mfma_f32_16x16x32_bf16 v[0:3],v[196:199],a[204:207],v[0:3]
v_exp_f32 v216,v56
v_mfma_f32_16x16x32_bf16 v[4:7],v[192:195],a[216:219],v[4:7]
v_max_f32 v228,v72,v73
v_add_f32 v74,v216,v74
v_mfma_f32_16x16x32_bf16 v[4:7],v[196:199],a[220:223],v[4:7]
v_exp_f32 v217,v60
v_mfma_f32_16x16x32_bf16 v[8:11],v[192:195],a[232:235],v[8:11]
buffer_load_dwordx4 v237,s[44:47],s61 offen offset:3072 nt lds
v_add_f32 v75,v217,v75
v_mfma_f32_16x16x32_bf16 v[8:11],v[196:199],a[236:239],v[8:11]
v_exp_f32 v218,v57
v_mfma_f32_16x16x32_bf16 v[12:15],v[192:195],a[248:251],v[12:15]
v_add_f32 v74,v218,v74
v_cvt_pk_bf16_f32 v138,v216,v218
v_mfma_f32_16x16x32_bf16 v[12:15],v[196:199],a[252:255],v[12:15]
v_exp_f32 v219,v61
v_mfma_f32_16x16x32_bf16 v[64:67],v[192:195],a[168:171],v[64:67]
s_add_u32 s62,s62,16384
v_add_f32 v75,v219,v75
v_cvt_pk_bf16_f32 v142,v217,v219
v_mfma_f32_16x16x32_bf16 v[64:67],v[196:199],a[172:175],v[64:67]
s_waitcnt lgkmcnt(6)
v_exp_f32 v220,v58
v_mfma_f32_16x16x32_bf16 v[16:19],v[200:203],a[192:195],v[80:83]
s_xor_b32 s61,s62,s75
ds_read_b64_tr_b16 v[192:193],v230 offset:16640
ds_read_b64_tr_b16 v[194:195],v230 offset:20736
v_mfma_f32_16x16x32_bf16 v[16:19],v[204:207],a[196:199],v[16:19]
s_add_u32 s56,s56,s57
ds_read_b64_tr_b16 v[196:197],v231 offset:16640
v_add_f32 v74,v220,v74
v_mfma_f32_16x16x32_bf16 v[20:23],v[200:203],a[208:211],v[84:87]
ds_read_b64_tr_b16 v[198:199],v231 offset:20736
v_mfma_f32_16x16x32_bf16 v[20:23],v[204:207],a[212:215],v[20:23]
v_exp_f32 v221,v62
v_mfma_f32_16x16x32_bf16 v[24:27],v[200:203],a[224:227],v[88:91]
v_add_f32 v75,v221,v75
v_mfma_f32_16x16x32_bf16 v[24:27],v[204:207],a[228:231],v[24:27]
v_exp_f32 v222,v59
v_mfma_f32_16x16x32_bf16 v[28:31],v[200:203],a[240:243],v[92:95]
v_exp_f32 v223,v63
v_mfma_f32_16x16x32_bf16 v[28:31],v[204:207],a[244:247],v[28:31]
v_add_f32 v74,v222,v74
v_add_f32 v75,v223,v75
v_max3_f32 v228,v228,v74,v75
v_cmp_nge_f32 vcc,0x46800000,v228
v_cvt_pk_bf16_f32 v139,v220,v222
v_mfma_f32_16x16x32_bf16 v[68:71],v[200:203],a[160:163],v[96:99]
ds_read_b64_tr_b16 v[200:201],v230 offset:16896
v_cvt_pk_bf16_f32 v143,v221,v223
v_mfma_f32_16x16x32_bf16 v[68:71],v[204:207],a[164:167],v[68:71]
s_waitcnt lgkmcnt(9)
s_cbranch_vccnz .Lst11
.Lbk11:
ds_read_b64_tr_b16 v[202:203],v230 offset:20992
v_mfma_f32_16x16x32_bf16 v[16:19],v[208:211],a[200:203],v[16:19]
ds_read_b64_tr_b16 v[204:205],v231 offset:16896
ds_read_b64_tr_b16 v[206:207],v231 offset:20992
v_mfma_f32_16x16x32_bf16 v[16:19],v[212:215],a[204:207],v[16:19]
v_exp_f32 v216,v0
v_mfma_f32_16x16x32_bf16 v[20:23],v[208:211],a[216:219],v[20:23]
v_add_f32 v104,v72,v104
v_add_f32 v105,v73,v105
v_mfma_f32_16x16x32_bf16 v[20:23],v[212:215],a[220:223],v[20:23]
v_exp_f32 v217,v4
v_mfma_f32_16x16x32_bf16 v[24:27],v[208:211],a[232:235],v[24:27]
v_exp_f32 v218,v1
v_mfma_f32_16x16x32_bf16 v[24:27],v[212:215],a[236:239],v[24:27]
v_add_f32 v72,v216,v218
v_cvt_pk_bf16_f32 v112,v216,v218
v_mfma_f32_16x16x32_bf16 v[28:31],v[208:211],a[248:251],v[28:31]
v_exp_f32 v219,v5
v_mfma_f32_16x16x32_bf16 v[28:31],v[212:215],a[252:255],v[28:31]
v_add_f32 v73,v217,v219
v_cvt_pk_bf16_f32 v116,v217,v219
v_mfma_f32_16x16x32_bf16 v[68:71],v[208:211],a[168:171],v[68:71]
v_exp_f32 v220,v2
v_mfma_f32_16x16x32_bf16 v[68:71],v[212:215],a[172:175],v[68:71]
s_waitcnt lgkmcnt(8)
ds_read_b64_tr_b16 v[208:209],v230 offset:17152
ds_read_b64_tr_b16 v[210:211],v230 offset:21248
v_mfma_f32_16x16x32_bf16 a[0:3],v[184:187],v[128:131],a[0:3]
v_exp_f32 v221,v6
v_mfma_f32_16x16x32_bf16 a[32:35],v[184:187],v[132:135],a[32:35]
ds_read_b64_tr_b16 v[212:213],v231 offset:17152
v_add_f32 v72,v220,v72
v_mfma_f32_16x16x32_bf16 a[64:67],v[184:187],v[136:139],a[64:67]
ds_read_b64_tr_b16 v[214:215],v231 offset:21248
v_add_f32 v73,v221,v73
v_mfma_f32_16x16x32_bf16 a[96:99],v[184:187],v[140:143],a[96:99]
v_exp_f32 v222,v3
v_mfma_f32_16x16x32_bf16 a[4:7],v[188:191],v[128:131],a[4:7]
v_add_f32 v72,v222,v72
v_cvt_pk_bf16_f32 v113,v220,v222
v_mfma_f32_16x16x32_bf16 a[36:39],v[188:191],v[132:135],a[36:39]
v_exp_f32 v223,v7
v_mfma_f32_16x16x32_bf16 a[68:71],v[188:191],v[136:139],a[68:71]
ds_read_b128 v[184:187],v226
v_add_f32 v73,v223,v73
v_mfma_f32_16x16x32_bf16 a[100:103],v[188:191],v[140:143],a[100:103]
s_waitcnt lgkmcnt(9)
v_cvt_pk_bf16_f32 v117,v221,v223
v_add_f32 v106,v74,v106
v_mfma_f32_16x16x32_bf16 a[8:11],v[192:195],v[128:131],a[8:11]
v_exp_f32 v216,v8
v_mfma_f32_16x16x32_bf16 a[40:43],v[192:195],v[132:135],a[40:43]
v_exp_f32 v217,v12
v_mfma_f32_16x16x32_bf16 a[72:75],v[192:195],v[136:139],a[72:75]
ds_read_b128 v[188:191],v226 offset:256
v_add_f32 v107,v75,v107
v_mfma_f32_16x16x32_bf16 a[104:107],v[192:195],v[140:143],a[104:107]
v_exp_f32 v218,v9
v_mfma_f32_16x16x32_bf16 a[12:15],v[196:199],v[128:131],a[12:15]
v_add_f32 v74,v216,v218
v_cvt_pk_bf16_f32 v120,v216,v218
v_mfma_f32_16x16x32_bf16 a[44:47],v[196:199],v[132:135],a[44:47]
v_exp_f32 v219,v13
v_mfma_f32_16x16x32_bf16 a[76:79],v[196:199],v[136:139],a[76:79]
v_add_f32 v75,v217,v219
v_cvt_pk_bf16_f32 v124,v217,v219
v_mfma_f32_16x16x32_bf16 a[108:111],v[196:199],v[140:143],a[108:111]
s_waitcnt lgkmcnt(6)
ds_read_b128 v[192:195],v226 offset:512
ds_read_b128 v[196:199],v226 offset:768
v_mfma_f32_16x16x32_bf16 a[16:19],v[200:203],v[128:131],a[16:19]
v_exp_f32 v220,v10
v_mfma_f32_16x16x32_bf16 a[48:51],v[200:203],v[132:135],a[48:51]
v_exp_f32 v221,v14
v_add_f32 v74,v220,v74
v_mfma_f32_16x16x32_bf16 a[80:83],v[200:203],v[136:139],a[80:83]
v_add_f32 v75,v221,v75
v_exp_f32 v222,v11
v_mfma_f32_16x16x32_bf16 a[112:115],v[200:203],v[140:143],a[112:115]
v_add_f32 v74,v222,v74
v_cvt_pk_bf16_f32 v121,v220,v222
v_mfma_f32_16x16x32_bf16 a[20:23],v[204:207],v[128:131],a[20:23]
v_exp_f32 v223,v15
v_mfma_f32_16x16x32_bf16 a[52:55],v[204:207],v[132:135],a[52:55]
v_add_f32 v75,v223,v75
v_cvt_pk_bf16_f32 v125,v221,v223
v_mfma_f32_16x16x32_bf16 a[84:87],v[204:207],v[136:139],a[84:87]
v_exp_f32 v216,v64
v_mfma_f32_16x16x32_bf16 a[116:119],v[204:207],v[140:143],a[116:119]
s_waitcnt lgkmcnt(4)
ds_read_b128 v[200:203],v226 offset:4096
v_add_f32 v108,v76,v108
v_mfma_f32_16x16x32_bf16 a[24:27],v[208:211],v[128:131],a[24:27]
v_exp_f32 v217,v65
v_mfma_f32_16x16x32_bf16 a[56:59],v[208:211],v[132:135],a[56:59]
v_add_f32 v76,v216,v217
v_cvt_pk_bf16_f32 v144,v216,v217
v_mfma_f32_16x16x32_bf16 a[88:91],v[208:211],v[136:139],a[88:91]
v_exp_f32 v218,v66
v_mfma_f32_16x16x32_bf16 a[120:123],v[208:211],v[140:143],a[120:123]
ds_read_b128 v[204:207],v226 offset:4352
v_add_f32 v76,v218,v76
v_mfma_f32_16x16x32_bf16 a[28:31],v[212:215],v[128:131],a[28:31]
v_exp_f32 v219,v67
v_mfma_f32_16x16x32_bf16 a[60:63],v[212:215],v[132:135],a[60:63]
v_add_f32 v76,v219,v76
v_cvt_pk_bf16_f32 v145,v218,v219
v_mfma_f32_16x16x32_bf16 a[92:95],v[212:215],v[136:139],a[92:95]
v_exp_f32 v220,v16
v_mfma_f32_16x16x32_bf16 a[124:127],v[212:215],v[140:143],a[124:127]
s_waitcnt lgkmcnt(4)
v_exp_f32 v221,v20
v_add_f32 v72,v220,v72
v_mfma_f32_16x16x32_bf16 v[32:35],v[184:187],a[192:195],v[80:83]
ds_read_b128 v[208:211],v226 offset:4608
v_add_f32 v73,v221,v73
v_mfma_f32_16x16x32_bf16 v[32:35],v[188:191],a[196:199],v[32:35]
v_exp_f32 v222,v17
v_mfma_f32_16x16x32_bf16 v[36:39],v[184:187],a[208:211],v[84:87]
v_add_f32 v72,v222,v72
v_cvt_pk_bf16_f32 v114,v220,v222
v_mfma_f32_16x16x32_bf16 v[36:39],v[188:191],a[212:215],v[36:39]
v_exp_f32 v223,v21
v_mfma_f32_16x16x32_bf16 v[40:43],v[184:187],a[224:227],v[88:91]
ds_read_b128 v[212:215],v226 offset:4864
v_add_f32 v73,v223,v73
v_mfma_f32_16x16x32_bf16 v[40:43],v[188:191],a[228:231],v[40:43]
s_cmp_eq_u32 s56,32768
s_cselect_b32 s57,0xffff8000,s58
v_cvt_pk_bf16_f32 v118,v221,v223
v_exp_f32 v216,v18
v_mfma_f32_16x16x32_bf16 v[44:47],v[184:187],a[240:243],v[92:95]
v_exp_f32 v217,v22
v_mfma_f32_16x16x32_bf16 v[44:47],v[188:191],a[244:247],v[44:47]
s_waitcnt lgkmcnt(4)
v_add_f32 v72,v216,v72
v_add_f32 v73,v217,v73
v_mfma_f32_16x16x32_bf16 v[32:35],v[192:195],a[200:203],v[32:35]
v_exp_f32 v218,v19
v_mfma_f32_16x16x32_bf16 v[32:35],v[196:199],a[204:207],v[32:35]
v_add_f32 v72,v218,v72
v_cvt_pk_bf16_f32 v115,v216,v218
v_mfma_f32_16x16x32_bf16 v[36:39],v[192:195],a[216:219],v[36:39]
v_exp_f32 v219,v23
v_mfma_f32_16x16x32_bf16 v[36:39],v[196:199],a[220:223],v[36:39]
ds_read_b64_tr_b16 v[184:185],v233 offset:32768
ds_read_b64_tr_b16 v[186:187],v233 offset:36864
v_add_f32 v73,v219,v73
v_cvt_pk_bf16_f32 v119,v217,v219
v_mfma_f32_16x16x32_bf16 v[40:43],v[192:195],a[232:235],v[40:43]
ds_read_b64_tr_b16 v[188:189],v234 offset:32768
ds_read_b64_tr_b16 v[190:191],v234 offset:36864
v_mfma_f32_16x16x32_bf16 v[40:43],v[196:199],a[236:239],v[40:43]
v_exp_f32 v220,v24
v_max_f32 v228,v72,v73
v_exp_f32 v221,v28
v_mfma_f32_16x16x32_bf16 v[44:47],v[192:195],a[248:251],v[44:47]
v_add_f32 v74,v220,v74
v_add_f32 v75,v221,v75
v_mfma_f32_16x16x32_bf16 v[44:47],v[196:199],a[252:255],v[44:47]
s_waitcnt lgkmcnt(6)
v_exp_f32 v222,v25
v_add_u32 v232,s57,v232
v_mfma_f32_16x16x32_bf16 v[48:51],v[200:203],a[192:195],v[80:83]
v_add_f32 v74,v222,v74
v_cvt_pk_bf16_f32 v122,v220,v222
v_mfma_f32_16x16x32_bf16 v[48:51],v[204:207],a[196:199],v[48:51]
v_exp_f32 v223,v29
v_add_u32 v226,s57,v226
v_mfma_f32_16x16x32_bf16 v[52:55],v[200:203],a[208:211],v[84:87]
v_add_f32 v75,v223,v75
v_cvt_pk_bf16_f32 v126,v221,v223
v_mfma_f32_16x16x32_bf16 v[52:55],v[204:207],a[212:215],v[52:55]
ds_read_b64_tr_b16 v[192:193],v233 offset:33024
ds_read_b64_tr_b16 v[194:195],v233 offset:37120
v_exp_f32 v216,v26
v_mfma_f32_16x16x32_bf16 v[56:59],v[200:203],a[224:227],v[88:91]
ds_read_b64_tr_b16 v[196:197],v234 offset:33024
ds_read_b64_tr_b16 v[198:199],v234 offset:37120
v_mfma_f32_16x16x32_bf16 v[56:59],v[204:207],a[228:231],v[56:59]
v_exp_f32 v217,v30
v_add_f32 v74,v216,v74
v_mfma_f32_16x16x32_bf16 v[60:63],v[200:203],a[240:243],v[92:95]
ds_read_b64_tr_b16 v[200:201],v233 offset:33280
v_add_f32 v75,v217,v75
v_mfma_f32_16x16x32_bf16 v[60:63],v[204:207],a[244:247],v[60:63]
s_waitcnt lgkmcnt(9)
v_exp_f32 v218,v27
v_mfma_f32_16x16x32_bf16 v[48:51],v[208:211],a[200:203],v[48:51]
v_add_f32 v74,v218,v74
v_cvt_pk_bf16_f32 v123,v216,v218
v_mfma_f32_16x16x32_bf16 v[48:51],v[212:215],a[204:207],v[48:51]
v_exp_f32 v219,v31
v_mfma_f32_16x16x32_bf16 v[52:55],v[208:211],a[216:219],v[52:55]
v_add_f32 v75,v219,v75
v_cvt_pk_bf16_f32 v127,v217,v219
v_mfma_f32_16x16x32_bf16 v[52:55],v[212:215],a[220:223],v[52:55]
ds_read_b64_tr_b16 v[202:203],v233 offset:37376
v_exp_f32 v220,v68
v_max3_f32 v228,v228,v74,v75
v_exp_f32 v221,v69
v_mfma_f32_16x16x32_bf16 v[56:59],v[208:211],a[232:235],v[56:59]
v_exp_f32 v222,v70
v_exp_f32 v223,v71
v_mfma_f32_16x16x32_bf16 v[56:59],v[212:215],a[236:239],v[56:59]
ds_read_b64_tr_b16 v[204:205],v234 offset:33280
ds_read_b64_tr_b16 v[206:207],v234 offset:37376
v_add_f32 v76,v220,v76
v_add_f32 v76,v221,v76
v_cvt_pk_bf16_f32 v146,v220,v221
v_add_f32 v76,v222,v76
v_add_f32 v76,v223,v76
v_max_f32 v228,v228,v76
v_cmp_nge_f32 vcc,0x46800000,v228
v_mfma_f32_16x16x32_bf16 v[60:63],v[208:211],a[248:251],v[60:63]
ds_read_b64_tr_b16 v[208:209],v233 offset:33536
v_cvt_pk_bf16_f32 v147,v222,v223
v_mfma_f32_16x16x32_bf16 v[60:63],v[212:215],a[252:255],v[60:63]
s_waitcnt lgkmcnt(9)
s_cbranch_vccnz .Lst12
.Lbk12:
ds_read_b64_tr_b16 v[210:211],v233 offset:37632
s_sub_u32 s64,s64,1
s_cmp_lg_u32 s64,0
s_cbranch_scc1 .La
s_cmp_lg_u32 s65,0
s_cbranch_scc1 .Lt15b
)A"
R"A(v_mfma_f32_16x16x32_bf16 a[0:3],v[184:187],v[112:115],a[0:3]
s_waitcnt vmcnt(8)
s_barrier
ds_read_b64_tr_b16 v[212:213],v234 offset:33536
v_mfma_f32_16x16x32_bf16 a[32:35],v[184:187],v[116:119],a[32:35]
s_add_u32 m0,s63,81920
ds_read_b64_tr_b16 v[214:215],v234 offset:37632
v_add_f32 v104,v72,v104
v_mfma_f32_16x16x32_bf16 a[64:67],v[184:187],v[120:123],a[64:67]
v_exp_f32 v216,v32
v_mfma_f32_16x16x32_bf16 a[96:99],v[184:187],v[124:127],a[96:99]
buffer_load_dwordx4 v235,s[48:51],s61 offen nt lds
v_add_f32 v105,v73,v105
v_mfma_f32_16x16x32_bf16 a[128:131],v[184:187],v[144:147],a[128:131]
v_exp_f32 v217,v36
v_mfma_f32_16x16x32_bf16 a[4:7],v[188:191],v[112:115],a[4:7]
ds_read_b128 v[184:187],v232
buffer_load_dwordx4 v236,s[48:51],s61 offen offset:1024 nt lds
v_mfma_f32_16x16x32_bf16 a[36:39],v[188:191],v[116:119],a[36:39]
v_exp_f32 v218,v33
v_mfma_f32_16x16x32_bf16 a[68:71],v[188:191],v[120:123],a[68:71]
v_add_f32 v72,v216,v218
v_cvt_pk_bf16_f32 v128,v216,v218
v_mfma_f32_16x16x32_bf16 a[100:103],v[188:191],v[124:127],a[100:103]
v_exp_f32 v219,v37
v_mfma_f32_16x16x32_bf16 a[132:135],v[188:191],v[144:147],a[132:135]
s_waitcnt lgkmcnt(9)
v_add_f32 v73,v217,v219
v_cvt_pk_bf16_f32 v132,v217,v219
v_mfma_f32_16x16x32_bf16 a[8:11],v[192:195],v[112:115],a[8:11]
v_exp_f32 v220,v34
v_mfma_f32_16x16x32_bf16 a[40:43],v[192:195],v[116:119],a[40:43]
ds_read_b128 v[188:191],v232 offset:256
v_add_f32 v72,v220,v72
v_mfma_f32_16x16x32_bf16 a[72:75],v[192:195],v[120:123],a[72:75]
v_exp_f32 v221,v38
v_mfma_f32_16x16x32_bf16 a[104:107],v[192:195],v[124:127],a[104:107]
buffer_load_dwordx4 v235,s[48:51],s61 offen offset:2048 nt lds
v_add_f32 v73,v221,v73
v_mfma_f32_16x16x32_bf16 a[136:139],v[192:195],v[144:147],a[136:139]
v_exp_f32 v222,v35
v_mfma_f32_16x16x32_bf16 a[12:15],v[196:199],v[112:115],a[12:15]
v_add_f32 v72,v222,v72
v_cvt_pk_bf16_f32 v129,v220,v222
v_mfma_f32_16x16x32_bf16 a[44:47],v[196:199],v[116:119],a[44:47]
ds_read_b128 v[192:195],v232 offset:512
v_exp_f32 v223,v39
v_mfma_f32_16x16x32_bf16 a[76:79],v[196:199],v[120:123],a[76:79]
v_add_f32 v73,v223,v73
v_cvt_pk_bf16_f32 v133,v221,v223
v_mfma_f32_16x16x32_bf16 a[108:111],v[196:199],v[124:127],a[108:111]
v_exp_f32 v216,v40
v_mfma_f32_16x16x32_bf16 a[140:143],v[196:199],v[144:147],a[140:143]
s_waitcnt lgkmcnt(7)
v_add_f32 v106,v74,v106
v_add_f32 v107,v75,v107
v_mfma_f32_16x16x32_bf16 a[16:19],v[200:203],v[112:115],a[16:19]
v_exp_f32 v217,v44
v_mfma_f32_16x16x32_bf16 a[48:51],v[200:203],v[116:119],a[48:51]
ds_read_b128 v[196:199],v232 offset:768
buffer_load_dwordx4 v236,s[48:51],s61 offen offset:3072 nt lds
v_mfma_f32_16x16x32_bf16 a[80:83],v[200:203],v[120:123],a[80:83]
v_exp_f32 v218,v41
v_mfma_f32_16x16x32_bf16 a[112:115],v[200:203],v[124:127],a[112:115]
v_add_f32 v74,v216,v218
v_cvt_pk_bf16_f32 v136,v216,v218
v_mfma_f32_16x16x32_bf16 a[144:147],v[200:203],v[144:147],a[144:147]
v_exp_f32 v219,v45
v_mfma_f32_16x16x32_bf16 a[20:23],v[204:207],v[112:115],a[20:23]
v_add_f32 v75,v217,v219
v_cvt_pk_bf16_f32 v140,v217,v219
v_mfma_f32_16x16x32_bf16 a[52:55],v[204:207],v[116:119],a[52:55]
s_add_u32 m0,s56,s63
ds_read_b128 v[200:203],v232 offset:4096
v_exp_f32 v220,v42
v_mfma_f32_16x16x32_bf16 a[84:87],v[204:207],v[120:123],a[84:87]
v_add_f32 v74,v220,v74
v_exp_f32 v221,v46
v_mfma_f32_16x16x32_bf16 a[116:119],v[204:207],v[124:127],a[116:119]
buffer_load_dwordx4 v235,s[44:47],s61 offen nt lds
v_add_f32 v75,v221,v75
v_mfma_f32_16x16x32_bf16 a[148:151],v[204:207],v[144:147],a[148:151]
s_waitcnt lgkmcnt(5)
v_exp_f32 v222,v43
v_mfma_f32_16x16x32_bf16 a[24:27],v[208:211],v[112:115],a[24:27]
ds_read_b128 v[204:207],v232 offset:4352
v_add_f32 v74,v222,v74
v_mfma_f32_16x16x32_bf16 a[56:59],v[208:211],v[116:119],a[56:59]
v_cvt_pk_bf16_f32 v137,v220,v222
v_exp_f32 v223,v47
v_mfma_f32_16x16x32_bf16 a[88:91],v[208:211],v[120:123],a[88:91]
v_add_f32 v75,v223,v75
v_cvt_pk_bf16_f32 v141,v221,v223
v_mfma_f32_16x16x32_bf16 a[120:123],v[208:211],v[124:127],a[120:123]
v_exp_f32 v216,v48
v_mfma_f32_16x16x32_bf16 a[152:155],v[208:211],v[144:147],a[152:155]
ds_read_b128 v[208:211],v232 offset:4608
v_add_f32 v72,v216,v72
v_mfma_f32_16x16x32_bf16 a[28:31],v[212:215],v[112:115],a[28:31]
v_exp_f32 v217,v52
v_mfma_f32_16x16x32_bf16 a[60:63],v[212:215],v[116:119],a[60:63]
buffer_load_dwordx4 v235,s[44:47],s61 offen offset:1024 nt lds
v_add_f32 v73,v217,v73
v_mfma_f32_16x16x32_bf16 a[92:95],v[212:215],v[120:123],a[92:95]
v_exp_f32 v218,v49
v_mfma_f32_16x16x32_bf16 a[124:127],v[212:215],v[124:127],a[124:127]
v_add_f32 v72,v218,v72
v_cvt_pk_bf16_f32 v130,v216,v218
v_mfma_f32_16x16x32_bf16 a[156:159],v[212:215],v[144:147],a[156:159]
s_waitcnt lgkmcnt(5)
ds_read_b128 v[212:215],v232 offset:4864
v_exp_f32 v219,v53
v_mfma_f32_16x16x32_bf16 v[0:3],v[184:187],a[192:195],v[80:83]
v_add_f32 v73,v219,v73
v_cvt_pk_bf16_f32 v134,v217,v219
v_mfma_f32_16x16x32_bf16 v[0:3],v[188:191],a[196:199],v[0:3]
v_exp_f32 v220,v50
v_mfma_f32_16x16x32_bf16 v[4:7],v[184:187],a[208:211],v[84:87]
v_add_f32 v72,v220,v72
v_mfma_f32_16x16x32_bf16 v[4:7],v[188:191],a[212:215],v[4:7]
v_exp_f32 v221,v54
v_mfma_f32_16x16x32_bf16 v[8:11],v[184:187],a[224:227],v[88:91]
buffer_load_dwordx4 v237,s[44:47],s61 offen offset:2048 nt lds
v_add_f32 v73,v221,v73
v_mfma_f32_16x16x32_bf16 v[8:11],v[188:191],a[228:231],v[8:11]
v_exp_f32 v222,v51
v_mfma_f32_16x16x32_bf16 v[12:15],v[184:187],a[240:243],v[92:95]
v_add_f32 v72,v222,v72
v_cvt_pk_bf16_f32 v131,v220,v222
v_mfma_f32_16x16x32_bf16 v[12:15],v[188:191],a[244:247],v[12:15]
v_exp_f32 v223,v55
v_mfma_f32_16x16x32_bf16 v[64:67],v[184:187],a[160:163],v[96:99]
ds_read_b64_tr_b16 v[184:185],v230 offset:32768
ds_read_b64_tr_b16 v[186:187],v230 offset:36864
v_mfma_f32_16x16x32_bf16 v[64:67],v[188:191],a[164:167],v[64:67]
s_waitcnt lgkmcnt(6)
v_add_f32 v73,v223,v73
v_cvt_pk_bf16_f32 v135,v221,v223
v_mfma_f32_16x16x32_bf16 v[0:3],v[192:195],a[200:203],v[0:3]
ds_read_b64_tr_b16 v[188:189],v231 offset:32768
ds_read_b64_tr_b16 v[190:191],v231 offset:36864
v_mfma_f32_16x16x32_bf16 v[0:3],v[196:199],a[204:207],v[0:3]
v_exp_f32 v216,v56
v_mfma_f32_16x16x32_bf16 v[4:7],v[192:195],a[216:219],v[4:7]
v_max_f32 v228,v72,v73
v_add_f32 v74,v216,v74
v_mfma_f32_16x16x32_bf16 v[4:7],v[196:199],a[220:223],v[4:7]
v_exp_f32 v217,v60
v_mfma_f32_16x16x32_bf16 v[8:11],v[192:195],a[232:235],v[8:11]
buffer_load_dwordx4 v237,s[44:47],s61 offen offset:3072 nt lds
v_add_f32 v75,v217,v75
v_mfma_f32_16x16x32_bf16 v[8:11],v[196:199],a[236:239],v[8:11]
v_exp_f32 v218,v57
v_mfma_f32_16x16x32_bf16 v[12:15],v[192:195],a[248:251],v[12:15]
v_add_f32 v74,v218,v74
v_cvt_pk_bf16_f32 v138,v216,v218
v_mfma_f32_16x16x32_bf16 v[12:15],v[196:199],a[252:255],v[12:15]
v_exp_f32 v219,v61
v_mfma_f32_16x16x32_bf16 v[64:67],v[192:195],a[168:171],v[64:67]
s_add_u32 s62,s62,16384
v_add_f32 v75,v219,v75
v_cvt_pk_bf16_f32 v142,v217,v219
v_mfma_f32_16x16x32_bf16 v[64:67],v[196:199],a[172:175],v[64:67]
s_waitcnt lgkmcnt(6)
v_exp_f32 v220,v58
v_mfma_f32_16x16x32_bf16 v[16:19],v[200:203],a[192:195],v[80:83]
s_xor_b32 s61,s62,s75
ds_read_b64_tr_b16 v[192:193],v230 offset:33024
ds_read_b64_tr_b16 v[194:195],v230 offset:37120
v_mfma_f32_16x16x32_bf16 v[16:19],v[204:207],a[196:199],v[16:19]
s_add_u32 s56,s56,s57
ds_read_b64_tr_b16 v[196:197],v231 offset:33024
v_add_f32 v74,v220,v74
v_mfma_f32_16x16x32_bf16 v[20:23],v[200:203],a[208:211],v[84:87]
s_lshl_b32 s73,s28,2
s_add_u32 s73,s73,s60
ds_read_b64_tr_b16 v[198:199],v231 offset:37120
v_mfma_f32_16x16x32_bf16 v[20:23],v[204:207],a[212:215],v[20:23]
s_mul_i32 s73,s73,768
v_exp_f32 v221,v62
v_mfma_f32_16x16x32_bf16 v[24:27],v[200:203],a[224:227],v[88:91]
s_add_u32 s73,s73,.Litem0-.Lk1pc
v_add_f32 v75,v221,v75
v_min_u32 v255,640,v251
v_mfma_f32_16x16x32_bf16 v[24:27],v[204:207],a[228:231],v[24:27]
v_exp_f32 v222,v59
v_mfma_f32_16x16x32_bf16 v[28:31],v[200:203],a[240:243],v[92:95]
v_exp_f32 v223,v63
v_mfma_f32_16x16x32_bf16 v[28:31],v[204:207],a[244:247],v[28:31]
v_add_f32 v74,v222,v74
v_add_f32 v75,v223,v75
v_max3_f32 v228,v228,v74,v75
v_cmp_nge_f32 vcc,0x46800000,v228
v_cvt_pk_bf16_f32 v139,v220,v222
v_add_u32 v255,s73,v255
v_min_u32 v255,.Lend-.Lk1pc,v255
v_mfma_f32_16x16x32_bf16 v[68:71],v[200:203],a[160:163],v[96:99]
global_load_dword v253,v255,s[30:31]
v_cvt_pk_bf16_f32 v143,v221,v223
v_mfma_f32_16x16x32_bf16 v[68:71],v[204:207],a[164:167],v[68:71]
s_waitcnt lgkmcnt(8)
s_cbranch_vccnz .Lst13
.Lbk13:
ds_read_b64_tr_b16 v[200:201],v230 offset:33280
v_mfma_f32_16x16x32_bf16 v[16:19],v[208:211],a[200:203],v[16:19]
ds_read_b64_tr_b16 v[202:203],v230 offset:37376
ds_read_b64_tr_b16 v[204:205],v231 offset:33280
v_mfma_f32_16x16x32_bf16 v[16:19],v[212:215],a[204:207],v[16:19]
ds_read_b64_tr_b16 v[206:207],v231 offset:37376
v_add_f32 v104,v72,v104
v_mfma_f32_16x16x32_bf16 v[20:23],v[208:211],a[216:219],v[20:23]
v_exp_f32 v216,v0
v_mfma_f32_16x16x32_bf16 v[20:23],v[212:215],a[220:223],v[20:23]
v_exp_f32 v217,v4
v_mfma_f32_16x16x32_bf16 v[24:27],v[208:211],a[232:235],v[24:27]
v_add_f32 v105,v73,v105
v_mfma_f32_16x16x32_bf16 v[24:27],v[212:215],a[236:239],v[24:27]
v_exp_f32 v218,v1
v_mfma_f32_16x16x32_bf16 v[28:31],v[208:211],a[248:251],v[28:31]
v_add_f32 v72,v216,v218
v_cvt_pk_bf16_f32 v112,v216,v218
v_mfma_f32_16x16x32_bf16 v[28:31],v[212:215],a[252:255],v[28:31]
v_exp_f32 v219,v5
v_mfma_f32_16x16x32_bf16 v[68:71],v[208:211],a[168:171],v[68:71]
v_add_f32 v73,v217,v219
v_cvt_pk_bf16_f32 v116,v217,v219
v_mfma_f32_16x16x32_bf16 v[68:71],v[212:215],a[172:175],v[68:71]
s_waitcnt lgkmcnt(8)
v_exp_f32 v220,v2
v_mfma_f32_16x16x32_bf16 a[0:3],v[184:187],v[128:131],a[0:3]
ds_read_b64_tr_b16 v[208:209],v230 offset:33536
ds_read_b64_tr_b16 v[210:211],v230 offset:37632
v_mfma_f32_16x16x32_bf16 a[32:35],v[184:187],v[132:135],a[32:35]
v_exp_f32 v221,v6
v_mfma_f32_16x16x32_bf16 a[64:67],v[184:187],v[136:139],a[64:67]
ds_read_b64_tr_b16 v[212:213],v231 offset:33536
v_add_f32 v72,v220,v72
v_mfma_f32_16x16x32_bf16 a[96:99],v[184:187],v[140:143],a[96:99]
ds_read_b64_tr_b16 v[214:215],v231 offset:37632
v_add_f32 v73,v221,v73
v_mfma_f32_16x16x32_bf16 a[4:7],v[188:191],v[128:131],a[4:7]
v_exp_f32 v222,v3
v_mfma_f32_16x16x32_bf16 a[36:39],v[188:191],v[132:135],a[36:39]
v_add_f32 v72,v222,v72
v_cvt_pk_bf16_f32 v113,v220,v222
v_mfma_f32_16x16x32_bf16 a[68:71],v[188:191],v[136:139],a[68:71]
v_exp_f32 v223,v7
v_mfma_f32_16x16x32_bf16 a[100:103],v[188:191],v[140:143],a[100:103]
s_waitcnt lgkmcnt(8)
ds_read_b128 v[184:187],v226
v_add_f32 v73,v223,v73
v_mfma_f32_16x16x32_bf16 a[8:11],v[192:195],v[128:131],a[8:11]
v_cvt_pk_bf16_f32 v117,v221,v223
v_add_f32 v106,v74,v106
v_mfma_f32_16x16x32_bf16 a[40:43],v[192:195],v[132:135],a[40:43]
v_exp_f32 v216,v8
v_mfma_f32_16x16x32_bf16 a[72:75],v[192:195],v[136:139],a[72:75]
v_exp_f32 v217,v12
v_add_f32 v107,v75,v107
v_mfma_f32_16x16x32_bf16 a[104:107],v[192:195],v[140:143],a[104:107]
ds_read_b128 v[188:191],v226 offset:256
ds_read_b128 v[192:195],v226 offset:512
v_mfma_f32_16x16x32_bf16 a[12:15],v[196:199],v[128:131],a[12:15]
v_exp_f32 v218,v9
v_mfma_f32_16x16x32_bf16 a[44:47],v[196:199],v[132:135],a[44:47]
v_add_f32 v74,v216,v218
v_cvt_pk_bf16_f32 v120,v216,v218
v_mfma_f32_16x16x32_bf16 a[76:79],v[196:199],v[136:139],a[76:79]
v_exp_f32 v219,v13
v_mfma_f32_16x16x32_bf16 a[108:111],v[196:199],v[140:143],a[108:111]
s_waitcnt lgkmcnt(7)
v_add_f32 v75,v217,v219
v_cvt_pk_bf16_f32 v124,v217,v219
v_mfma_f32_16x16x32_bf16 a[16:19],v[200:203],v[128:131],a[16:19]
v_exp_f32 v220,v10
v_mfma_f32_16x16x32_bf16 a[48:51],v[200:203],v[132:135],a[48:51]
v_exp_f32 v221,v14
v_add_f32 v74,v220,v74
v_mfma_f32_16x16x32_bf16 a[80:83],v[200:203],v[136:139],a[80:83]
ds_read_b128 v[196:199],v226 offset:768
v_add_f32 v75,v221,v75
v_mfma_f32_16x16x32_bf16 a[112:115],v[200:203],v[140:143],a[112:115]
v_exp_f32 v222,v11
v_mfma_f32_16x16x32_bf16 a[20:23],v[204:207],v[128:131],a[20:23]
v_add_f32 v74,v222,v74
v_cvt_pk_bf16_f32 v121,v220,v222
v_mfma_f32_16x16x32_bf16 a[52:55],v[204:207],v[132:135],a[52:55]
v_exp_f32 v223,v15
v_mfma_f32_16x16x32_bf16 a[84:87],v[204:207],v[136:139],a[84:87]
v_add_f32 v75,v223,v75
v_cvt_pk_bf16_f32 v125,v221,v223
)A"
R"A(v_mfma_f32_16x16x32_bf16 a[116:119],v[204:207],v[140:143],a[116:119]
s_waitcnt lgkmcnt(4)
v_exp_f32 v216,v64
v_add_f32 v108,v76,v108
v_mfma_f32_16x16x32_bf16 a[24:27],v[208:211],v[128:131],a[24:27]
ds_read_b128 v[200:203],v226 offset:4096
ds_read_b128 v[204:207],v226 offset:4352
v_exp_f32 v217,v65
v_mfma_f32_16x16x32_bf16 a[56:59],v[208:211],v[132:135],a[56:59]
v_add_f32 v76,v216,v217
v_cvt_pk_bf16_f32 v144,v216,v217
v_mfma_f32_16x16x32_bf16 a[88:91],v[208:211],v[136:139],a[88:91]
v_exp_f32 v218,v66
v_mfma_f32_16x16x32_bf16 a[120:123],v[208:211],v[140:143],a[120:123]
ds_read_b128 v[208:211],v226 offset:4608
v_add_f32 v76,v218,v76
v_mfma_f32_16x16x32_bf16 a[28:31],v[212:215],v[128:131],a[28:31]
v_exp_f32 v219,v67
v_mfma_f32_16x16x32_bf16 a[60:63],v[212:215],v[132:135],a[60:63]
v_add_f32 v76,v219,v76
v_cvt_pk_bf16_f32 v145,v218,v219
v_mfma_f32_16x16x32_bf16 a[92:95],v[212:215],v[136:139],a[92:95]
v_exp_f32 v220,v16
v_mfma_f32_16x16x32_bf16 a[124:127],v[212:215],v[140:143],a[124:127]
s_waitcnt lgkmcnt(5)
v_exp_f32 v221,v20
v_add_f32 v72,v220,v72
v_mfma_f32_16x16x32_bf16 v[32:35],v[184:187],a[192:195],v[80:83]
ds_read_b128 v[212:215],v226 offset:4864
v_add_f32 v73,v221,v73
v_mfma_f32_16x16x32_bf16 v[32:35],v[188:191],a[196:199],v[32:35]
v_exp_f32 v222,v17
v_mfma_f32_16x16x32_bf16 v[36:39],v[184:187],a[208:211],v[84:87]
v_add_f32 v72,v222,v72
v_cvt_pk_bf16_f32 v114,v220,v222
v_mfma_f32_16x16x32_bf16 v[36:39],v[188:191],a[212:215],v[36:39]
v_exp_f32 v223,v21
v_mfma_f32_16x16x32_bf16 v[40:43],v[184:187],a[224:227],v[88:91]
v_add_f32 v73,v223,v73
v_cvt_pk_bf16_f32 v118,v221,v223
v_mfma_f32_16x16x32_bf16 v[40:43],v[188:191],a[228:231],v[40:43]
v_exp_f32 v216,v18
v_mfma_f32_16x16x32_bf16 v[44:47],v[184:187],a[240:243],v[92:95]
v_exp_f32 v217,v22
v_mfma_f32_16x16x32_bf16 v[44:47],v[188:191],a[244:247],v[44:47]
s_waitcnt lgkmcnt(4)
v_add_f32 v72,v216,v72
v_add_f32 v73,v217,v73
v_mfma_f32_16x16x32_bf16 v[32:35],v[192:195],a[200:203],v[32:35]
v_exp_f32 v218,v19
v_mfma_f32_16x16x32_bf16 v[32:35],v[196:199],a[204:207],v[32:35]
v_add_f32 v72,v218,v72
v_cvt_pk_bf16_f32 v115,v216,v218
v_mfma_f32_16x16x32_bf16 v[36:39],v[192:195],a[216:219],v[36:39]
v_exp_f32 v219,v23
v_mfma_f32_16x16x32_bf16 v[36:39],v[196:199],a[220:223],v[36:39]
ds_read_b64_tr_b16 v[184:185],v233 offset:49152
ds_read_b64_tr_b16 v[186:187],v233 offset:53248
v_add_f32 v73,v219,v73
v_cvt_pk_bf16_f32 v119,v217,v219
v_mfma_f32_16x16x32_bf16 v[40:43],v[192:195],a[232:235],v[40:43]
ds_read_b64_tr_b16 v[188:189],v234 offset:49152
ds_read_b64_tr_b16 v[190:191],v234 offset:53248
v_mfma_f32_16x16x32_bf16 v[40:43],v[196:199],a[236:239],v[40:43]
s_cmp_eq_u32 s56,32768
s_cselect_b32 s57,0xffff8000,s58
v_exp_f32 v220,v24
v_max_f32 v228,v72,v73
v_exp_f32 v221,v28
v_mfma_f32_16x16x32_bf16 v[44:47],v[192:195],a[248:251],v[44:47]
v_add_f32 v74,v220,v74
v_add_f32 v75,v221,v75
v_mfma_f32_16x16x32_bf16 v[44:47],v[196:199],a[252:255],v[44:47]
s_waitcnt lgkmcnt(6)
v_exp_f32 v222,v25
v_add_u32 v232,s57,v232
v_mfma_f32_16x16x32_bf16 v[48:51],v[200:203],a[192:195],v[80:83]
v_add_f32 v74,v222,v74
v_cvt_pk_bf16_f32 v122,v220,v222
v_mfma_f32_16x16x32_bf16 v[48:51],v[204:207],a[196:199],v[48:51]
v_exp_f32 v223,v29
v_add_u32 v226,s57,v226
v_mfma_f32_16x16x32_bf16 v[52:55],v[200:203],a[208:211],v[84:87]
v_add_f32 v75,v223,v75
v_cvt_pk_bf16_f32 v126,v221,v223
v_mfma_f32_16x16x32_bf16 v[52:55],v[204:207],a[212:215],v[52:55]
ds_read_b64_tr_b16 v[192:193],v233 offset:49408
ds_read_b64_tr_b16 v[194:195],v233 offset:53504
v_exp_f32 v216,v26
v_mfma_f32_16x16x32_bf16 v[56:59],v[200:203],a[224:227],v[88:91]
ds_read_b64_tr_b16 v[196:197],v234 offset:49408
ds_read_b64_tr_b16 v[198:199],v234 offset:53504
v_mfma_f32_16x16x32_bf16 v[56:59],v[204:207],a[228:231],v[56:59]
v_exp_f32 v217,v30
v_add_f32 v74,v216,v74
v_mfma_f32_16x16x32_bf16 v[60:63],v[200:203],a[240:243],v[92:95]
ds_read_b64_tr_b16 v[200:201],v233 offset:49664
v_add_f32 v75,v217,v75
v_mfma_f32_16x16x32_bf16 v[60:63],v[204:207],a[244:247],v[60:63]
s_waitcnt lgkmcnt(9)
v_exp_f32 v218,v27
v_mfma_f32_16x16x32_bf16 v[48:51],v[208:211],a[200:203],v[48:51]
v_add_f32 v74,v218,v74
v_cvt_pk_bf16_f32 v123,v216,v218
v_mfma_f32_16x16x32_bf16 v[48:51],v[212:215],a[204:207],v[48:51]
v_exp_f32 v219,v31
v_mfma_f32_16x16x32_bf16 v[52:55],v[208:211],a[216:219],v[52:55]
v_add_f32 v75,v219,v75
v_cvt_pk_bf16_f32 v127,v217,v219
v_mfma_f32_16x16x32_bf16 v[52:55],v[212:215],a[220:223],v[52:55]
ds_read_b64_tr_b16 v[202:203],v233 offset:53760
v_exp_f32 v220,v68
v_max3_f32 v228,v228,v74,v75
v_exp_f32 v221,v69
v_mfma_f32_16x16x32_bf16 v[56:59],v[208:211],a[232:235],v[56:59]
v_exp_f32 v222,v70
v_exp_f32 v223,v71
v_mfma_f32_16x16x32_bf16 v[56:59],v[212:215],a[236:239],v[56:59]
ds_read_b64_tr_b16 v[204:205],v234 offset:49664
ds_read_b64_tr_b16 v[206:207],v234 offset:53760
v_add_f32 v76,v220,v76
v_add_f32 v76,v221,v76
v_cvt_pk_bf16_f32 v146,v220,v221
v_add_f32 v76,v222,v76
v_add_f32 v76,v223,v76
v_max_f32 v228,v228,v76
v_cmp_nge_f32 vcc,0x46800000,v228
v_mfma_f32_16x16x32_bf16 v[60:63],v[208:211],a[248:251],v[60:63]
ds_read_b64_tr_b16 v[208:209],v233 offset:49920
v_cvt_pk_bf16_f32 v147,v222,v223
v_mfma_f32_16x16x32_bf16 v[60:63],v[212:215],a[252:255],v[60:63]
s_waitcnt lgkmcnt(9)
s_cbranch_vccnz .Lst14
.Lbk14:
ds_read_b64_tr_b16 v[210:211],v233 offset:54016
v_mfma_f32_16x16x32_bf16 a[0:3],v[184:187],v[112:115],a[0:3]
s_waitcnt vmcnt(9)
s_barrier
ds_read_b64_tr_b16 v[212:213],v234 offset:49920
v_mfma_f32_16x16x32_bf16 a[32:35],v[184:187],v[116:119],a[32:35]
s_add_u32 m0,s63,98304
ds_read_b64_tr_b16 v[214:215],v234 offset:54016
v_add_f32 v104,v72,v104
v_mfma_f32_16x16x32_bf16 a[64:67],v[184:187],v[120:123],a[64:67]
v_exp_f32 v216,v32
v_mfma_f32_16x16x32_bf16 a[96:99],v[184:187],v[124:127],a[96:99]
v_add_u32 v255,s74,v239
v_add_f32 v241,v76,v108
v_mfma_f32_16x16x32_bf16 a[128:131],v[184:187],v[144:147],a[128:131]
buffer_load_dwordx4 v235,s[48:51],s61 offen nt lds
v_mov_b32 v240,v96
v_mfma_f32_16x16x32_bf16 a[4:7],v[188:191],v[112:115],a[4:7]
ds_write_b64 v227,v[240:241]
v_mfma_f32_16x16x32_bf16 a[36:39],v[188:191],v[116:119],a[36:39]
v_exp_f32 v217,v36
v_mfma_f32_16x16x32_bf16 a[68:71],v[188:191],v[120:123],a[68:71]
ds_read_b128 a[192:195],v255
v_add_f32 v105,v73,v105
v_mfma_f32_16x16x32_bf16 a[100:103],v[188:191],v[124:127],a[100:103]
ds_read_b128 v[184:187],v232
ds_read_b128 a[208:211],v255 offset:4096
v_mfma_f32_16x16x32_bf16 a[132:135],v[188:191],v[144:147],a[132:135]
s_waitcnt lgkmcnt(12)
v_exp_f32 v218,v33
v_mfma_f32_16x16x32_bf16 a[8:11],v[192:195],v[112:115],a[8:11]
v_add_f32 v72,v216,v218
v_cvt_pk_bf16_f32 v128,v216,v218
v_mfma_f32_16x16x32_bf16 a[40:43],v[192:195],v[116:119],a[40:43]
v_exp_f32 v219,v37
v_mfma_f32_16x16x32_bf16 a[72:75],v[192:195],v[120:123],a[72:75]
ds_read_b128 a[160:163],v252 offset:8192
v_add_f32 v73,v217,v219
v_mfma_f32_16x16x32_bf16 a[104:107],v[192:195],v[124:127],a[104:107]
ds_read_b128 v[188:191],v232 offset:256
v_cvt_pk_bf16_f32 v132,v217,v219
v_mfma_f32_16x16x32_bf16 a[136:139],v[192:195],v[144:147],a[136:139]
buffer_load_dwordx4 v236,s[48:51],s61 offen offset:1024 nt lds
ds_read_b128 a[196:199],v255 offset:1024
v_exp_f32 v220,v34
v_mfma_f32_16x16x32_bf16 a[12:15],v[196:199],v[112:115],a[12:15]
ds_read_b128 a[212:215],v255 offset:5120
v_add_f32 v72,v220,v72
v_mfma_f32_16x16x32_bf16 a[44:47],v[196:199],v[116:119],a[44:47]
v_exp_f32 v221,v38
v_mfma_f32_16x16x32_bf16 a[76:79],v[196:199],v[120:123],a[76:79]
ds_read_b128 a[164:167],v252 offset:9216
v_add_f32 v73,v221,v73
v_mfma_f32_16x16x32_bf16 a[108:111],v[196:199],v[124:127],a[108:111]
ds_read_b128 v[192:195],v232 offset:512
ds_read_b128 a[200:203],v255 offset:2048
v_mfma_f32_16x16x32_bf16 a[140:143],v[196:199],v[144:147],a[140:143]
s_waitcnt lgkmcnt(15)
v_exp_f32 v222,v35
v_mfma_f32_16x16x32_bf16 a[16:19],v[200:203],v[112:115],a[16:19]
v_add_f32 v72,v222,v72
v_cvt_pk_bf16_f32 v129,v220,v222
v_mfma_f32_16x16x32_bf16 a[48:51],v[200:203],v[116:119],a[48:51]
v_exp_f32 v223,v39
v_mfma_f32_16x16x32_bf16 a[80:83],v[200:203],v[120:123],a[80:83]
buffer_load_dwordx4 v235,s[48:51],s61 offen offset:2048 nt lds
ds_read_b128 a[216:219],v255 offset:6144
v_add_f32 v73,v223,v73
v_cvt_pk_bf16_f32 v133,v221,v223
v_mfma_f32_16x16x32_bf16 a[112:115],v[200:203],v[124:127],a[112:115]
ds_read_b128 v[196:199],v232 offset:768
v_add_f32 v106,v74,v106
v_mfma_f32_16x16x32_bf16 a[144:147],v[200:203],v[144:147],a[144:147]
ds_read_b128 a[168:171],v252 offset:10240
v_exp_f32 v216,v40
v_mfma_f32_16x16x32_bf16 a[20:23],v[204:207],v[112:115],a[20:23]
v_exp_f32 v217,v44
v_mfma_f32_16x16x32_bf16 a[52:55],v[204:207],v[116:119],a[52:55]
ds_read_b128 a[204:207],v255 offset:3072
v_add_f32 v107,v75,v107
v_mfma_f32_16x16x32_bf16 a[84:87],v[204:207],v[120:123],a[84:87]
v_exp_f32 v218,v41
v_mfma_f32_16x16x32_bf16 a[116:119],v[204:207],v[124:127],a[116:119]
ds_read_b128 a[220:223],v255 offset:7168
ds_read_b128 v[200:203],v232 offset:4096
v_mfma_f32_16x16x32_bf16 a[148:151],v[204:207],v[144:147],a[148:151]
s_waitcnt lgkmcnt(15)
v_add_f32 v74,v216,v218
v_cvt_pk_bf16_f32 v136,v216,v218
v_mfma_f32_16x16x32_bf16 a[24:27],v[208:211],v[112:115],a[24:27]
ds_read_b128 a[172:175],v252 offset:11264
buffer_load_dwordx4 v236,s[48:51],s61 offen offset:3072 nt lds
v_accvgpr_write_b32 a224,v152
v_accvgpr_write_b32 a225,v153
v_accvgpr_write_b32 a226,v154
v_accvgpr_write_b32 a227,v155
v_accvgpr_write_b32 a240,v168
v_accvgpr_write_b32 a241,v169
v_accvgpr_write_b32 a242,v170
v_accvgpr_write_b32 a243,v171
v_accvgpr_write_b32 a228,v156
v_accvgpr_write_b32 a229,v157
v_accvgpr_write_b32 a230,v158
v_accvgpr_write_b32 a231,v159
v_accvgpr_write_b32 a244,v172
v_accvgpr_write_b32 a245,v173
v_accvgpr_write_b32 a246,v174
v_accvgpr_write_b32 a247,v175
v_accvgpr_write_b32 a232,v160
v_accvgpr_write_b32 a233,v161
v_mfma_f32_16x16x32_bf16 a[56:59],v[208:211],v[116:119],a[56:59]
s_add_u32 m0,s56,s63
v_exp_f32 v219,v45
v_accvgpr_write_b32 a234,v162
v_mfma_f32_16x16x32_bf16 a[88:91],v[208:211],v[120:123],a[88:91]
v_add_f32 v75,v217,v219
v_cvt_pk_bf16_f32 v140,v217,v219
v_accvgpr_write_b32 a235,v163
v_mfma_f32_16x16x32_bf16 a[120:123],v[208:211],v[124:127],a[120:123]
v_exp_f32 v220,v42
v_accvgpr_write_b32 a248,v176
v_mfma_f32_16x16x32_bf16 a[152:155],v[208:211],v[144:147],a[152:155]
ds_read_b128 v[204:207],v232 offset:4352
v_add_f32 v74,v220,v74
v_accvgpr_write_b32 a249,v177
v_mfma_f32_16x16x32_bf16 a[28:31],v[212:215],v[112:115],a[28:31]
v_exp_f32 v221,v46
v_accvgpr_write_b32 a250,v178
v_mfma_f32_16x16x32_bf16 a[60:63],v[212:215],v[116:119],a[60:63]
v_add_f32 v75,v221,v75
v_accvgpr_write_b32 a251,v179
v_mfma_f32_16x16x32_bf16 a[92:95],v[212:215],v[120:123],a[92:95]
buffer_load_dwordx4 v235,s[44:47],s61 offen nt lds
v_accvgpr_write_b32 a236,v164
v_mfma_f32_16x16x32_bf16 a[124:127],v[212:215],v[124:127],a[124:127]
v_exp_f32 v222,v43
v_accvgpr_write_b32 a237,v165
v_mfma_f32_16x16x32_bf16 a[156:159],v[212:215],v[144:147],a[156:159]
s_waitcnt lgkmcnt(1)
ds_read_b128 v[208:211],v232 offset:4608
v_add_f32 v74,v222,v74
v_cvt_pk_bf16_f32 v137,v220,v222
v_accvgpr_write_b32 a238,v166
v_mfma_f32_16x16x32_bf16 v[0:3],v[184:187],a[192:195],0
v_exp_f32 v223,v47
v_mfma_f32_16x16x32_bf16 v[0:3],v[188:191],a[196:199],v[0:3]
v_accvgpr_write_b32 a239,v167
v_add_f32 v75,v223,v75
v_cvt_pk_bf16_f32 v141,v221,v223
v_accvgpr_write_b32 a252,v180
v_mfma_f32_16x16x32_bf16 v[4:7],v[184:187],a[208:211],0
v_accvgpr_write_b32 a253,v181
v_accvgpr_write_b32 a254,v182
v_mfma_f32_16x16x32_bf16 v[4:7],v[188:191],a[212:215],v[4:7]
v_exp_f32 v216,v48
v_mfma_f32_16x16x32_bf16 v[8:11],v[184:187],a[224:227],0
ds_read_b128 v[212:215],v232 offset:4864
v_add_f32 v72,v216,v72
v_mfma_f32_16x16x32_bf16 v[8:11],v[188:191],a[228:231],v[8:11]
buffer_load_dwordx4 v235,s[44:47],s61 offen offset:1024 nt lds
v_accvgpr_write_b32 a255,v183
v_exp_f32 v217,v52
v_mfma_f32_16x16x32_bf16 v[12:15],v[184:187],a[240:243],0
buffer_load_dwordx4 v237,s[44:47],s61 offen offset:2048 nt lds
v_add_f32 v73,v217,v73
v_mfma_f32_16x16x32_bf16 v[12:15],v[188:191],a[244:247],v[12:15]
v_exp_f32 v218,v49
v_mfma_f32_16x16x32_bf16 v[64:67],v[184:187],a[160:163],0
)A"
R"A(v_add_f32 v72,v218,v72
v_cvt_pk_bf16_f32 v130,v216,v218
v_mfma_f32_16x16x32_bf16 v[64:67],v[188:191],a[164:167],v[64:67]
ds_write_b128 v255,a[128:131]
v_exp_f32 v219,v53
v_mfma_f32_16x16x32_bf16 v[0:3],v[192:195],a[200:203],v[0:3]
v_add_f32 v73,v219,v73
v_cvt_pk_bf16_f32 v134,v217,v219
v_mfma_f32_16x16x32_bf16 v[0:3],v[196:199],a[204:207],v[0:3]
v_exp_f32 v220,v50
v_mfma_f32_16x16x32_bf16 v[4:7],v[192:195],a[216:219],v[4:7]
ds_read_b64_tr_b16 v[184:185],v230 offset:49152
ds_read_b64_tr_b16 v[186:187],v230 offset:53248
v_mfma_f32_16x16x32_bf16 v[4:7],v[196:199],a[220:223],v[4:7]
ds_read_b64_tr_b16 v[188:189],v231 offset:49152
v_add_f32 v72,v220,v72
v_mfma_f32_16x16x32_bf16 v[8:11],v[192:195],a[232:235],v[8:11]
ds_read_b64_tr_b16 v[190:191],v231 offset:53248
buffer_load_dwordx4 v237,s[44:47],s61 offen offset:3072 nt lds
v_mfma_f32_16x16x32_bf16 v[8:11],v[196:199],a[236:239],v[8:11]
v_exp_f32 v221,v54
v_mfma_f32_16x16x32_bf16 v[12:15],v[192:195],a[248:251],v[12:15]
v_add_f32 v73,v221,v73
v_mfma_f32_16x16x32_bf16 v[12:15],v[196:199],a[252:255],v[12:15]
v_exp_f32 v222,v51
v_mfma_f32_16x16x32_bf16 v[64:67],v[192:195],a[168:171],v[64:67]
s_add_u32 s62,s62,16384
v_add_f32 v72,v222,v72
v_cvt_pk_bf16_f32 v131,v220,v222
v_mfma_f32_16x16x32_bf16 v[64:67],v[196:199],a[172:175],v[64:67]
s_waitcnt lgkmcnt(7)
v_exp_f32 v223,v55
v_mfma_f32_16x16x32_bf16 v[16:19],v[200:203],a[192:195],0
s_xor_b32 s61,s62,s75
v_add_f32 v73,v223,v73
v_cvt_pk_bf16_f32 v135,v221,v223
v_mfma_f32_16x16x32_bf16 v[16:19],v[204:207],a[196:199],v[16:19]
s_add_u32 s56,s56,s57
ds_read_b64_tr_b16 v[192:193],v230 offset:49408
ds_write_b128 v255,a[132:135] offset:1024
v_max_f32 v228,v72,v73
v_mfma_f32_16x16x32_bf16 v[20:23],v[200:203],a[208:211],0
v_exp_f32 v216,v56
v_mfma_f32_16x16x32_bf16 v[20:23],v[204:207],a[212:215],v[20:23]
ds_read_b64_tr_b16 v[194:195],v230 offset:53504
v_add_f32 v74,v216,v74
v_mfma_f32_16x16x32_bf16 v[24:27],v[200:203],a[224:227],0
ds_read_b64_tr_b16 v[196:197],v231 offset:49408
ds_read_b64_tr_b16 v[198:199],v231 offset:53504
v_mfma_f32_16x16x32_bf16 v[24:27],v[204:207],a[228:231],v[24:27]
v_exp_f32 v217,v60
v_mfma_f32_16x16x32_bf16 v[28:31],v[200:203],a[240:243],0
v_add_f32 v75,v217,v75
v_mfma_f32_16x16x32_bf16 v[28:31],v[204:207],a[244:247],v[28:31]
v_exp_f32 v218,v57
v_mfma_f32_16x16x32_bf16 v[68:71],v[200:203],a[160:163],0
v_add_f32 v74,v218,v74
v_cvt_pk_bf16_f32 v138,v216,v218
v_mfma_f32_16x16x32_bf16 v[68:71],v[204:207],a[164:167],v[68:71]
s_waitcnt lgkmcnt(10)
v_exp_f32 v219,v61
v_mfma_f32_16x16x32_bf16 v[16:19],v[208:211],a[200:203],v[16:19]
v_add_f32 v75,v219,v75
v_cvt_pk_bf16_f32 v142,v217,v219
v_mfma_f32_16x16x32_bf16 v[16:19],v[212:215],a[204:207],v[16:19]
v_exp_f32 v220,v58
v_mfma_f32_16x16x32_bf16 v[20:23],v[208:211],a[216:219],v[20:23]
ds_read_b64_tr_b16 v[200:201],v230 offset:49664
v_add_f32 v74,v220,v74
v_mfma_f32_16x16x32_bf16 v[20:23],v[212:215],a[220:223],v[20:23]
ds_read_b64_tr_b16 v[202:203],v230 offset:53760
ds_read_b64_tr_b16 v[204:205],v231 offset:49664
ds_write_b128 v255,a[136:139] offset:2048
v_exp_f32 v221,v62
v_mfma_f32_16x16x32_bf16 v[24:27],v[208:211],a[232:235],v[24:27]
ds_read_b64_tr_b16 v[206:207],v231 offset:53760
v_add_f32 v75,v221,v75
v_mfma_f32_16x16x32_bf16 v[24:27],v[212:215],a[236:239],v[24:27]
v_exp_f32 v222,v59
v_mfma_f32_16x16x32_bf16 v[28:31],v[208:211],a[248:251],v[28:31]
v_exp_f32 v223,v63
v_mfma_f32_16x16x32_bf16 v[28:31],v[212:215],a[252:255],v[28:31]
v_add_f32 v74,v222,v74
v_add_f32 v75,v223,v75
v_max3_f32 v228,v228,v74,v75
v_cmp_nge_f32 vcc,0x46800000,v228
v_cvt_pk_bf16_f32 v139,v220,v222
v_mfma_f32_16x16x32_bf16 v[68:71],v[208:211],a[168:171],v[68:71]
ds_read_b64_tr_b16 v[208:209],v230 offset:49920
v_cvt_pk_bf16_f32 v143,v221,v223
v_mfma_f32_16x16x32_bf16 v[68:71],v[212:215],a[172:175],v[68:71]
s_waitcnt lgkmcnt(11)
s_cbranch_vccnz .Lst15
.Lbk15:
ds_read_b64_tr_b16 v[210:211],v230 offset:54016
v_mfma_f32_16x16x32_bf16 a[0:3],v[184:187],v[128:131],a[0:3]
ds_read_b64_tr_b16 v[212:213],v231 offset:49920
ds_read_b64_tr_b16 v[214:215],v231 offset:54016
v_mfma_f32_16x16x32_bf16 a[32:35],v[184:187],v[132:135],a[32:35]
v_add_f32 v104,v72,v104
v_add_f32 v105,v73,v105
v_mfma_f32_16x16x32_bf16 a[64:67],v[184:187],v[136:139],a[64:67]
v_add_f32 v106,v74,v106
v_add_f32 v107,v75,v107
v_mfma_f32_16x16x32_bf16 a[96:99],v[184:187],v[140:143],a[96:99]
v_mov_b32 v72,v104
v_mov_b32 v73,v105
v_mfma_f32_16x16x32_bf16 a[4:7],v[188:191],v[128:131],a[4:7]
v_mov_b32 v74,v106
v_mov_b32 v75,v107
v_mfma_f32_16x16x32_bf16 a[36:39],v[188:191],v[132:135],a[36:39]
ds_write_b128 v255,a[140:143] offset:3072
v_permlane32_swap_b32 v72,v104
v_mfma_f32_16x16x32_bf16 a[68:71],v[188:191],v[136:139],a[68:71]
v_permlane32_swap_b32 v73,v105
v_mfma_f32_16x16x32_bf16 a[100:103],v[188:191],v[140:143],a[100:103]
s_waitcnt lgkmcnt(10)
ds_read_b128 v[184:187],v226
ds_read_b128 v[188:191],v226 offset:256
v_mfma_f32_16x16x32_bf16 a[8:11],v[192:195],v[128:131],a[8:11]
v_permlane32_swap_b32 v74,v106
v_permlane32_swap_b32 v75,v107
v_mfma_f32_16x16x32_bf16 a[40:43],v[192:195],v[132:135],a[40:43]
v_add_f32 v104,v104,v72
v_add_f32 v105,v105,v73
v_add_f32 v106,v106,v74
v_add_f32 v107,v107,v75
v_mfma_f32_16x16x32_bf16 a[72:75],v[192:195],v[136:139],a[72:75]
v_mov_b32 v72,v104
v_mov_b32 v73,v105
v_mov_b32 v74,v106
v_mov_b32 v75,v107
v_mfma_f32_16x16x32_bf16 a[104:107],v[192:195],v[140:143],a[104:107]
v_permlane16_swap_b32 v72,v104
v_permlane16_swap_b32 v73,v105
v_mfma_f32_16x16x32_bf16 a[12:15],v[196:199],v[128:131],a[12:15]
v_permlane16_swap_b32 v74,v106
v_permlane16_swap_b32 v75,v107
v_mfma_f32_16x16x32_bf16 a[44:47],v[196:199],v[132:135],a[44:47]
v_add_f32 v104,v104,v72
v_add_f32 v105,v105,v73
v_add_f32 v106,v106,v74
v_mfma_f32_16x16x32_bf16 a[76:79],v[196:199],v[136:139],a[76:79]
ds_read_b128 v[192:195],v226 offset:512
v_add_f32 v107,v107,v75
v_rcp_f32 v104,v104
v_rcp_f32 v105,v105
v_mfma_f32_16x16x32_bf16 a[108:111],v[196:199],v[140:143],a[108:111]
s_waitcnt lgkmcnt(8)
v_rcp_f32 v106,v106
v_rcp_f32 v107,v107
v_mfma_f32_16x16x32_bf16 a[16:19],v[200:203],v[128:131],a[16:19]
v_max3_f32 v72,v0,v1,v2
v_max3_f32 v73,v4,v5,v6
v_max3_f32 v74,v8,v9,v10
v_mfma_f32_16x16x32_bf16 a[48:51],v[200:203],v[132:135],a[48:51]
ds_write_b128 v255,a[144:147] offset:4096
v_max3_f32 v75,v12,v13,v14
v_max3_f32 v76,v64,v65,v66
v_max3_f32 v72,v72,v3,v16
v_max3_f32 v73,v73,v7,v20
v_mfma_f32_16x16x32_bf16 a[80:83],v[200:203],v[136:139],a[80:83]
v_max3_f32 v74,v74,v11,v24
v_max3_f32 v75,v75,v15,v28
v_max3_f32 v76,v76,v67,v68
v_max3_f32 v72,v72,v17,v18
v_mfma_f32_16x16x32_bf16 a[112:115],v[200:203],v[140:143],a[112:115]
v_max3_f32 v73,v73,v21,v22
v_max3_f32 v74,v74,v25,v26
v_max3_f32 v75,v75,v29,v30
v_max3_f32 v76,v76,v69,v70
v_mfma_f32_16x16x32_bf16 a[20:23],v[204:207],v[128:131],a[20:23]
ds_read_b128 v[196:199],v226 offset:768
v_max_f32 v72,v72,v19
v_max_f32 v73,v73,v23
v_max_f32 v74,v74,v27
v_mfma_f32_16x16x32_bf16 a[52:55],v[204:207],v[132:135],a[52:55]
v_max_f32 v75,v75,v31
v_max_f32 v76,v76,v71
v_mov_b32 v216,v72
v_mov_b32 v217,v73
v_mfma_f32_16x16x32_bf16 a[84:87],v[204:207],v[136:139],a[84:87]
v_mov_b32 v218,v74
v_mov_b32 v219,v75
v_mov_b32 v220,v76
v_permlane32_swap_b32 v216,v72
v_mfma_f32_16x16x32_bf16 a[116:119],v[204:207],v[140:143],a[116:119]
s_waitcnt lgkmcnt(6)
v_permlane32_swap_b32 v217,v73
v_permlane32_swap_b32 v218,v74
v_mfma_f32_16x16x32_bf16 a[24:27],v[208:211],v[128:131],a[24:27]
ds_read_b128 v[200:203],v226 offset:4096
ds_read_b128 v[204:207],v226 offset:4352
v_permlane32_swap_b32 v219,v75
v_permlane32_swap_b32 v220,v76
v_mfma_f32_16x16x32_bf16 a[56:59],v[208:211],v[132:135],a[56:59]
v_max_f32 v72,v72,v216
v_max_f32 v73,v73,v217
v_max_f32 v74,v74,v218
v_mfma_f32_16x16x32_bf16 a[88:91],v[208:211],v[136:139],a[88:91]
v_max_f32 v75,v75,v219
v_max_f32 v76,v76,v220
v_mov_b32 v216,v72
v_mov_b32 v217,v73
v_mfma_f32_16x16x32_bf16 a[120:123],v[208:211],v[140:143],a[120:123]
v_mov_b32 v218,v74
v_mov_b32 v219,v75
v_mov_b32 v220,v76
v_permlane16_swap_b32 v216,v72
v_mfma_f32_16x16x32_bf16 a[28:31],v[212:215],v[128:131],a[28:31]
v_permlane16_swap_b32 v217,v73
v_mfma_f32_16x16x32_bf16 a[60:63],v[212:215],v[132:135],a[60:63]
ds_write_b128 v255,a[148:151] offset:5120
v_permlane16_swap_b32 v218,v74
v_permlane16_swap_b32 v219,v75
v_mfma_f32_16x16x32_bf16 a[92:95],v[212:215],v[136:139],a[92:95]
v_permlane16_swap_b32 v220,v76
v_max_f32 v72,v72,v216
v_max_f32 v73,v73,v217
v_mfma_f32_16x16x32_bf16 a[124:127],v[212:215],v[140:143],a[124:127]
s_waitcnt lgkmcnt(6)
v_max_f32 v74,v74,v218
v_max_f32 v75,v75,v219
v_max_f32 v76,v76,v220
v_sub_f32 v80,0,v72
v_sub_f32 v84,0,v73
v_sub_f32 v88,0,v74
v_sub_f32 v92,0,v75
v_sub_f32 v96,0,v76
v_mov_b32 v81,v80
v_mov_b32 v85,v84
v_mov_b32 v89,v88
v_mov_b32 v93,v92
v_mov_b32 v97,v96
v_mov_b32 v82,v80
v_mov_b32 v86,v84
v_mov_b32 v90,v88
v_mov_b32 v94,v92
v_mov_b32 v98,v96
v_mov_b32 v83,v80
v_mov_b32 v87,v84
v_mov_b32 v91,v88
v_mov_b32 v95,v92
v_mov_b32 v99,v96
v_mfma_f32_16x16x32_bf16 v[32:35],v[184:187],a[192:195],v[80:83]
ds_read_b128 v[208:211],v226 offset:4608
ds_read_b128 v[212:215],v226 offset:4864
v_mfma_f32_16x16x32_bf16 v[32:35],v[188:191],a[196:199],v[32:35]
v_add_f32 v0,v80,v0
v_exp_f32 v216,v0
v_mfma_f32_16x16x32_bf16 v[36:39],v[184:187],a[208:211],v[84:87]
v_mfma_f32_16x16x32_bf16 v[36:39],v[188:191],a[212:215],v[36:39]
s_cmp_eq_u32 s56,32768
s_cselect_b32 s57,0xffff8000,s58
v_add_f32 v4,v84,v4
v_exp_f32 v217,v4
v_mfma_f32_16x16x32_bf16 v[40:43],v[184:187],a[224:227],v[88:91]
v_mfma_f32_16x16x32_bf16 v[40:43],v[188:191],a[228:231],v[40:43]
v_add_u32 v232,s57,v232
v_add_f32 v1,v80,v1
v_exp_f32 v218,v1
v_mfma_f32_16x16x32_bf16 v[44:47],v[184:187],a[240:243],v[92:95]
v_add_f32 v72,v216,v218
v_cvt_pk_bf16_f32 v112,v216,v218
v_mfma_f32_16x16x32_bf16 v[44:47],v[188:191],a[244:247],v[44:47]
s_waitcnt lgkmcnt(5)
v_add_u32 v226,s57,v226
v_add_f32 v5,v84,v5
v_exp_f32 v219,v5
v_add_f32 v2,v80,v2
v_exp_f32 v220,v2
v_add_f32 v6,v84,v6
v_exp_f32 v221,v6
v_mfma_f32_16x16x32_bf16 v[32:35],v[192:195],a[200:203],v[32:35]
v_add_f32 v73,v217,v219
v_cvt_pk_bf16_f32 v116,v217,v219
v_mfma_f32_16x16x32_bf16 v[32:35],v[196:199],a[204:207],v[32:35]
ds_write_b128 v255,a[152:155] offset:6144
v_add_f32 v72,v220,v72
v_add_f32 v73,v221,v73
v_add_f32 v3,v80,v3
v_exp_f32 v222,v3
v_add_f32 v7,v84,v7
v_exp_f32 v223,v7
v_add_f32 v8,v88,v8
v_exp_f32 v216,v8
v_mfma_f32_16x16x32_bf16 v[36:39],v[192:195],a[216:219],v[36:39]
v_add_f32 v72,v222,v72
v_cvt_pk_bf16_f32 v113,v220,v222
v_mfma_f32_16x16x32_bf16 v[36:39],v[196:199],a[220:223],v[36:39]
v_add_f32 v73,v223,v73
v_cvt_pk_bf16_f32 v117,v221,v223
v_add_f32 v12,v92,v12
v_exp_f32 v217,v12
v_add_f32 v9,v88,v9
v_exp_f32 v218,v9
v_add_f32 v13,v92,v13
v_exp_f32 v219,v13
v_mfma_f32_16x16x32_bf16 v[40:43],v[192:195],a[232:235],v[40:43]
v_add_f32 v74,v216,v218
v_cvt_pk_bf16_f32 v120,v216,v218
v_mfma_f32_16x16x32_bf16 v[40:43],v[196:199],a[236:239],v[40:43]
v_add_f32 v75,v217,v219
v_cvt_pk_bf16_f32 v124,v217,v219
v_add_f32 v10,v88,v10
v_exp_f32 v220,v10
v_add_f32 v14,v92,v14
v_exp_f32 v221,v14
v_mfma_f32_16x16x32_bf16 v[44:47],v[192:195],a[248:251],v[44:47]
v_add_f32 v74,v220,v74
v_add_f32 v75,v221,v75
v_mfma_f32_16x16x32_bf16 v[44:47],v[196:199],a[252:255],v[44:47]
s_waitcnt lgkmcnt(4)
v_add_f32 v11,v88,v11
v_exp_f32 v222,v11
v_add_f32 v15,v92,v15
v_exp_f32 v223,v15
v_add_f32 v64,v96,v64
v_exp_f32 v216,v64
v_mfma_f32_16x16x32_bf16 v[48:51],v[200:203],a[192:195],v[80:83]
v_add_f32 v74,v222,v74
v_cvt_pk_bf16_f32 v121,v220,v222
v_mfma_f32_16x16x32_bf16 v[48:51],v[204:207],a[196:199],v[48:51]
v_add_f32 v75,v223,v75
v_cvt_pk_bf16_f32 v125,v221,v223
v_add_f32 v65,v96,v65
v_exp_f32 v217,v65
v_add_f32 v66,v96,v66
v_exp_f32 v218,v66
v_add_f32 v67,v96,v67
v_exp_f32 v219,v67
v_mfma_f32_16x16x32_bf16 v[52:55],v[200:203],a[208:211],v[84:87]
v_add_f32 v76,v216,v217
v_cvt_pk_bf16_f32 v144,v216,v217
v_mfma_f32_16x16x32_bf16 v[52:55],v[204:207],a[212:215],v[52:55]
v_add_f32 v76,v218,v76
v_add_f32 v76,v219,v76
v_add_f32 v16,v80,v16
v_exp_f32 v220,v16
v_cvt_pk_bf16_f32 v145,v218,v219
v_add_f32 v20,v84,v20
)A"
R"A(v_exp_f32 v221,v20
v_add_f32 v17,v80,v17
v_exp_f32 v222,v17
v_mfma_f32_16x16x32_bf16 v[56:59],v[200:203],a[224:227],v[88:91]
v_add_f32 v72,v220,v72
v_add_f32 v73,v221,v73
v_mfma_f32_16x16x32_bf16 v[56:59],v[204:207],a[228:231],v[56:59]
ds_write_b128 v255,a[156:159] offset:7168
v_add_f32 v72,v222,v72
v_cvt_pk_bf16_f32 v114,v220,v222
v_add_f32 v21,v84,v21
v_exp_f32 v223,v21
v_add_f32 v18,v80,v18
v_exp_f32 v216,v18
v_mfma_f32_16x16x32_bf16 v[60:63],v[200:203],a[240:243],v[92:95]
v_add_f32 v73,v223,v73
v_cvt_pk_bf16_f32 v118,v221,v223
v_mfma_f32_16x16x32_bf16 v[60:63],v[204:207],a[244:247],v[60:63]
s_waitcnt lgkmcnt(2)
v_add_f32 v72,v216,v72
v_add_f32 v22,v84,v22
v_exp_f32 v217,v22
v_add_f32 v19,v80,v19
v_exp_f32 v218,v19
v_add_f32 v23,v84,v23
v_exp_f32 v219,v23
v_mfma_f32_16x16x32_bf16 v[48:51],v[208:211],a[200:203],v[48:51]
v_add_f32 v73,v217,v73
v_add_f32 v72,v218,v72
v_mfma_f32_16x16x32_bf16 v[48:51],v[212:215],a[204:207],v[48:51]
v_cvt_pk_bf16_f32 v115,v216,v218
v_add_f32 v73,v219,v73
v_add_f32 v24,v88,v24
v_exp_f32 v220,v24
v_cvt_pk_bf16_f32 v119,v217,v219
v_add_f32 v28,v92,v28
v_exp_f32 v221,v28
v_add_f32 v25,v88,v25
v_exp_f32 v222,v25
v_mfma_f32_16x16x32_bf16 v[52:55],v[208:211],a[216:219],v[52:55]
v_add_f32 v74,v220,v74
v_add_f32 v75,v221,v75
v_mfma_f32_16x16x32_bf16 v[52:55],v[212:215],a[220:223],v[52:55]
v_add_f32 v74,v222,v74
v_cvt_pk_bf16_f32 v122,v220,v222
v_add_f32 v29,v92,v29
v_exp_f32 v223,v29
v_add_f32 v26,v88,v26
v_exp_f32 v216,v26
v_add_f32 v30,v92,v30
v_exp_f32 v217,v30
v_mfma_f32_16x16x32_bf16 v[56:59],v[208:211],a[232:235],v[56:59]
v_add_f32 v75,v223,v75
v_cvt_pk_bf16_f32 v126,v221,v223
v_mfma_f32_16x16x32_bf16 v[56:59],v[212:215],a[236:239],v[56:59]
v_add_f32 v27,v88,v27
v_exp_f32 v218,v27
v_add_f32 v31,v92,v31
v_exp_f32 v219,v31
v_add_f32 v68,v96,v68
v_exp_f32 v220,v68
v_add_f32 v69,v96,v69
v_exp_f32 v221,v69
v_add_f32 v70,v96,v70
v_exp_f32 v222,v70
v_add_f32 v71,v96,v71
v_exp_f32 v223,v71
v_add_f32 v74,v216,v74
v_add_f32 v75,v217,v75
v_mfma_f32_16x16x32_bf16 v[60:63],v[208:211],a[248:251],v[60:63]
v_add_f32 v74,v218,v74
v_cvt_pk_bf16_f32 v123,v216,v218
v_add_f32 v75,v219,v75
v_cvt_pk_bf16_f32 v127,v217,v219
v_add_f32 v76,v220,v76
v_add_f32 v76,v221,v76
v_cvt_pk_bf16_f32 v146,v220,v221
v_add_f32 v76,v222,v76
v_add_f32 v76,v223,v76
v_cvt_pk_bf16_f32 v147,v222,v223
v_mfma_f32_16x16x32_bf16 v[60:63],v[212:215],a[252:255],v[60:63]
s_mov_b32 s65,1
s_mov_b32 s38,0x18c000
s_and_b32 s72,s60,1
s_lshl_b32 s72,s72,12
v_add_u32 v248,s72,v255
v_xor_b32 v249,8192,v248
v_xor_b32 v250,512,v227
s_lshl_b32 s72,s60,17
v_lshlrev_b32 v224,13,v239
v_add_u32 v224,s72,v224
s_waitcnt lgkmcnt(0)
s_barrier
ds_read_b64 v[240:241],v227
ds_read_b64 v[242:243],v250
ds_read_b128 v[152:155],v248
ds_read_b128 v[156:159],v248 offset:1024
ds_read_b128 v[160:163],v248 offset:2048
ds_read_b128 v[164:167],v248 offset:3072
ds_read_b128 v[168:171],v249
ds_read_b128 v[172:175],v249 offset:1024
ds_read_b128 v[176:179],v249 offset:2048
ds_read_b128 v[180:183],v249 offset:3072
s_mov_b32 s72,s27
v_accvgpr_read_b32 v200,a0
v_accvgpr_read_b32 v201,a1
v_accvgpr_read_b32 v202,a2
v_accvgpr_read_b32 v203,a3
v_accvgpr_read_b32 v204,a4
v_accvgpr_read_b32 v205,a5
v_accvgpr_read_b32 v206,a6
v_accvgpr_read_b32 v207,a7
v_pk_mul_f32 v[200:201],v[200:201],v[104:105] op_sel_hi:[1,0]
v_pk_mul_f32 v[202:203],v[202:203],v[104:105] op_sel_hi:[1,0]
v_pk_mul_f32 v[204:205],v[204:205],v[104:105] op_sel_hi:[1,0]
v_pk_mul_f32 v[206:207],v[206:207],v[104:105] op_sel_hi:[1,0]
v_cvt_pk_bf16_f32 v128,v200,v201
v_cvt_pk_bf16_f32 v129,v202,v203
v_cvt_pk_bf16_f32 v130,v204,v205
v_cvt_pk_bf16_f32 v131,v206,v207
buffer_store_dwordx4 v[128:131],v239,s[52:55],s72 offen sc1
v_accvgpr_read_b32 v208,a32
v_accvgpr_read_b32 v209,a33
v_accvgpr_read_b32 v210,a34
v_accvgpr_read_b32 v211,a35
v_accvgpr_read_b32 v212,a36
v_accvgpr_read_b32 v213,a37
v_accvgpr_read_b32 v214,a38
v_accvgpr_read_b32 v215,a39
v_pk_mul_f32 v[208:209],v[208:209],v[104:105] op_sel:[0,1] op_sel_hi:[1,1]
v_pk_mul_f32 v[210:211],v[210:211],v[104:105] op_sel:[0,1] op_sel_hi:[1,1]
v_pk_mul_f32 v[212:213],v[212:213],v[104:105] op_sel:[0,1] op_sel_hi:[1,1]
v_pk_mul_f32 v[214:215],v[214:215],v[104:105] op_sel:[0,1] op_sel_hi:[1,1]
v_cvt_pk_bf16_f32 v132,v208,v209
v_cvt_pk_bf16_f32 v133,v210,v211
v_cvt_pk_bf16_f32 v134,v212,v213
v_cvt_pk_bf16_f32 v135,v214,v215
buffer_store_dwordx4 v[132:135],v239,s[52:55],s72 offen offset:1024 sc1
s_add_u32 s72,s27,6144
v_accvgpr_read_b32 v200,a8
v_accvgpr_read_b32 v201,a9
v_accvgpr_read_b32 v202,a10
v_accvgpr_read_b32 v203,a11
v_accvgpr_read_b32 v204,a12
v_accvgpr_read_b32 v205,a13
v_accvgpr_read_b32 v206,a14
v_accvgpr_read_b32 v207,a15
v_pk_mul_f32 v[200:201],v[200:201],v[104:105] op_sel_hi:[1,0]
v_pk_mul_f32 v[202:203],v[202:203],v[104:105] op_sel_hi:[1,0]
v_pk_mul_f32 v[204:205],v[204:205],v[104:105] op_sel_hi:[1,0]
v_pk_mul_f32 v[206:207],v[206:207],v[104:105] op_sel_hi:[1,0]
v_cvt_pk_bf16_f32 v136,v200,v201
v_cvt_pk_bf16_f32 v137,v202,v203
v_cvt_pk_bf16_f32 v138,v204,v205
v_cvt_pk_bf16_f32 v139,v206,v207
buffer_store_dwordx4 v[136:139],v239,s[52:55],s72 offen sc1
v_accvgpr_read_b32 v208,a40
v_accvgpr_read_b32 v209,a41
v_accvgpr_read_b32 v210,a42
v_accvgpr_read_b32 v211,a43
v_accvgpr_read_b32 v212,a44
v_accvgpr_read_b32 v213,a45
v_accvgpr_read_b32 v214,a46
v_accvgpr_read_b32 v215,a47
v_pk_mul_f32 v[208:209],v[208:209],v[104:105] op_sel:[0,1] op_sel_hi:[1,1]
v_pk_mul_f32 v[210:211],v[210:211],v[104:105] op_sel:[0,1] op_sel_hi:[1,1]
v_pk_mul_f32 v[212:213],v[212:213],v[104:105] op_sel:[0,1] op_sel_hi:[1,1]
v_pk_mul_f32 v[214:215],v[214:215],v[104:105] op_sel:[0,1] op_sel_hi:[1,1]
v_cvt_pk_bf16_f32 v140,v208,v209
v_cvt_pk_bf16_f32 v141,v210,v211
v_cvt_pk_bf16_f32 v142,v212,v213
v_cvt_pk_bf16_f32 v143,v214,v215
buffer_store_dwordx4 v[140:143],v239,s[52:55],s72 offen offset:1024 sc1
s_add_u32 s72,s27,12288
v_accvgpr_read_b32 v200,a16
v_accvgpr_read_b32 v201,a17
v_accvgpr_read_b32 v202,a18
v_accvgpr_read_b32 v203,a19
v_accvgpr_read_b32 v204,a20
v_accvgpr_read_b32 v205,a21
v_accvgpr_read_b32 v206,a22
v_accvgpr_read_b32 v207,a23
v_pk_mul_f32 v[200:201],v[200:201],v[104:105] op_sel_hi:[1,0]
v_pk_mul_f32 v[202:203],v[202:203],v[104:105] op_sel_hi:[1,0]
v_pk_mul_f32 v[204:205],v[204:205],v[104:105] op_sel_hi:[1,0]
v_pk_mul_f32 v[206:207],v[206:207],v[104:105] op_sel_hi:[1,0]
v_cvt_pk_bf16_f32 v128,v200,v201
v_cvt_pk_bf16_f32 v129,v202,v203
v_cvt_pk_bf16_f32 v130,v204,v205
v_cvt_pk_bf16_f32 v131,v206,v207
buffer_store_dwordx4 v[128:131],v239,s[52:55],s72 offen sc1
v_accvgpr_read_b32 v208,a48
v_accvgpr_read_b32 v209,a49
v_accvgpr_read_b32 v210,a50
v_accvgpr_read_b32 v211,a51
v_accvgpr_read_b32 v212,a52
v_accvgpr_read_b32 v213,a53
v_accvgpr_read_b32 v214,a54
v_accvgpr_read_b32 v215,a55
v_pk_mul_f32 v[208:209],v[208:209],v[104:105] op_sel:[0,1] op_sel_hi:[1,1]
v_pk_mul_f32 v[210:211],v[210:211],v[104:105] op_sel:[0,1] op_sel_hi:[1,1]
v_pk_mul_f32 v[212:213],v[212:213],v[104:105] op_sel:[0,1] op_sel_hi:[1,1]
v_pk_mul_f32 v[214:215],v[214:215],v[104:105] op_sel:[0,1] op_sel_hi:[1,1]
v_cvt_pk_bf16_f32 v132,v208,v209
v_cvt_pk_bf16_f32 v133,v210,v211
v_cvt_pk_bf16_f32 v134,v212,v213
v_cvt_pk_bf16_f32 v135,v214,v215
buffer_store_dwordx4 v[132:135],v239,s[52:55],s72 offen offset:1024 sc1
s_add_u32 s72,s27,18432
v_accvgpr_read_b32 v200,a24
v_accvgpr_read_b32 v201,a25
v_accvgpr_read_b32 v202,a26
v_accvgpr_read_b32 v203,a27
v_accvgpr_read_b32 v204,a28
v_accvgpr_read_b32 v205,a29
v_accvgpr_read_b32 v206,a30
v_accvgpr_read_b32 v207,a31
v_pk_mul_f32 v[200:201],v[200:201],v[104:105] op_sel_hi:[1,0]
v_pk_mul_f32 v[202:203],v[202:203],v[104:105] op_sel_hi:[1,0]
v_pk_mul_f32 v[204:205],v[204:205],v[104:105] op_sel_hi:[1,0]
v_pk_mul_f32 v[206:207],v[206:207],v[104:105] op_sel_hi:[1,0]
v_cvt_pk_bf16_f32 v136,v200,v201
v_cvt_pk_bf16_f32 v137,v202,v203
v_cvt_pk_bf16_f32 v138,v204,v205
v_cvt_pk_bf16_f32 v139,v206,v207
buffer_store_dwordx4 v[136:139],v239,s[52:55],s72 offen sc1
v_accvgpr_read_b32 v208,a56
v_accvgpr_read_b32 v209,a57
v_accvgpr_read_b32 v210,a58
v_accvgpr_read_b32 v211,a59
v_accvgpr_read_b32 v212,a60
v_accvgpr_read_b32 v213,a61
v_accvgpr_read_b32 v214,a62
v_accvgpr_read_b32 v215,a63
v_pk_mul_f32 v[208:209],v[208:209],v[104:105] op_sel:[0,1] op_sel_hi:[1,1]
v_pk_mul_f32 v[210:211],v[210:211],v[104:105] op_sel:[0,1] op_sel_hi:[1,1]
v_pk_mul_f32 v[212:213],v[212:213],v[104:105] op_sel:[0,1] op_sel_hi:[1,1]
v_pk_mul_f32 v[214:215],v[214:215],v[104:105] op_sel:[0,1] op_sel_hi:[1,1]
v_cvt_pk_bf16_f32 v140,v208,v209
v_cvt_pk_bf16_f32 v141,v210,v211
v_cvt_pk_bf16_f32 v142,v212,v213
v_cvt_pk_bf16_f32 v143,v214,v215
buffer_store_dwordx4 v[140:143],v239,s[52:55],s72 offen offset:1024 sc1
s_waitcnt lgkmcnt(8)
v_min_f32 v244,v240,v242
v_sub_f32 v240,v244,v240
v_sub_f32 v242,v244,v242
v_exp_f32 v240,v240
v_exp_f32 v242,v242
v_mul_f32 v244,v240,v241
v_fma_f32 v244,v242,v243,v244
v_mov_b32 v245,v244
s_nop 1
v_permlane32_swap_b32 v245,v244
v_add_f32 v244,v244,v245
v_mov_b32 v245,v244
s_nop 1
v_permlane16_swap_b32 v245,v244
v_add_f32 v244,v244,v245
v_rcp_f32 v244,v244
s_nop 0
v_mul_f32 v240,v244,v240
v_mul_f32 v242,v244,v242
s_waitcnt lgkmcnt(7)
v_pk_mul_f32 v[152:153],v[152:153],v[240:241] op_sel_hi:[1,0]
v_pk_mul_f32 v[154:155],v[154:155],v[240:241] op_sel_hi:[1,0]
s_waitcnt lgkmcnt(6)
v_pk_mul_f32 v[156:157],v[156:157],v[240:241] op_sel_hi:[1,0]
v_pk_mul_f32 v[158:159],v[158:159],v[240:241] op_sel_hi:[1,0]
s_waitcnt lgkmcnt(5)
v_pk_mul_f32 v[160:161],v[160:161],v[240:241] op_sel_hi:[1,0]
v_pk_mul_f32 v[162:163],v[162:163],v[240:241] op_sel_hi:[1,0]
s_waitcnt lgkmcnt(4)
v_pk_mul_f32 v[164:165],v[164:165],v[240:241] op_sel_hi:[1,0]
v_pk_mul_f32 v[166:167],v[166:167],v[240:241] op_sel_hi:[1,0]
s_waitcnt lgkmcnt(3)
v_pk_fma_f32 v[152:153],v[168:169],v[242:243],v[152:153] op_sel_hi:[1,0,1]
v_pk_fma_f32 v[154:155],v[170:171],v[242:243],v[154:155] op_sel_hi:[1,0,1]
s_waitcnt lgkmcnt(2)
v_pk_fma_f32 v[156:157],v[172:173],v[242:243],v[156:157] op_sel_hi:[1,0,1]
v_pk_fma_f32 v[158:159],v[174:175],v[242:243],v[158:159] op_sel_hi:[1,0,1]
s_waitcnt lgkmcnt(1)
v_pk_fma_f32 v[160:161],v[176:177],v[242:243],v[160:161] op_sel_hi:[1,0,1]
v_pk_fma_f32 v[162:163],v[178:179],v[242:243],v[162:163] op_sel_hi:[1,0,1]
s_waitcnt lgkmcnt(0)
v_pk_fma_f32 v[164:165],v[180:181],v[242:243],v[164:165] op_sel_hi:[1,0,1]
v_pk_fma_f32 v[166:167],v[182:183],v[242:243],v[166:167] op_sel_hi:[1,0,1]
v_cvt_pk_bf16_f32 v184,v152,v153
v_cvt_pk_bf16_f32 v185,v154,v155
v_cvt_pk_bf16_f32 v186,v156,v157
v_cvt_pk_bf16_f32 v187,v158,v159
v_cvt_pk_bf16_f32 v188,v160,v161
v_cvt_pk_bf16_f32 v189,v162,v163
v_cvt_pk_bf16_f32 v190,v164,v165
v_cvt_pk_bf16_f32 v191,v166,v167
s_mov_b32 s72,s81
buffer_store_dwordx4 v[184:187],v239,s[52:55],s72 offen sc1
s_add_u32 s72,s81,6144
buffer_store_dwordx4 v[188:191],v239,s[52:55],s72 offen sc1
ds_read_b64_tr_b16 v[192:193],v233 offset:256
ds_read_b64_tr_b16 v[194:195],v233 offset:4352
ds_read_b64_tr_b16 v[196:197],v234 offset:256
ds_read_b64_tr_b16 v[198:199],v234 offset:4352
ds_read_b64_tr_b16 v[200:201],v233 offset:512
ds_read_b64_tr_b16 v[202:203],v233 offset:4608
ds_read_b64_tr_b16 v[204:205],v234 offset:512
ds_read_b64_tr_b16 v[206:207],v234 offset:4608
ds_read_b64_tr_b16 v[208:209],v233 offset:768
ds_read_b64_tr_b16 v[210:211],v233 offset:4864
ds_read_b64_tr_b16 v[212:213],v234 offset:768
ds_read_b64_tr_b16 v[214:215],v234 offset:4864
v_accvgpr_read_b32 v128,a64
v_accvgpr_read_b32 v129,a65
v_accvgpr_read_b32 v130,a66
v_accvgpr_read_b32 v131,a67
v_accvgpr_read_b32 v132,a68
v_accvgpr_read_b32 v133,a69
v_accvgpr_read_b32 v134,a70
v_accvgpr_read_b32 v135,a71
v_pk_mul_f32 v[128:129],v[128:129],v[106:107] op_sel_hi:[1,0]
v_pk_mul_f32 v[130:131],v[130:131],v[106:107] op_sel_hi:[1,0]
v_pk_mul_f32 v[132:133],v[132:133],v[106:107] op_sel_hi:[1,0]
v_pk_mul_f32 v[134:135],v[134:135],v[106:107] op_sel_hi:[1,0]
v_cvt_pk_bf16_f32 v152,v128,v129
v_cvt_pk_bf16_f32 v153,v130,v131
v_cvt_pk_bf16_f32 v154,v132,v133
v_cvt_pk_bf16_f32 v155,v134,v135
v_accvgpr_read_b32 v136,a96
v_accvgpr_read_b32 v137,a97
)A"
R"A(v_accvgpr_read_b32 v138,a98
v_accvgpr_read_b32 v139,a99
v_accvgpr_read_b32 v140,a100
v_accvgpr_read_b32 v141,a101
v_accvgpr_read_b32 v142,a102
v_accvgpr_read_b32 v143,a103
v_pk_mul_f32 v[136:137],v[136:137],v[106:107] op_sel:[0,1] op_sel_hi:[1,1]
v_pk_mul_f32 v[138:139],v[138:139],v[106:107] op_sel:[0,1] op_sel_hi:[1,1]
v_pk_mul_f32 v[140:141],v[140:141],v[106:107] op_sel:[0,1] op_sel_hi:[1,1]
v_pk_mul_f32 v[142:143],v[142:143],v[106:107] op_sel:[0,1] op_sel_hi:[1,1]
v_cvt_pk_bf16_f32 v168,v136,v137
v_cvt_pk_bf16_f32 v169,v138,v139
v_cvt_pk_bf16_f32 v170,v140,v141
v_cvt_pk_bf16_f32 v171,v142,v143
ds_read_b64_tr_b16 v[184:185],v233
ds_read_b64_tr_b16 v[186:187],v233 offset:4096
ds_read_b64_tr_b16 v[188:189],v234
ds_read_b64_tr_b16 v[190:191],v234 offset:4096
v_accvgpr_read_b32 v128,a72
v_accvgpr_read_b32 v129,a73
v_accvgpr_read_b32 v130,a74
v_accvgpr_read_b32 v131,a75
v_accvgpr_read_b32 v132,a76
v_accvgpr_read_b32 v133,a77
v_accvgpr_read_b32 v134,a78
v_accvgpr_read_b32 v135,a79
v_pk_mul_f32 v[128:129],v[128:129],v[106:107] op_sel_hi:[1,0]
v_pk_mul_f32 v[130:131],v[130:131],v[106:107] op_sel_hi:[1,0]
v_pk_mul_f32 v[132:133],v[132:133],v[106:107] op_sel_hi:[1,0]
v_pk_mul_f32 v[134:135],v[134:135],v[106:107] op_sel_hi:[1,0]
v_cvt_pk_bf16_f32 v156,v128,v129
v_cvt_pk_bf16_f32 v157,v130,v131
v_cvt_pk_bf16_f32 v158,v132,v133
v_cvt_pk_bf16_f32 v159,v134,v135
v_accvgpr_read_b32 v136,a104
v_accvgpr_read_b32 v137,a105
v_accvgpr_read_b32 v138,a106
v_accvgpr_read_b32 v139,a107
v_accvgpr_read_b32 v140,a108
v_accvgpr_read_b32 v141,a109
v_accvgpr_read_b32 v142,a110
v_accvgpr_read_b32 v143,a111
v_pk_mul_f32 v[136:137],v[136:137],v[106:107] op_sel:[0,1] op_sel_hi:[1,1]
v_pk_mul_f32 v[138:139],v[138:139],v[106:107] op_sel:[0,1] op_sel_hi:[1,1]
v_pk_mul_f32 v[140:141],v[140:141],v[106:107] op_sel:[0,1] op_sel_hi:[1,1]
v_pk_mul_f32 v[142:143],v[142:143],v[106:107] op_sel:[0,1] op_sel_hi:[1,1]
v_cvt_pk_bf16_f32 v172,v136,v137
v_cvt_pk_bf16_f32 v173,v138,v139
v_cvt_pk_bf16_f32 v174,v140,v141
v_cvt_pk_bf16_f32 v175,v142,v143
v_accvgpr_read_b32 v128,a80
v_accvgpr_read_b32 v129,a81
v_accvgpr_read_b32 v130,a82
v_accvgpr_read_b32 v131,a83
v_accvgpr_read_b32 v132,a84
v_accvgpr_read_b32 v133,a85
v_accvgpr_read_b32 v134,a86
v_accvgpr_read_b32 v135,a87
v_pk_mul_f32 v[128:129],v[128:129],v[106:107] op_sel_hi:[1,0]
v_pk_mul_f32 v[130:131],v[130:131],v[106:107] op_sel_hi:[1,0]
v_pk_mul_f32 v[132:133],v[132:133],v[106:107] op_sel_hi:[1,0]
v_pk_mul_f32 v[134:135],v[134:135],v[106:107] op_sel_hi:[1,0]
v_cvt_pk_bf16_f32 v160,v128,v129
v_cvt_pk_bf16_f32 v161,v130,v131
v_cvt_pk_bf16_f32 v162,v132,v133
v_cvt_pk_bf16_f32 v163,v134,v135
v_accvgpr_read_b32 v136,a112
v_accvgpr_read_b32 v137,a113
v_accvgpr_read_b32 v138,a114
v_accvgpr_read_b32 v139,a115
v_accvgpr_read_b32 v140,a116
v_accvgpr_read_b32 v141,a117
v_accvgpr_read_b32 v142,a118
v_accvgpr_read_b32 v143,a119
v_pk_mul_f32 v[136:137],v[136:137],v[106:107] op_sel:[0,1] op_sel_hi:[1,1]
v_pk_mul_f32 v[138:139],v[138:139],v[106:107] op_sel:[0,1] op_sel_hi:[1,1]
v_pk_mul_f32 v[140:141],v[140:141],v[106:107] op_sel:[0,1] op_sel_hi:[1,1]
v_pk_mul_f32 v[142:143],v[142:143],v[106:107] op_sel:[0,1] op_sel_hi:[1,1]
v_cvt_pk_bf16_f32 v176,v136,v137
v_cvt_pk_bf16_f32 v177,v138,v139
v_cvt_pk_bf16_f32 v178,v140,v141
v_cvt_pk_bf16_f32 v179,v142,v143
v_accvgpr_read_b32 v128,a88
v_accvgpr_read_b32 v129,a89
v_accvgpr_read_b32 v130,a90
v_accvgpr_read_b32 v131,a91
v_accvgpr_read_b32 v132,a92
v_accvgpr_read_b32 v133,a93
v_accvgpr_read_b32 v134,a94
v_accvgpr_read_b32 v135,a95
v_pk_mul_f32 v[128:129],v[128:129],v[106:107] op_sel_hi:[1,0]
v_pk_mul_f32 v[130:131],v[130:131],v[106:107] op_sel_hi:[1,0]
v_pk_mul_f32 v[132:133],v[132:133],v[106:107] op_sel_hi:[1,0]
v_pk_mul_f32 v[134:135],v[134:135],v[106:107] op_sel_hi:[1,0]
v_cvt_pk_bf16_f32 v164,v128,v129
v_cvt_pk_bf16_f32 v165,v130,v131
v_cvt_pk_bf16_f32 v166,v132,v133
v_cvt_pk_bf16_f32 v167,v134,v135
v_accvgpr_read_b32 v136,a120
v_accvgpr_read_b32 v137,a121
v_accvgpr_read_b32 v138,a122
v_accvgpr_read_b32 v139,a123
v_accvgpr_read_b32 v140,a124
v_accvgpr_read_b32 v141,a125
v_accvgpr_read_b32 v142,a126
v_accvgpr_read_b32 v143,a127
v_pk_mul_f32 v[136:137],v[136:137],v[106:107] op_sel:[0,1] op_sel_hi:[1,1]
v_pk_mul_f32 v[138:139],v[138:139],v[106:107] op_sel:[0,1] op_sel_hi:[1,1]
v_pk_mul_f32 v[140:141],v[140:141],v[106:107] op_sel:[0,1] op_sel_hi:[1,1]
v_pk_mul_f32 v[142:143],v[142:143],v[106:107] op_sel:[0,1] op_sel_hi:[1,1]
v_cvt_pk_bf16_f32 v180,v136,v137
v_cvt_pk_bf16_f32 v181,v138,v139
v_cvt_pk_bf16_f32 v182,v140,v141
v_cvt_pk_bf16_f32 v183,v142,v143
s_waitcnt vmcnt(19) lgkmcnt(0)
s_branch .Lt0
.Lt15b:
v_mfma_f32_16x16x32_bf16 a[0:3],v[184:187],v[112:115],a[0:3]
s_waitcnt vmcnt(8)
s_barrier
ds_read_b64_tr_b16 v[212:213],v234 offset:33536
v_mfma_f32_16x16x32_bf16 a[32:35],v[184:187],v[116:119],a[32:35]
s_lshl_b32 s73,s28,2
ds_read_b64_tr_b16 v[214:215],v234 offset:37632
v_add_f32 v104,v72,v104
v_mfma_f32_16x16x32_bf16 a[64:67],v[184:187],v[120:123],a[64:67]
s_add_u32 s73,s73,s60
v_exp_f32 v216,v32
v_mfma_f32_16x16x32_bf16 a[96:99],v[184:187],v[124:127],a[96:99]
s_mul_i32 s73,s73,768
s_add_u32 s73,s73,.Litem0-.Lk1pc
v_min_u32 v255,640,v251
v_mfma_f32_16x16x32_bf16 a[128:131],v[184:187],v[144:147],a[128:131]
v_add_u32 v255,s73,v255
v_min_u32 v255,.Lend-.Lk1pc,v255
v_mfma_f32_16x16x32_bf16 a[4:7],v[188:191],v[112:115],a[4:7]
global_load_dword v253,v255,s[30:31]
v_and_b32 v255,0x380,v251
v_mfma_f32_16x16x32_bf16 a[36:39],v[188:191],v[116:119],a[36:39]
buffer_load_dword v253,v255,s[40:43],s69 offen sc1
v_add_f32 v105,v73,v105
v_mfma_f32_16x16x32_bf16 a[68:71],v[188:191],v[120:123],a[68:71]
s_mov_b64 s[48:49],s[22:23]
s_cmp_lt_u32 s28,30
s_cselect_b32 s50,0xc0000,0
s_cselect_b32 s34,s34,0
s_mul_i32 s61,s60,196608
s_mul_i32 s62,s60,1310720
s_lshl_b32 s72,s60,6
v_exp_f32 v217,v36
v_bfe_u32 v255,v235,8,2
v_xor_b32 v235,s72,v235
v_lshl_add_u32 v235,v255,10,v235
v_xor_b32 v236,32,v235
v_add_u32 v237,8192,v235
v_add_u32 v238,12288,v236
v_add_u32 v236,4096,v236
v_add_u32 v252,4096,v239
v_mfma_f32_16x16x32_bf16 a[100:103],v[188:191],v[124:127],a[100:103]
ds_read_b128 v[184:187],v232
v_exp_f32 v218,v33
v_mfma_f32_16x16x32_bf16 a[132:135],v[188:191],v[144:147],a[132:135]
s_waitcnt lgkmcnt(9)
v_add_f32 v72,v216,v218
v_cvt_pk_bf16_f32 v128,v216,v218
v_mfma_f32_16x16x32_bf16 a[8:11],v[192:195],v[112:115],a[8:11]
v_exp_f32 v219,v37
v_mfma_f32_16x16x32_bf16 a[40:43],v[192:195],v[116:119],a[40:43]
v_add_f32 v73,v217,v219
v_cvt_pk_bf16_f32 v132,v217,v219
v_mfma_f32_16x16x32_bf16 a[72:75],v[192:195],v[120:123],a[72:75]
v_exp_f32 v220,v34
v_mfma_f32_16x16x32_bf16 a[104:107],v[192:195],v[124:127],a[104:107]
ds_read_b128 v[188:191],v232 offset:256
v_add_f32 v72,v220,v72
v_mfma_f32_16x16x32_bf16 a[136:139],v[192:195],v[144:147],a[136:139]
v_exp_f32 v221,v38
v_mfma_f32_16x16x32_bf16 a[12:15],v[196:199],v[112:115],a[12:15]
ds_read_b128 v[192:195],v232 offset:512
v_add_f32 v73,v221,v73
v_mfma_f32_16x16x32_bf16 a[44:47],v[196:199],v[116:119],a[44:47]
v_exp_f32 v222,v35
v_mfma_f32_16x16x32_bf16 a[76:79],v[196:199],v[120:123],a[76:79]
s_add_u32 m0,s63,49152
v_add_f32 v72,v222,v72
v_cvt_pk_bf16_f32 v129,v220,v222
v_mfma_f32_16x16x32_bf16 a[108:111],v[196:199],v[124:127],a[108:111]
v_exp_f32 v223,v39
v_mfma_f32_16x16x32_bf16 a[140:143],v[196:199],v[144:147],a[140:143]
s_waitcnt lgkmcnt(7)
v_add_f32 v73,v223,v73
v_cvt_pk_bf16_f32 v133,v221,v223
v_mfma_f32_16x16x32_bf16 a[16:19],v[200:203],v[112:115],a[16:19]
v_exp_f32 v216,v40
v_mfma_f32_16x16x32_bf16 a[48:51],v[200:203],v[116:119],a[48:51]
buffer_load_dwordx4 v235,s[32:35],s62 offen lds
v_add_f32 v106,v74,v106
v_mfma_f32_16x16x32_bf16 a[80:83],v[200:203],v[120:123],a[80:83]
v_exp_f32 v217,v44
v_mfma_f32_16x16x32_bf16 a[112:115],v[200:203],v[124:127],a[112:115]
ds_read_b128 v[196:199],v232 offset:768
v_add_f32 v107,v75,v107
v_mfma_f32_16x16x32_bf16 a[144:147],v[200:203],v[144:147],a[144:147]
v_exp_f32 v218,v41
v_mfma_f32_16x16x32_bf16 a[20:23],v[204:207],v[112:115],a[20:23]
buffer_load_dwordx4 v236,s[32:35],s62 offen offset:1024 lds
v_add_f32 v74,v216,v218
v_mfma_f32_16x16x32_bf16 a[52:55],v[204:207],v[116:119],a[52:55]
buffer_load_dwordx4 v237,s[32:35],s62 offen offset:2048 lds
v_cvt_pk_bf16_f32 v136,v216,v218
v_mfma_f32_16x16x32_bf16 a[84:87],v[204:207],v[120:123],a[84:87]
v_exp_f32 v219,v45
v_mfma_f32_16x16x32_bf16 a[116:119],v[204:207],v[124:127],a[116:119]
v_add_f32 v75,v217,v219
v_cvt_pk_bf16_f32 v140,v217,v219
v_mfma_f32_16x16x32_bf16 a[148:151],v[204:207],v[144:147],a[148:151]
s_waitcnt lgkmcnt(4)
ds_read_b128 v[200:203],v232 offset:4096
buffer_load_dwordx4 v238,s[32:35],s62 offen offset:3072 lds
v_exp_f32 v220,v42
v_mfma_f32_16x16x32_bf16 a[24:27],v[208:211],v[112:115],a[24:27]
s_add_u32 s62,s62,20480
s_add_u32 m0,s63,147456
v_add_f32 v74,v220,v74
v_mfma_f32_16x16x32_bf16 a[56:59],v[208:211],v[116:119],a[56:59]
v_exp_f32 v221,v46
v_mfma_f32_16x16x32_bf16 a[88:91],v[208:211],v[120:123],a[88:91]
ds_read_b128 v[204:207],v232 offset:4352
v_add_f32 v75,v221,v75
v_mfma_f32_16x16x32_bf16 a[120:123],v[208:211],v[124:127],a[120:123]
v_exp_f32 v222,v43
v_mfma_f32_16x16x32_bf16 a[152:155],v[208:211],v[144:147],a[152:155]
buffer_load_dwordx4 v235,s[32:35],s62 offen lds
v_add_f32 v74,v222,v74
v_cvt_pk_bf16_f32 v137,v220,v222
v_mfma_f32_16x16x32_bf16 a[28:31],v[212:215],v[112:115],a[28:31]
v_exp_f32 v223,v47
v_mfma_f32_16x16x32_bf16 a[60:63],v[212:215],v[116:119],a[60:63]
v_add_f32 v75,v223,v75
v_cvt_pk_bf16_f32 v141,v221,v223
v_mfma_f32_16x16x32_bf16 a[92:95],v[212:215],v[120:123],a[92:95]
buffer_load_dwordx4 v236,s[32:35],s62 offen offset:1024 lds
ds_read_b128 v[208:211],v232 offset:4608
v_mfma_f32_16x16x32_bf16 a[124:127],v[212:215],v[124:127],a[124:127]
v_exp_f32 v216,v48
v_mfma_f32_16x16x32_bf16 a[156:159],v[212:215],v[144:147],a[156:159]
s_waitcnt lgkmcnt(5)
buffer_load_dwordx4 v237,s[32:35],s62 offen offset:2048 lds
v_add_f32 v72,v216,v72
v_mfma_f32_16x16x32_bf16 v[0:3],v[184:187],a[192:195],v[80:83]
v_exp_f32 v217,v52
v_mfma_f32_16x16x32_bf16 v[0:3],v[188:191],a[196:199],v[0:3]
buffer_load_dwordx4 v238,s[32:35],s62 offen offset:3072 lds
v_add_f32 v73,v217,v73
v_mfma_f32_16x16x32_bf16 v[4:7],v[184:187],a[208:211],v[84:87]
s_add_u32 s62,s62,20480
v_exp_f32 v218,v49
v_mfma_f32_16x16x32_bf16 v[4:7],v[188:191],a[212:215],v[4:7]
s_mov_b32 m0,s63
v_add_f32 v72,v218,v72
v_cvt_pk_bf16_f32 v130,v216,v218
v_mfma_f32_16x16x32_bf16 v[8:11],v[184:187],a[224:227],v[88:91]
ds_read_b128 v[212:215],v232 offset:4864
buffer_load_dwordx4 v235,s[32:35],s62 offen lds
v_mfma_f32_16x16x32_bf16 v[8:11],v[188:191],a[228:231],v[8:11]
v_exp_f32 v219,v53
v_mfma_f32_16x16x32_bf16 v[12:15],v[184:187],a[240:243],v[92:95]
v_add_f32 v73,v219,v73
v_cvt_pk_bf16_f32 v134,v217,v219
v_mfma_f32_16x16x32_bf16 v[12:15],v[188:191],a[244:247],v[12:15]
v_exp_f32 v220,v50
v_mfma_f32_16x16x32_bf16 v[64:67],v[184:187],a[160:163],v[96:99]
buffer_load_dwordx4 v236,s[32:35],s62 offen offset:1024 lds
v_add_f32 v72,v220,v72
v_mfma_f32_16x16x32_bf16 v[64:67],v[188:191],a[164:167],v[64:67]
s_waitcnt lgkmcnt(4)
v_exp_f32 v221,v54
v_mfma_f32_16x16x32_bf16 v[0:3],v[192:195],a[200:203],v[0:3]
buffer_load_dwordx4 v237,s[32:35],s62 offen offset:2048 lds
v_add_f32 v73,v221,v73
v_mfma_f32_16x16x32_bf16 v[0:3],v[196:199],a[204:207],v[0:3]
v_exp_f32 v222,v51
v_mfma_f32_16x16x32_bf16 v[4:7],v[192:195],a[216:219],v[4:7]
v_add_f32 v72,v222,v72
v_cvt_pk_bf16_f32 v131,v220,v222
v_mfma_f32_16x16x32_bf16 v[4:7],v[196:199],a[220:223],v[4:7]
ds_read_b64_tr_b16 v[184:185],v230 offset:32768
ds_read_b64_tr_b16 v[186:187],v230 offset:36864
v_exp_f32 v223,v55
v_mfma_f32_16x16x32_bf16 v[8:11],v[192:195],a[232:235],v[8:11]
v_add_f32 v73,v223,v73
v_cvt_pk_bf16_f32 v135,v221,v223
v_mfma_f32_16x16x32_bf16 v[8:11],v[196:199],a[236:239],v[8:11]
ds_read_b64_tr_b16 v[188:189],v231 offset:32768
ds_read_b64_tr_b16 v[190:191],v231 offset:36864
buffer_load_dwordx4 v238,s[32:35],s62 offen offset:3072 lds
v_exp_f32 v216,v56
v_max_f32 v228,v72,v73
v_mfma_f32_16x16x32_bf16 v[12:15],v[192:195],a[248:251],v[12:15]
s_add_u32 s62,s62,20480
)A"
R"A(s_add_u32 m0,s63,81920
v_add_f32 v74,v216,v74
v_mfma_f32_16x16x32_bf16 v[12:15],v[196:199],a[252:255],v[12:15]
v_exp_f32 v217,v60
v_mfma_f32_16x16x32_bf16 v[64:67],v[192:195],a[168:171],v[64:67]
buffer_load_dwordx4 v235,s[32:35],s62 offen lds
v_add_f32 v75,v217,v75
v_mfma_f32_16x16x32_bf16 v[64:67],v[196:199],a[172:175],v[64:67]
s_waitcnt lgkmcnt(6)
v_exp_f32 v218,v57
v_mfma_f32_16x16x32_bf16 v[16:19],v[200:203],a[192:195],v[80:83]
v_add_f32 v74,v218,v74
v_cvt_pk_bf16_f32 v138,v216,v218
v_mfma_f32_16x16x32_bf16 v[16:19],v[204:207],a[196:199],v[16:19]
buffer_load_dwordx4 v236,s[32:35],s62 offen offset:1024 lds
v_exp_f32 v219,v61
v_mfma_f32_16x16x32_bf16 v[20:23],v[200:203],a[208:211],v[84:87]
v_add_f32 v75,v219,v75
v_cvt_pk_bf16_f32 v142,v217,v219
v_mfma_f32_16x16x32_bf16 v[20:23],v[204:207],a[212:215],v[20:23]
ds_read_b64_tr_b16 v[192:193],v230 offset:33024
ds_read_b64_tr_b16 v[194:195],v230 offset:37120
v_exp_f32 v220,v58
v_mfma_f32_16x16x32_bf16 v[24:27],v[200:203],a[224:227],v[88:91]
ds_read_b64_tr_b16 v[196:197],v231 offset:33024
v_add_f32 v74,v220,v74
v_mfma_f32_16x16x32_bf16 v[24:27],v[204:207],a[228:231],v[24:27]
ds_read_b64_tr_b16 v[198:199],v231 offset:37120
buffer_load_dwordx4 v237,s[32:35],s62 offen offset:2048 lds
v_exp_f32 v221,v62
v_mfma_f32_16x16x32_bf16 v[28:31],v[200:203],a[240:243],v[92:95]
v_exp_f32 v222,v59
v_exp_f32 v223,v63
v_mfma_f32_16x16x32_bf16 v[28:31],v[204:207],a[244:247],v[28:31]
buffer_load_dwordx4 v238,s[32:35],s62 offen offset:3072 lds
v_add_f32 v75,v221,v75
v_add_f32 v74,v222,v74
v_add_f32 v75,v223,v75
v_max3_f32 v228,v228,v74,v75
v_cmp_nge_f32 vcc,0x46800000,v228
v_cvt_pk_bf16_f32 v139,v220,v222
v_mfma_f32_16x16x32_bf16 v[68:71],v[200:203],a[160:163],v[96:99]
s_add_u32 s62,s62,20480
s_waitcnt vmcnt(18)
v_cvt_pk_bf16_f32 v143,v221,v223
v_mfma_f32_16x16x32_bf16 v[68:71],v[204:207],a[164:167],v[68:71]
s_waitcnt lgkmcnt(8)
s_cbranch_vccnz .Lst16
.Lbk16:
s_add_u32 m0,s63,32768
v_mfma_f32_16x16x32_bf16 v[16:19],v[208:211],a[200:203],v[16:19]
buffer_load_dwordx4 v235,s[32:35],s62 offen lds
ds_read_b64_tr_b16 v[200:201],v230 offset:33280
v_mfma_f32_16x16x32_bf16 v[16:19],v[212:215],a[204:207],v[16:19]
ds_read_b64_tr_b16 v[202:203],v230 offset:37376
buffer_load_dwordx4 v236,s[32:35],s62 offen offset:1024 lds
v_mfma_f32_16x16x32_bf16 v[20:23],v[208:211],a[216:219],v[20:23]
ds_read_b64_tr_b16 v[204:205],v231 offset:33280
ds_read_b64_tr_b16 v[206:207],v231 offset:37376
v_mfma_f32_16x16x32_bf16 v[20:23],v[212:215],a[220:223],v[20:23]
v_exp_f32 v216,v0
v_mfma_f32_16x16x32_bf16 v[24:27],v[208:211],a[232:235],v[24:27]
v_add_f32 v104,v72,v104
v_add_f32 v105,v73,v105
v_mfma_f32_16x16x32_bf16 v[24:27],v[212:215],a[236:239],v[24:27]
v_exp_f32 v217,v4
v_mfma_f32_16x16x32_bf16 v[28:31],v[208:211],a[248:251],v[28:31]
buffer_load_dwordx4 v237,s[32:35],s62 offen offset:2048 lds
buffer_load_dwordx4 v238,s[32:35],s62 offen offset:3072 lds
v_mfma_f32_16x16x32_bf16 v[28:31],v[212:215],a[252:255],v[28:31]
s_add_u32 s62,s62,20480
v_exp_f32 v218,v1
v_mfma_f32_16x16x32_bf16 v[68:71],v[208:211],a[168:171],v[68:71]
s_add_u32 m0,s63,65536
v_add_f32 v72,v216,v218
v_cvt_pk_bf16_f32 v112,v216,v218
v_mfma_f32_16x16x32_bf16 v[68:71],v[212:215],a[172:175],v[68:71]
s_waitcnt lgkmcnt(8)
v_exp_f32 v219,v5
v_mfma_f32_16x16x32_bf16 a[0:3],v[184:187],v[128:131],a[0:3]
v_add_f32 v73,v217,v219
v_cvt_pk_bf16_f32 v116,v217,v219
v_mfma_f32_16x16x32_bf16 a[32:35],v[184:187],v[132:135],a[32:35]
v_exp_f32 v220,v2
v_mfma_f32_16x16x32_bf16 a[64:67],v[184:187],v[136:139],a[64:67]
buffer_load_dwordx4 v235,s[32:35],s62 offen lds
ds_read_b64_tr_b16 v[208:209],v230 offset:33536
v_mfma_f32_16x16x32_bf16 a[96:99],v[184:187],v[140:143],a[96:99]
ds_read_b64_tr_b16 v[210:211],v230 offset:37632
v_add_f32 v72,v220,v72
v_exp_f32 v221,v6
v_mfma_f32_16x16x32_bf16 a[4:7],v[188:191],v[128:131],a[4:7]
ds_read_b64_tr_b16 v[212:213],v231 offset:33536
ds_read_b64_tr_b16 v[214:215],v231 offset:37632
v_add_f32 v73,v221,v73
v_mfma_f32_16x16x32_bf16 a[36:39],v[188:191],v[132:135],a[36:39]
buffer_load_dwordx4 v236,s[32:35],s62 offen offset:1024 lds
v_exp_f32 v222,v3
v_mfma_f32_16x16x32_bf16 a[68:71],v[188:191],v[136:139],a[68:71]
v_add_f32 v72,v222,v72
v_cvt_pk_bf16_f32 v113,v220,v222
v_mfma_f32_16x16x32_bf16 a[100:103],v[188:191],v[140:143],a[100:103]
s_waitcnt lgkmcnt(8)
v_exp_f32 v223,v7
v_mfma_f32_16x16x32_bf16 a[8:11],v[192:195],v[128:131],a[8:11]
ds_read_b128 v[184:187],v226
buffer_load_dwordx4 v237,s[32:35],s62 offen offset:2048 lds
v_add_f32 v73,v223,v73
v_cvt_pk_bf16_f32 v117,v221,v223
v_mfma_f32_16x16x32_bf16 a[40:43],v[192:195],v[132:135],a[40:43]
buffer_load_dwordx4 v238,s[32:35],s62 offen offset:3072 lds
s_add_u32 s62,s62,20480
v_exp_f32 v216,v8
v_add_f32 v106,v74,v106
v_mfma_f32_16x16x32_bf16 a[72:75],v[192:195],v[136:139],a[72:75]
v_exp_f32 v217,v12
v_add_f32 v107,v75,v107
v_mfma_f32_16x16x32_bf16 a[104:107],v[192:195],v[140:143],a[104:107]
ds_read_b128 v[188:191],v226 offset:256
ds_read_b128 v[192:195],v226 offset:512
v_mfma_f32_16x16x32_bf16 a[12:15],v[196:199],v[128:131],a[12:15]
v_exp_f32 v218,v9
v_mfma_f32_16x16x32_bf16 a[44:47],v[196:199],v[132:135],a[44:47]
v_add_f32 v74,v216,v218
v_cvt_pk_bf16_f32 v120,v216,v218
v_mfma_f32_16x16x32_bf16 a[76:79],v[196:199],v[136:139],a[76:79]
v_exp_f32 v219,v13
s_waitcnt vmcnt(24)
v_cmp_eq_u32 s[72:73],s59,v253
s_nop 0
s_and_b32 s72,s72,15
s_cmp_eq_u32 s72,15
s_cbranch_scc0 .Lspa
.Lsba:
v_mfma_f32_16x16x32_bf16 a[108:111],v[196:199],v[140:143],a[108:111]
s_waitcnt lgkmcnt(7)
buffer_load_dwordx4 v[152:155],v239,s[48:51],s61 offen sc1
v_add_f32 v75,v217,v219
v_cvt_pk_bf16_f32 v124,v217,v219
v_mfma_f32_16x16x32_bf16 a[16:19],v[200:203],v[128:131],a[16:19]
v_exp_f32 v220,v10
v_mfma_f32_16x16x32_bf16 a[48:51],v[200:203],v[132:135],a[48:51]
v_exp_f32 v221,v14
v_add_f32 v74,v220,v74
v_mfma_f32_16x16x32_bf16 a[80:83],v[200:203],v[136:139],a[80:83]
buffer_load_dwordx4 v[156:159],v239,s[48:51],s61 offen offset:1024 sc1
v_add_f32 v75,v221,v75
v_mfma_f32_16x16x32_bf16 a[112:115],v[200:203],v[140:143],a[112:115]
v_exp_f32 v222,v11
v_mfma_f32_16x16x32_bf16 a[20:23],v[204:207],v[128:131],a[20:23]
ds_read_b128 v[196:199],v226 offset:768
v_add_f32 v74,v222,v74
v_cvt_pk_bf16_f32 v121,v220,v222
v_mfma_f32_16x16x32_bf16 a[52:55],v[204:207],v[132:135],a[52:55]
v_exp_f32 v223,v15
v_mfma_f32_16x16x32_bf16 a[84:87],v[204:207],v[136:139],a[84:87]
v_add_f32 v75,v223,v75
v_cvt_pk_bf16_f32 v125,v221,v223
v_mfma_f32_16x16x32_bf16 a[116:119],v[204:207],v[140:143],a[116:119]
s_waitcnt lgkmcnt(4)
v_exp_f32 v216,v64
v_add_f32 v108,v76,v108
v_mfma_f32_16x16x32_bf16 a[24:27],v[208:211],v[128:131],a[24:27]
ds_read_b128 v[200:203],v226 offset:4096
buffer_load_dwordx4 v[160:163],v239,s[48:51],s61 offen offset:2048 sc1
v_exp_f32 v217,v65
v_mfma_f32_16x16x32_bf16 a[56:59],v[208:211],v[132:135],a[56:59]
v_add_f32 v76,v216,v217
v_cvt_pk_bf16_f32 v144,v216,v217
v_mfma_f32_16x16x32_bf16 a[88:91],v[208:211],v[136:139],a[88:91]
v_exp_f32 v218,v66
v_mfma_f32_16x16x32_bf16 a[120:123],v[208:211],v[140:143],a[120:123]
ds_read_b128 v[204:207],v226 offset:4352
v_add_f32 v76,v218,v76
v_mfma_f32_16x16x32_bf16 a[28:31],v[212:215],v[128:131],a[28:31]
buffer_load_dwordx4 v[164:167],v239,s[48:51],s61 offen offset:3072 sc1
v_exp_f32 v219,v67
v_mfma_f32_16x16x32_bf16 a[60:63],v[212:215],v[132:135],a[60:63]
v_add_f32 v76,v219,v76
v_cvt_pk_bf16_f32 v145,v218,v219
v_mfma_f32_16x16x32_bf16 a[92:95],v[212:215],v[136:139],a[92:95]
v_exp_f32 v220,v16
v_mfma_f32_16x16x32_bf16 a[124:127],v[212:215],v[140:143],a[124:127]
s_waitcnt lgkmcnt(4)
v_exp_f32 v221,v20
v_add_f32 v72,v220,v72
v_mfma_f32_16x16x32_bf16 v[32:35],v[184:187],a[192:195],v[80:83]
ds_read_b128 v[208:211],v226 offset:4608
v_add_f32 v73,v221,v73
v_mfma_f32_16x16x32_bf16 v[32:35],v[188:191],a[196:199],v[32:35]
buffer_load_dwordx4 v[168:171],v252,s[48:51],s61 offen sc1
v_exp_f32 v222,v17
v_mfma_f32_16x16x32_bf16 v[36:39],v[184:187],a[208:211],v[84:87]
v_add_f32 v72,v222,v72
v_cvt_pk_bf16_f32 v114,v220,v222
v_mfma_f32_16x16x32_bf16 v[36:39],v[188:191],a[212:215],v[36:39]
v_exp_f32 v223,v21
v_mfma_f32_16x16x32_bf16 v[40:43],v[184:187],a[224:227],v[88:91]
ds_read_b128 v[212:215],v226 offset:4864
v_add_f32 v73,v223,v73
v_mfma_f32_16x16x32_bf16 v[40:43],v[188:191],a[228:231],v[40:43]
buffer_load_dwordx4 v[172:175],v252,s[48:51],s61 offen offset:1024 sc1
v_cvt_pk_bf16_f32 v118,v221,v223
v_exp_f32 v216,v18
v_mfma_f32_16x16x32_bf16 v[44:47],v[184:187],a[240:243],v[92:95]
s_add_u32 s61,s61,6144
v_exp_f32 v217,v22
v_mfma_f32_16x16x32_bf16 v[44:47],v[188:191],a[244:247],v[44:47]
s_waitcnt lgkmcnt(4)
v_add_f32 v72,v216,v72
v_add_f32 v73,v217,v73
v_mfma_f32_16x16x32_bf16 v[32:35],v[192:195],a[200:203],v[32:35]
v_exp_f32 v218,v19
v_mfma_f32_16x16x32_bf16 v[32:35],v[196:199],a[204:207],v[32:35]
v_add_f32 v72,v218,v72
v_cvt_pk_bf16_f32 v115,v216,v218
v_mfma_f32_16x16x32_bf16 v[36:39],v[192:195],a[216:219],v[36:39]
v_exp_f32 v219,v23
v_mfma_f32_16x16x32_bf16 v[36:39],v[196:199],a[220:223],v[36:39]
buffer_load_dwordx4 v[176:179],v239,s[48:51],s61 offen sc1
ds_read_b64_tr_b16 v[184:185],v233 offset:49152
ds_read_b64_tr_b16 v[186:187],v233 offset:53248
v_add_f32 v73,v219,v73
v_cvt_pk_bf16_f32 v119,v217,v219
v_mfma_f32_16x16x32_bf16 v[40:43],v[192:195],a[232:235],v[40:43]
ds_read_b64_tr_b16 v[188:189],v234 offset:49152
ds_read_b64_tr_b16 v[190:191],v234 offset:53248
v_mfma_f32_16x16x32_bf16 v[40:43],v[196:199],a[236:239],v[40:43]
s_cmp_eq_u32 s56,32768
s_cselect_b32 s57,0xffff8000,s58
v_exp_f32 v220,v24
v_max_f32 v228,v72,v73
v_exp_f32 v221,v28
v_mfma_f32_16x16x32_bf16 v[44:47],v[192:195],a[248:251],v[44:47]
v_add_f32 v74,v220,v74
v_add_f32 v75,v221,v75
v_mfma_f32_16x16x32_bf16 v[44:47],v[196:199],a[252:255],v[44:47]
s_waitcnt lgkmcnt(6)
buffer_load_dwordx4 v[180:183],v239,s[48:51],s61 offen offset:1024 sc1
v_add_u32 v232,s57,v232
v_exp_f32 v222,v25
v_mfma_f32_16x16x32_bf16 v[48:51],v[200:203],a[192:195],v[80:83]
v_add_f32 v74,v222,v74
v_cvt_pk_bf16_f32 v122,v220,v222
v_mfma_f32_16x16x32_bf16 v[48:51],v[204:207],a[196:199],v[48:51]
v_exp_f32 v223,v29
v_add_u32 v226,s57,v226
v_mfma_f32_16x16x32_bf16 v[52:55],v[200:203],a[208:211],v[84:87]
v_add_f32 v75,v223,v75
v_cvt_pk_bf16_f32 v126,v221,v223
v_mfma_f32_16x16x32_bf16 v[52:55],v[204:207],a[212:215],v[52:55]
ds_read_b64_tr_b16 v[192:193],v233 offset:49408
ds_read_b64_tr_b16 v[194:195],v233 offset:53504
buffer_load_dwordx4 a[176:179],v239,s[48:51],s61 offen offset:2048 sc1
v_exp_f32 v216,v26
v_mfma_f32_16x16x32_bf16 v[56:59],v[200:203],a[224:227],v[88:91]
ds_read_b64_tr_b16 v[196:197],v234 offset:49408
ds_read_b64_tr_b16 v[198:199],v234 offset:53504
v_mfma_f32_16x16x32_bf16 v[56:59],v[204:207],a[228:231],v[56:59]
v_exp_f32 v217,v30
v_add_f32 v74,v216,v74
v_mfma_f32_16x16x32_bf16 v[60:63],v[200:203],a[240:243],v[92:95]
buffer_load_dwordx4 a[180:183],v239,s[48:51],s61 offen offset:3072 sc1
v_add_f32 v75,v217,v75
v_mfma_f32_16x16x32_bf16 v[60:63],v[204:207],a[244:247],v[60:63]
s_waitcnt lgkmcnt(8)
v_exp_f32 v218,v27
v_mfma_f32_16x16x32_bf16 v[48:51],v[208:211],a[200:203],v[48:51]
v_add_f32 v74,v218,v74
v_cvt_pk_bf16_f32 v123,v216,v218
v_mfma_f32_16x16x32_bf16 v[48:51],v[212:215],a[204:207],v[48:51]
v_exp_f32 v219,v31
v_mfma_f32_16x16x32_bf16 v[52:55],v[208:211],a[216:219],v[52:55]
v_add_f32 v75,v219,v75
v_cvt_pk_bf16_f32 v127,v217,v219
v_mfma_f32_16x16x32_bf16 v[52:55],v[212:215],a[220:223],v[52:55]
ds_read_b64_tr_b16 v[200:201],v233 offset:49664
ds_read_b64_tr_b16 v[202:203],v233 offset:53760
v_exp_f32 v220,v68
v_max3_f32 v228,v228,v74,v75
v_exp_f32 v221,v69
v_mfma_f32_16x16x32_bf16 v[56:59],v[208:211],a[232:235],v[56:59]
v_exp_f32 v222,v70
v_exp_f32 v223,v71
v_mfma_f32_16x16x32_bf16 v[56:59],v[212:215],a[236:239],v[56:59]
ds_read_b64_tr_b16 v[204:205],v234 offset:49664
ds_read_b64_tr_b16 v[206:207],v234 offset:53760
buffer_load_dwordx4 a[184:187],v252,s[48:51],s61 offen sc1
v_add_f32 v76,v220,v76
v_add_f32 v76,v221,v76
v_cvt_pk_bf16_f32 v146,v220,v221
v_add_f32 v76,v222,v76
v_add_f32 v76,v223,v76
v_max_f32 v228,v228,v76
v_cmp_nge_f32 vcc,0x46800000,v228
)A"
R"A(v_mfma_f32_16x16x32_bf16 v[60:63],v[208:211],a[248:251],v[60:63]
buffer_load_dwordx4 a[188:191],v252,s[48:51],s61 offen offset:1024 sc1
v_cvt_pk_bf16_f32 v147,v222,v223
v_mfma_f32_16x16x32_bf16 v[60:63],v[212:215],a[252:255],v[60:63]
s_waitcnt lgkmcnt(8)
s_cbranch_vccnz .Lst17
.Lbk17:
s_add_u32 s61,s61,6144
v_mfma_f32_16x16x32_bf16 a[0:3],v[184:187],v[112:115],a[0:3]
s_barrier
ds_read_b64_tr_b16 v[208:209],v233 offset:49920
ds_read_b64_tr_b16 v[210:211],v233 offset:54016
v_mfma_f32_16x16x32_bf16 a[32:35],v[184:187],v[116:119],a[32:35]
ds_read_b64_tr_b16 v[212:213],v234 offset:49920
ds_read_b64_tr_b16 v[214:215],v234 offset:54016
v_mfma_f32_16x16x32_bf16 a[64:67],v[184:187],v[120:123],a[64:67]
v_exp_f32 v216,v32
v_mfma_f32_16x16x32_bf16 a[96:99],v[184:187],v[124:127],a[96:99]
v_add_f32 v104,v72,v104
v_add_f32 v241,v76,v108
v_mfma_f32_16x16x32_bf16 a[128:131],v[184:187],v[144:147],a[128:131]
v_mov_b32 v240,v96
v_add_f32 v105,v73,v105
v_mfma_f32_16x16x32_bf16 a[4:7],v[188:191],v[112:115],a[4:7]
ds_write_b64 v227,v[240:241]
v_mfma_f32_16x16x32_bf16 a[36:39],v[188:191],v[116:119],a[36:39]
buffer_load_dwordx4 a[160:163],v239,s[48:51],s61 offen sc1
v_exp_f32 v217,v36
v_mfma_f32_16x16x32_bf16 a[68:71],v[188:191],v[120:123],a[68:71]
v_exp_f32 v218,v33
v_mfma_f32_16x16x32_bf16 a[100:103],v[188:191],v[124:127],a[100:103]
buffer_load_dwordx4 a[164:167],v239,s[48:51],s61 offen offset:1024 sc1
v_exp_f32 v219,v37
v_add_f32 v72,v216,v218
v_cvt_pk_bf16_f32 v128,v216,v218
v_mfma_f32_16x16x32_bf16 a[132:135],v[188:191],v[144:147],a[132:135]
s_waitcnt lgkmcnt(9)
ds_read_b64_tr_b16 v[184:185],v230 offset:49152
ds_read_b64_tr_b16 v[186:187],v230 offset:53248
v_exp_f32 v220,v34
v_add_f32 v73,v217,v219
v_cvt_pk_bf16_f32 v132,v217,v219
v_mfma_f32_16x16x32_bf16 a[8:11],v[192:195],v[112:115],a[8:11]
v_exp_f32 v221,v38
v_add_f32 v72,v220,v72
v_mfma_f32_16x16x32_bf16 a[40:43],v[192:195],v[116:119],a[40:43]
buffer_load_dwordx4 a[168:171],v239,s[48:51],s61 offen offset:2048 sc1
v_exp_f32 v222,v35
v_add_f32 v73,v221,v73
v_mfma_f32_16x16x32_bf16 a[72:75],v[192:195],v[120:123],a[72:75]
v_exp_f32 v223,v39
v_add_f32 v72,v222,v72
v_cvt_pk_bf16_f32 v129,v220,v222
v_mfma_f32_16x16x32_bf16 a[104:107],v[192:195],v[124:127],a[104:107]
v_add_f32 v73,v223,v73
v_cvt_pk_bf16_f32 v133,v221,v223
v_mfma_f32_16x16x32_bf16 a[136:139],v[192:195],v[144:147],a[136:139]
ds_read_b64_tr_b16 v[188:189],v231 offset:49152
ds_read_b64_tr_b16 v[190:191],v231 offset:53248
buffer_load_dwordx4 a[172:175],v239,s[48:51],s61 offen offset:3072 sc1
v_exp_f32 v216,v40
v_add_f32 v106,v74,v106
v_mfma_f32_16x16x32_bf16 a[12:15],v[196:199],v[112:115],a[12:15]
v_exp_f32 v217,v44
s_and_b32 s72,s60,1
s_mul_i32 s73,s72,77824
s_add_u32 s73,s73,s63
s_add_u32 s73,s73,16384
v_add_u32 v246,s73,v239
s_lshl_b32 s73,s72,12
v_add_u32 v247,s73,v246
s_lshl_b32 s72,s72,13
s_sub_u32 s72,s63,s72
v_add_u32 v233,s72,v233
v_add_u32 v234,s72,v234
v_add_u32 v226,0x10000,v233
v_add_u32 v232,0x10000,v234
v_add_f32 v107,v75,v107
v_mfma_f32_16x16x32_bf16 a[44:47],v[196:199],v[116:119],a[44:47]
v_exp_f32 v218,v41
v_mfma_f32_16x16x32_bf16 a[76:79],v[196:199],v[120:123],a[76:79]
buffer_load_dwordx4 a[192:195],v252,s[48:51],s61 offen sc1
v_exp_f32 v219,v45
v_add_f32 v74,v216,v218
v_cvt_pk_bf16_f32 v136,v216,v218
v_mfma_f32_16x16x32_bf16 a[108:111],v[196:199],v[124:127],a[108:111]
v_exp_f32 v220,v42
v_add_f32 v75,v217,v219
v_cvt_pk_bf16_f32 v140,v217,v219
v_mfma_f32_16x16x32_bf16 a[140:143],v[196:199],v[144:147],a[140:143]
s_waitcnt lgkmcnt(9)
ds_read_b64_tr_b16 v[192:193],v230 offset:49408
ds_read_b64_tr_b16 v[194:195],v230 offset:53504
buffer_load_dwordx4 a[196:199],v252,s[48:51],s61 offen offset:1024 sc1
v_exp_f32 v221,v46
v_add_f32 v74,v220,v74
v_mfma_f32_16x16x32_bf16 a[16:19],v[200:203],v[112:115],a[16:19]
s_add_u32 s61,s61,6144
v_exp_f32 v222,v43
v_add_f32 v75,v221,v75
v_mfma_f32_16x16x32_bf16 a[48:51],v[200:203],v[116:119],a[48:51]
v_exp_f32 v223,v47
v_add_f32 v74,v222,v74
v_cvt_pk_bf16_f32 v137,v220,v222
v_mfma_f32_16x16x32_bf16 a[80:83],v[200:203],v[120:123],a[80:83]
buffer_load_dwordx4 a[200:203],v239,s[48:51],s61 offen sc1
v_add_f32 v75,v223,v75
v_cvt_pk_bf16_f32 v141,v221,v223
v_mfma_f32_16x16x32_bf16 a[112:115],v[200:203],v[124:127],a[112:115]
v_exp_f32 v216,v48
v_mfma_f32_16x16x32_bf16 a[144:147],v[200:203],v[144:147],a[144:147]
ds_read_b64_tr_b16 v[196:197],v231 offset:49408
ds_read_b64_tr_b16 v[198:199],v231 offset:53504
v_exp_f32 v217,v52
v_add_f32 v72,v216,v72
v_mfma_f32_16x16x32_bf16 a[20:23],v[204:207],v[112:115],a[20:23]
buffer_load_dwordx4 a[204:207],v239,s[48:51],s61 offen offset:1024 sc1
v_exp_f32 v218,v49
v_add_f32 v73,v217,v73
v_mfma_f32_16x16x32_bf16 a[52:55],v[204:207],v[116:119],a[52:55]
v_exp_f32 v219,v53
v_add_f32 v72,v218,v72
v_cvt_pk_bf16_f32 v130,v216,v218
v_mfma_f32_16x16x32_bf16 a[84:87],v[204:207],v[120:123],a[84:87]
v_exp_f32 v220,v50
v_add_f32 v73,v219,v73
v_cvt_pk_bf16_f32 v134,v217,v219
v_mfma_f32_16x16x32_bf16 a[116:119],v[204:207],v[124:127],a[116:119]
buffer_load_dwordx4 a[208:211],v239,s[48:51],s61 offen offset:2048 sc1
v_exp_f32 v221,v54
v_add_f32 v72,v220,v72
v_mfma_f32_16x16x32_bf16 a[148:151],v[204:207],v[144:147],a[148:151]
s_waitcnt lgkmcnt(9)
ds_read_b64_tr_b16 v[200:201],v230 offset:49664
ds_read_b64_tr_b16 v[202:203],v230 offset:53760
v_exp_f32 v222,v51
v_add_f32 v73,v221,v73
v_mfma_f32_16x16x32_bf16 a[24:27],v[208:211],v[112:115],a[24:27]
v_exp_f32 v223,v55
v_add_f32 v72,v222,v72
v_cvt_pk_bf16_f32 v131,v220,v222
v_mfma_f32_16x16x32_bf16 a[56:59],v[208:211],v[116:119],a[56:59]
buffer_load_dwordx4 a[212:215],v239,s[48:51],s61 offen offset:3072 sc1
v_add_f32 v73,v223,v73
v_cvt_pk_bf16_f32 v135,v221,v223
v_mfma_f32_16x16x32_bf16 a[88:91],v[208:211],v[120:123],a[88:91]
v_exp_f32 v216,v56
v_max_f32 v228,v72,v73
v_mfma_f32_16x16x32_bf16 a[120:123],v[208:211],v[124:127],a[120:123]
buffer_load_dwordx4 a[216:219],v252,s[48:51],s61 offen sc1
v_exp_f32 v217,v60
v_add_f32 v74,v216,v74
v_mfma_f32_16x16x32_bf16 a[152:155],v[208:211],v[144:147],a[152:155]
ds_read_b64_tr_b16 v[204:205],v231 offset:49664
ds_read_b64_tr_b16 v[206:207],v231 offset:53760
v_exp_f32 v218,v57
v_add_f32 v75,v217,v75
v_mfma_f32_16x16x32_bf16 a[28:31],v[212:215],v[112:115],a[28:31]
v_exp_f32 v219,v61
v_add_f32 v74,v218,v74
v_cvt_pk_bf16_f32 v138,v216,v218
v_mfma_f32_16x16x32_bf16 a[60:63],v[212:215],v[116:119],a[60:63]
buffer_load_dwordx4 a[220:223],v252,s[48:51],s61 offen offset:1024 sc1
v_exp_f32 v220,v58
v_exp_f32 v221,v62
v_exp_f32 v222,v59
v_exp_f32 v223,v63
v_add_f32 v75,v219,v75
v_cvt_pk_bf16_f32 v142,v217,v219
v_mfma_f32_16x16x32_bf16 a[92:95],v[212:215],v[120:123],a[92:95]
s_add_u32 s61,s61,30720
v_add_f32 v74,v220,v74
v_add_f32 v75,v221,v75
v_add_f32 v74,v222,v74
v_add_f32 v75,v223,v75
v_max3_f32 v228,v228,v74,v75
v_cmp_nge_f32 vcc,0x46800000,v228
v_mfma_f32_16x16x32_bf16 a[124:127],v[212:215],v[124:127],a[124:127]
v_cvt_pk_bf16_f32 v139,v220,v222
v_cvt_pk_bf16_f32 v143,v221,v223
v_mfma_f32_16x16x32_bf16 a[156:159],v[212:215],v[144:147],a[156:159]
s_waitcnt lgkmcnt(8)
s_cbranch_vccnz .Lst18
.Lbk18:
ds_read_b64_tr_b16 v[208:209],v230 offset:49920
ds_read_b64_tr_b16 v[210:211],v230 offset:54016
buffer_load_dwordx4 a[224:227],v239,s[48:51],s61 offen sc1
v_mfma_f32_16x16x32_bf16 a[0:3],v[184:187],v[128:131],a[0:3]
buffer_load_dwordx4 a[228:231],v239,s[48:51],s61 offen offset:1024 sc1
ds_read_b64_tr_b16 v[212:213],v231 offset:49920
v_mfma_f32_16x16x32_bf16 a[32:35],v[184:187],v[132:135],a[32:35]
ds_read_b64_tr_b16 v[214:215],v231 offset:54016
v_add_u32 v230,0xffff0000,v233
v_add_u32 v231,0xffff0000,v234
v_mfma_f32_16x16x32_bf16 a[64:67],v[184:187],v[136:139],a[64:67]
s_waitcnt vmcnt(42)
buffer_load_dwordx4 a[232:235],v239,s[48:51],s61 offen offset:2048 sc1
v_add_f32 v104,v72,v104
v_mfma_f32_16x16x32_bf16 a[96:99],v[184:187],v[140:143],a[96:99]
v_add_f32 v105,v73,v105
v_add_f32 v106,v74,v106
v_mfma_f32_16x16x32_bf16 a[4:7],v[188:191],v[128:131],a[4:7]
v_add_u32 v229,135168,v239
v_mov_b32 v228,0
v_mfma_f32_16x16x32_bf16 a[36:39],v[188:191],v[132:135],a[36:39]
ds_write_b32 v229,v228
v_mfma_f32_16x16x32_bf16 a[68:71],v[188:191],v[136:139],a[68:71]
s_waitcnt vmcnt(35)
v_xor_b32 v248,114688,v247
v_xor_b32 v250,512,v227
v_add_f32 v107,v75,v107
v_mov_b32 v72,v104
v_mov_b32 v73,v105
v_mfma_f32_16x16x32_bf16 a[100:103],v[188:191],v[140:143],a[100:103]
s_waitcnt lgkmcnt(9)
ds_write_b128 v246,a[128:131]
buffer_load_dwordx4 a[236:239],v239,s[48:51],s61 offen offset:3072 sc1
v_mov_b32 v74,v106
v_mov_b32 v75,v107
v_mfma_f32_16x16x32_bf16 a[8:11],v[192:195],v[128:131],a[8:11]
ds_read_b64_tr_b16 v[184:185],v230 offset:49152
ds_read_b64_tr_b16 v[186:187],v226 offset:16384
v_permlane32_swap_b32 v72,v104
v_permlane32_swap_b32 v73,v105
v_mfma_f32_16x16x32_bf16 a[40:43],v[192:195],v[132:135],a[40:43]
ds_write_b128 v246,a[132:135] offset:1024
v_permlane32_swap_b32 v74,v106
v_mfma_f32_16x16x32_bf16 a[72:75],v[192:195],v[136:139],a[72:75]
buffer_load_dwordx4 a[240:243],v252,s[48:51],s61 offen sc1
buffer_load_dwordx4 a[244:247],v252,s[48:51],s61 offen offset:1024 sc1
s_add_u32 s61,s61,6144
v_permlane32_swap_b32 v75,v107
v_mfma_f32_16x16x32_bf16 a[104:107],v[192:195],v[140:143],a[104:107]
ds_write_b128 v246,a[136:139] offset:2048
v_add_f32 v104,v104,v72
v_add_f32 v105,v105,v73
v_add_f32 v106,v106,v74
v_mfma_f32_16x16x32_bf16 a[12:15],v[196:199],v[128:131],a[12:15]
ds_read_b64_tr_b16 v[188:189],v230
ds_read_b64_tr_b16 v[190:191],v233 offset:16384
v_add_f32 v107,v107,v75
v_mov_b32 v72,v104
v_mfma_f32_16x16x32_bf16 a[44:47],v[196:199],v[132:135],a[44:47]
ds_write_b128 v246,a[140:143] offset:3072
v_mov_b32 v73,v105
v_mov_b32 v74,v106
v_mov_b32 v75,v107
v_mfma_f32_16x16x32_bf16 a[76:79],v[196:199],v[136:139],a[76:79]
v_permlane16_swap_b32 v72,v104
v_mfma_f32_16x16x32_bf16 a[108:111],v[196:199],v[140:143],a[108:111]
s_waitcnt lgkmcnt(13)
ds_write_b128 v246,a[144:147] offset:4096
v_permlane16_swap_b32 v73,v105
v_permlane16_swap_b32 v74,v106
v_mfma_f32_16x16x32_bf16 a[16:19],v[200:203],v[128:131],a[16:19]
ds_read_b64_tr_b16 v[192:193],v231 offset:49152
ds_read_b64_tr_b16 v[194:195],v232 offset:16384
v_permlane16_swap_b32 v75,v107
v_mfma_f32_16x16x32_bf16 a[48:51],v[200:203],v[132:135],a[48:51]
ds_write_b128 v246,a[148:151] offset:5120
v_add_f32 v104,v104,v72
v_add_f32 v105,v105,v73
v_mfma_f32_16x16x32_bf16 a[80:83],v[200:203],v[136:139],a[80:83]
v_add_f32 v106,v106,v74
v_add_f32 v107,v107,v75
v_rcp_f32 v104,v104
v_mfma_f32_16x16x32_bf16 a[112:115],v[200:203],v[140:143],a[112:115]
ds_write_b128 v246,a[152:155] offset:6144
ds_write_b128 v246,a[156:159] offset:7168
v_rcp_f32 v105,v105
v_mfma_f32_16x16x32_bf16 a[20:23],v[204:207],v[128:131],a[20:23]
s_waitcnt lgkmcnt(0)
s_barrier
ds_read_b64_tr_b16 v[196:197],v231
ds_read_b64_tr_b16 v[198:199],v234 offset:16384
v_rcp_f32 v106,v106
v_mfma_f32_16x16x32_bf16 a[52:55],v[204:207],v[132:135],a[52:55]
s_add_u32 m0,s63,114688
v_rcp_f32 v107,v107
v_mfma_f32_16x16x32_bf16 a[84:87],v[204:207],v[136:139],a[84:87]
v_accvgpr_read_b32 v64,a0
v_accvgpr_read_b32 v65,a1
v_accvgpr_read_b32 v66,a2
v_mfma_f32_16x16x32_bf16 a[116:119],v[204:207],v[140:143],a[116:119]
v_accvgpr_read_b32 v67,a3
v_accvgpr_read_b32 v68,a4
v_mfma_f32_16x16x32_bf16 a[24:27],v[208:211],v[128:131],a[24:27]
ds_read_b64_tr_b16 v[200:201],v230 offset:49408
ds_read_b64_tr_b16 v[202:203],v226 offset:16640
v_accvgpr_read_b32 v69,a5
v_accvgpr_read_b32 v70,a6
v_accvgpr_read_b32 v71,a7
v_mfma_f32_16x16x32_bf16 a[56:59],v[208:211],v[132:135],a[56:59]
v_mul_f32 v64,v104,v64
v_mul_f32 v65,v104,v65
v_mfma_f32_16x16x32_bf16 a[88:91],v[208:211],v[136:139],a[88:91]
ds_read_b128 v[112:115],v247
ds_read_b128 v[116:119],v247 offset:1024
ds_read_b128 v[120:123],v248
ds_read_b128 v[124:127],v248 offset:1024
ds_read_b128 v[80:83],v247 offset:2048
ds_read_b128 v[84:87],v247 offset:3072
ds_read_b128 v[88:91],v248 offset:2048
ds_read_b128 v[92:95],v248 offset:3072
ds_read_b64 v[240:241],v227
ds_read_b64 v[242:243],v250
buffer_load_dwordx4 v235,s[32:35],s62 offen lds
v_mul_f32 v66,v104,v66
v_mul_f32 v67,v104,v67
v_mul_f32 v68,v104,v68
)A"
R"A(v_mfma_f32_16x16x32_bf16 a[120:123],v[208:211],v[140:143],a[120:123]
v_mul_f32 v69,v104,v69
v_mul_f32 v70,v104,v70
v_mfma_f32_16x16x32_bf16 a[28:31],v[212:215],v[128:131],a[28:31]
ds_read_b64_tr_b16 v[204:205],v230 offset:256
ds_read_b64_tr_b16 v[206:207],v233 offset:16640
v_mul_f32 v71,v104,v71
v_cvt_pk_bf16_f32 v64,v64,v65
v_cvt_pk_bf16_f32 v65,v66,v67
v_mfma_f32_16x16x32_bf16 a[60:63],v[212:215],v[132:135],a[60:63]
buffer_load_dwordx4 v236,s[32:35],s62 offen offset:1024 lds
v_cvt_pk_bf16_f32 v66,v68,v69
v_cvt_pk_bf16_f32 v67,v70,v71
v_mfma_f32_16x16x32_bf16 a[92:95],v[212:215],v[136:139],a[92:95]
s_add_u32 s66,s27,24576
buffer_store_dwordx4 v[64:67],v239,s[52:55],s66 offen sc1
v_accvgpr_read_b32 v148,a32
v_accvgpr_read_b32 v149,a33
v_accvgpr_read_b32 v150,a34
v_mfma_f32_16x16x32_bf16 a[124:127],v[212:215],v[140:143],a[124:127]
v_accvgpr_read_b32 v151,a35
v_accvgpr_read_b32 v100,a36
s_waitcnt vmcnt(32)
v_mfma_f32_16x16x32_bf16 v[0:3],v[184:187],v[152:155],0
s_waitcnt lgkmcnt(2)
ds_read_b64_tr_b16 v[208:209],v231 offset:49408
ds_read_b64_tr_b16 v[210:211],v232 offset:16640
s_waitcnt vmcnt(26)
v_mfma_f32_16x16x32_bf16 v[0:3],v[188:191],v[176:179],v[0:3]
buffer_load_dwordx4 v237,s[32:35],s62 offen offset:2048 lds
v_accvgpr_read_b32 v101,a37
v_accvgpr_read_b32 v102,a38
v_accvgpr_read_b32 v103,a39
v_mul_f32 v148,v105,v148
v_mul_f32 v149,v105,v149
v_mfma_f32_16x16x32_bf16 v[8:11],v[184:187],v[156:159],0
v_mul_f32 v150,v105,v150
v_mul_f32 v151,v105,v151
s_waitcnt vmcnt(26)
v_mfma_f32_16x16x32_bf16 v[8:11],v[188:191],v[180:183],v[8:11]
buffer_load_dwordx4 v238,s[32:35],s62 offen offset:3072 lds
v_mul_f32 v100,v105,v100
v_mul_f32 v101,v105,v101
v_mul_f32 v102,v105,v102
v_mfma_f32_16x16x32_bf16 v[16:19],v[184:187],v[160:163],0
s_add_u32 s62,s62,20480
ds_read_b64_tr_b16 v[212:213],v231 offset:256
ds_read_b64_tr_b16 v[214:215],v234 offset:16640
s_waitcnt vmcnt(26)
v_mfma_f32_16x16x32_bf16 v[16:19],v[188:191],a[176:179],v[16:19]
s_add_u32 m0,s63,16384
v_mul_f32 v103,v105,v103
v_cvt_pk_bf16_f32 v148,v148,v149
v_cvt_pk_bf16_f32 v149,v150,v151
v_cvt_pk_bf16_f32 v150,v100,v101
v_cvt_pk_bf16_f32 v151,v102,v103
v_mfma_f32_16x16x32_bf16 v[24:27],v[184:187],v[164:167],0
s_add_u32 s66,s27,24576
buffer_store_dwordx4 v[148:151],v239,s[52:55],s66 offen offset:1024 sc1
s_waitcnt vmcnt(26)
v_mfma_f32_16x16x32_bf16 v[24:27],v[188:191],a[180:183],v[24:27]
buffer_load_dwordx4 v235,s[32:35],s62 offen lds
v_accvgpr_read_b32 v68,a64
v_accvgpr_read_b32 v69,a65
v_accvgpr_read_b32 v70,a66
v_accvgpr_read_b32 v71,a67
v_accvgpr_read_b32 v100,a68
v_mfma_f32_16x16x32_bf16 v[32:35],v[184:187],v[168:171],0
v_accvgpr_read_b32 v101,a69
v_accvgpr_read_b32 v102,a70
s_waitcnt vmcnt(26)
v_mfma_f32_16x16x32_bf16 v[32:35],v[188:191],a[184:187],v[32:35]
buffer_load_dwordx4 v236,s[32:35],s62 offen offset:1024 lds
v_accvgpr_read_b32 v103,a71
v_mul_f32 v68,v106,v68
v_mul_f32 v69,v106,v69
v_mfma_f32_16x16x32_bf16 v[40:43],v[184:187],v[172:175],0
v_mul_f32 v70,v106,v70
v_mul_f32 v71,v106,v71
s_waitcnt vmcnt(26)
v_mfma_f32_16x16x32_bf16 v[40:43],v[188:191],a[188:191],v[40:43]
buffer_load_dwordx4 v237,s[32:35],s62 offen offset:2048 lds
v_mul_f32 v100,v106,v100
v_mul_f32 v101,v106,v101
v_mul_f32 v102,v106,v102
v_mfma_f32_16x16x32_bf16 v[4:7],v[192:195],v[152:155],0
v_mul_f32 v103,v106,v103
v_cvt_pk_bf16_f32 v68,v68,v69
v_mfma_f32_16x16x32_bf16 v[4:7],v[196:199],v[176:179],v[4:7]
buffer_load_dwordx4 v238,s[32:35],s62 offen offset:3072 lds
v_cvt_pk_bf16_f32 v69,v70,v71
v_cvt_pk_bf16_f32 v70,v100,v101
v_cvt_pk_bf16_f32 v71,v102,v103
v_mfma_f32_16x16x32_bf16 v[12:15],v[192:195],v[156:159],0
s_add_u32 s62,s62,184320
s_add_u32 m0,s63,98304
v_accvgpr_read_b32 v64,a96
v_mfma_f32_16x16x32_bf16 v[12:15],v[196:199],v[180:183],v[12:15]
s_add_u32 s66,s29,24576
buffer_store_dwordx4 v[68:71],v239,s[52:55],s66 offen sc1
buffer_load_dwordx4 v235,s[32:35],s62 offen lds
v_accvgpr_read_b32 v65,a97
v_accvgpr_read_b32 v66,a98
v_accvgpr_read_b32 v67,a99
v_accvgpr_read_b32 v100,a100
v_mfma_f32_16x16x32_bf16 v[20:23],v[192:195],v[160:163],0
v_accvgpr_read_b32 v101,a101
v_accvgpr_read_b32 v102,a102
v_mfma_f32_16x16x32_bf16 v[20:23],v[196:199],a[176:179],v[20:23]
buffer_load_dwordx4 v236,s[32:35],s62 offen offset:1024 lds
v_accvgpr_read_b32 v103,a103
v_mul_f32 v64,v107,v64
v_mul_f32 v65,v107,v65
v_mfma_f32_16x16x32_bf16 v[28:31],v[192:195],v[164:167],0
v_mul_f32 v66,v107,v66
v_mul_f32 v67,v107,v67
v_mfma_f32_16x16x32_bf16 v[28:31],v[196:199],a[180:183],v[28:31]
buffer_load_dwordx4 v237,s[32:35],s62 offen offset:2048 lds
v_mul_f32 v100,v107,v100
v_mul_f32 v101,v107,v101
v_mul_f32 v102,v107,v102
v_mfma_f32_16x16x32_bf16 v[36:39],v[192:195],v[168:171],0
v_mul_f32 v103,v107,v103
v_cvt_pk_bf16_f32 v64,v64,v65
v_mfma_f32_16x16x32_bf16 v[36:39],v[196:199],a[184:187],v[36:39]
buffer_load_dwordx4 v238,s[32:35],s62 offen offset:3072 lds
v_cvt_pk_bf16_f32 v65,v66,v67
v_cvt_pk_bf16_f32 v66,v100,v101
v_cvt_pk_bf16_f32 v67,v102,v103
v_mfma_f32_16x16x32_bf16 v[44:47],v[192:195],v[172:175],0
s_add_u32 s62,s62,20480
s_add_u32 s66,s29,24576
buffer_store_dwordx4 v[64:67],v239,s[52:55],s66 offen offset:1024 sc1
v_mfma_f32_16x16x32_bf16 v[44:47],v[196:199],a[188:191],v[44:47]
s_waitcnt lgkmcnt(4)
v_accvgpr_read_b32 v148,a8
v_accvgpr_read_b32 v149,a9
v_accvgpr_read_b32 v150,a10
v_accvgpr_read_b32 v151,a11
v_accvgpr_read_b32 v100,a12
v_mfma_f32_16x16x32_bf16 v[48:51],v[200:203],v[152:155],0
v_accvgpr_read_b32 v101,a13
v_accvgpr_read_b32 v102,a14
v_mfma_f32_16x16x32_bf16 v[48:51],v[204:207],v[176:179],v[48:51]
v_accvgpr_read_b32 v103,a15
v_mul_f32 v148,v104,v148
v_mul_f32 v149,v104,v149
v_mfma_f32_16x16x32_bf16 v[56:59],v[200:203],v[156:159],0
v_mul_f32 v150,v104,v150
v_mul_f32 v151,v104,v151
v_mfma_f32_16x16x32_bf16 v[56:59],v[204:207],v[180:183],v[56:59]
v_mul_f32 v100,v104,v100
v_mul_f32 v101,v104,v101
v_mul_f32 v102,v104,v102
v_mfma_f32_16x16x32_bf16 v[128:131],v[200:203],v[160:163],0
v_mul_f32 v103,v104,v103
v_cvt_pk_bf16_f32 v148,v148,v149
v_mfma_f32_16x16x32_bf16 v[128:131],v[204:207],a[176:179],v[128:131]
ds_read_b64_tr_b16 v[184:185],v230 offset:49664
ds_read_b64_tr_b16 v[186:187],v226 offset:16896
v_cvt_pk_bf16_f32 v149,v150,v151
v_cvt_pk_bf16_f32 v150,v100,v101
v_cvt_pk_bf16_f32 v151,v102,v103
v_mfma_f32_16x16x32_bf16 v[136:139],v[200:203],v[164:167],0
ds_read_b64_tr_b16 v[188:189],v230 offset:512
ds_read_b64_tr_b16 v[190:191],v233 offset:16896
v_mfma_f32_16x16x32_bf16 v[136:139],v[204:207],a[180:183],v[136:139]
s_add_u32 s66,s27,30720
buffer_store_dwordx4 v[148:151],v239,s[52:55],s66 offen sc1
v_accvgpr_read_b32 v68,a40
v_accvgpr_read_b32 v69,a41
v_accvgpr_read_b32 v70,a42
v_accvgpr_read_b32 v71,a43
v_accvgpr_read_b32 v100,a44
v_mfma_f32_16x16x32_bf16 v[96:99],v[200:203],v[168:171],0
v_accvgpr_read_b32 v101,a45
v_accvgpr_read_b32 v102,a46
v_mfma_f32_16x16x32_bf16 v[96:99],v[204:207],a[184:187],v[96:99]
v_accvgpr_read_b32 v103,a47
v_mul_f32 v68,v105,v68
v_mul_f32 v69,v105,v69
v_mfma_f32_16x16x32_bf16 v[216:219],v[200:203],v[172:175],0
v_mul_f32 v70,v105,v70
v_mul_f32 v71,v105,v71
v_mfma_f32_16x16x32_bf16 v[216:219],v[204:207],a[188:191],v[216:219]
s_waitcnt lgkmcnt(4)
v_mul_f32 v100,v105,v100
v_mul_f32 v101,v105,v101
v_mul_f32 v102,v105,v102
v_mfma_f32_16x16x32_bf16 v[52:55],v[208:211],v[152:155],0
v_mul_f32 v103,v105,v103
v_cvt_pk_bf16_f32 v68,v68,v69
v_mfma_f32_16x16x32_bf16 v[52:55],v[212:215],v[176:179],v[52:55]
v_cvt_pk_bf16_f32 v69,v70,v71
v_cvt_pk_bf16_f32 v70,v100,v101
v_cvt_pk_bf16_f32 v71,v102,v103
v_mfma_f32_16x16x32_bf16 v[60:63],v[208:211],v[156:159],0
s_add_u32 s66,s27,30720
buffer_store_dwordx4 v[68:71],v239,s[52:55],s66 offen offset:1024 sc1
v_mfma_f32_16x16x32_bf16 v[60:63],v[212:215],v[180:183],v[60:63]
ds_read_b64_tr_b16 v[192:193],v231 offset:49664
ds_read_b64_tr_b16 v[194:195],v232 offset:16896
v_mfma_f32_16x16x32_bf16 v[132:135],v[208:211],v[160:163],0
ds_read_b64_tr_b16 v[196:197],v231 offset:512
ds_read_b64_tr_b16 v[198:199],v234 offset:16896
v_mfma_f32_16x16x32_bf16 v[132:135],v[212:215],a[176:179],v[132:135]
v_min_f32 v244,v240,v242
v_sub_f32 v240,v244,v240
v_mfma_f32_16x16x32_bf16 v[140:143],v[208:211],v[164:167],0
v_sub_f32 v242,v244,v242
v_mfma_f32_16x16x32_bf16 v[140:143],v[212:215],a[180:183],v[140:143]
v_exp_f32 v240,v240
v_mfma_f32_16x16x32_bf16 v[144:147],v[208:211],v[168:171],0
v_exp_f32 v242,v242
v_mfma_f32_16x16x32_bf16 v[144:147],v[212:215],a[184:187],v[144:147]
v_mul_f32 v244,v240,v241
v_fma_f32 v244,v242,v243,v244
v_mfma_f32_16x16x32_bf16 v[220:223],v[208:211],v[172:175],0
v_mov_b32 v245,v244
v_mfma_f32_16x16x32_bf16 v[220:223],v[212:215],a[188:191],v[220:223]
s_waitcnt lgkmcnt(4)
v_permlane32_swap_b32 v245,v244
v_mfma_f32_16x16x32_bf16 a[128:131],v[184:187],v[152:155],0
v_add_f32 v244,v244,v245
v_mov_b32 v245,v244
v_mfma_f32_16x16x32_bf16 a[128:131],v[188:191],v[176:179],a[128:131]
s_nop 0
v_permlane16_swap_b32 v245,v244
v_mfma_f32_16x16x32_bf16 a[136:139],v[184:187],v[156:159],0
ds_read_b64_tr_b16 v[200:201],v230 offset:49920
ds_read_b64_tr_b16 v[202:203],v226 offset:17152
v_mfma_f32_16x16x32_bf16 a[136:139],v[188:191],v[180:183],a[136:139]
ds_read_b64_tr_b16 v[204:205],v230 offset:768
v_add_f32 v244,v244,v245
v_mfma_f32_16x16x32_bf16 a[144:147],v[184:187],v[160:163],0
v_rcp_f32 v244,v244
v_mfma_f32_16x16x32_bf16 a[144:147],v[188:191],a[176:179],a[144:147]
ds_read_b64_tr_b16 v[206:207],v233 offset:17152
v_mul_f32 v240,v244,v240
v_mfma_f32_16x16x32_bf16 a[152:155],v[184:187],v[164:167],0
v_mul_f32 v242,v244,v242
v_mul_f32 v112,v240,v112
v_mfma_f32_16x16x32_bf16 a[152:155],v[188:191],a[180:183],a[152:155]
v_mul_f32 v113,v240,v113
v_mul_f32 v114,v240,v114
v_mfma_f32_16x16x32_bf16 a[248:251],v[184:187],v[168:171],0
v_mul_f32 v115,v240,v115
v_mul_f32 v116,v240,v116
v_mfma_f32_16x16x32_bf16 a[248:251],v[188:191],a[184:187],a[248:251]
v_mul_f32 v117,v240,v117
v_mul_f32 v118,v240,v118
v_mfma_f32_16x16x32_bf16 v[72:75],v[184:187],v[172:175],0
v_mul_f32 v119,v240,v119
v_fma_f32 v112,v242,v120,v112
v_mfma_f32_16x16x32_bf16 v[72:75],v[188:191],a[188:191],v[72:75]
s_waitcnt lgkmcnt(4)
v_fma_f32 v113,v242,v121,v113
v_fma_f32 v114,v242,v122,v114
v_mfma_f32_16x16x32_bf16 a[132:135],v[192:195],v[152:155],0
v_fma_f32 v115,v242,v123,v115
v_fma_f32 v116,v242,v124,v116
v_mfma_f32_16x16x32_bf16 a[132:135],v[196:199],v[176:179],a[132:135]
v_fma_f32 v117,v242,v125,v117
v_fma_f32 v118,v242,v126,v118
v_mfma_f32_16x16x32_bf16 a[140:143],v[192:195],v[156:159],0
v_fma_f32 v119,v242,v127,v119
v_cvt_pk_bf16_f32 v112,v112,v113
v_mfma_f32_16x16x32_bf16 a[140:143],v[196:199],v[180:183],a[140:143]
v_cvt_pk_bf16_f32 v113,v114,v115
v_cvt_pk_bf16_f32 v114,v116,v117
v_mfma_f32_16x16x32_bf16 a[148:151],v[192:195],v[160:163],0
ds_read_b64_tr_b16 v[208:209],v231 offset:49920
ds_read_b64_tr_b16 v[210:211],v232 offset:17152
v_mfma_f32_16x16x32_bf16 a[148:151],v[196:199],a[176:179],a[148:151]
v_cvt_pk_bf16_f32 v115,v118,v119
v_mul_f32 v80,v240,v80
v_mfma_f32_16x16x32_bf16 a[156:159],v[192:195],v[164:167],0
ds_read_b64_tr_b16 v[212:213],v231 offset:768
ds_read_b64_tr_b16 v[214:215],v234 offset:17152
v_mfma_f32_16x16x32_bf16 a[156:159],v[196:199],a[180:183],a[156:159]
v_mul_f32 v81,v240,v81
v_mul_f32 v82,v240,v82
v_mul_f32 v83,v240,v83
v_mul_f32 v84,v240,v84
v_mfma_f32_16x16x32_bf16 a[252:255],v[192:195],v[168:171],0
v_mul_f32 v85,v240,v85
v_mul_f32 v86,v240,v86
v_mfma_f32_16x16x32_bf16 a[252:255],v[196:199],a[184:187],a[252:255]
s_waitcnt lgkmcnt(2)
v_mul_f32 v87,v240,v87
v_fma_f32 v80,v242,v88,v80
v_mfma_f32_16x16x32_bf16 v[76:79],v[192:195],v[172:175],0
s_waitcnt lgkmcnt(0)
v_fma_f32 v81,v242,v89,v81
v_fma_f32 v82,v242,v90,v82
v_mfma_f32_16x16x32_bf16 v[76:79],v[196:199],a[188:191],v[76:79]
s_add_u32 s66,s81,24576
buffer_store_dwordx4 v[112:115],v239,s[52:55],s66 offen sc1
v_fma_f32 v83,v242,v91,v83
v_fma_f32 v84,v242,v92,v84
v_mfma_f32_16x16x32_bf16 a[0:3],v[200:203],v[152:155],0
s_waitcnt vmcnt(9)
v_fma_f32 v85,v242,v93,v85
v_fma_f32 v86,v242,v94,v86
v_mfma_f32_16x16x32_bf16 a[0:3],v[204:207],v[176:179],a[0:3]
s_add_u32 m0,s63,49152
v_fma_f32 v87,v242,v95,v87
v_cvt_pk_bf16_f32 v80,v80,v81
)A"
R"A(v_mfma_f32_16x16x32_bf16 a[32:35],v[200:203],v[156:159],0
v_cvt_pk_bf16_f32 v81,v82,v83
v_cvt_pk_bf16_f32 v82,v84,v85
v_mfma_f32_16x16x32_bf16 a[32:35],v[204:207],v[180:183],a[32:35]
buffer_load_dwordx4 v235,s[32:35],s62 offen lds
v_accvgpr_read_b32 v64,a72
v_accvgpr_read_b32 v65,a73
v_cvt_pk_bf16_f32 v83,v86,v87
v_mfma_f32_16x16x32_bf16 a[64:67],v[200:203],v[160:163],0
v_accvgpr_read_b32 v66,a74
v_accvgpr_read_b32 v67,a75
v_mfma_f32_16x16x32_bf16 a[64:67],v[204:207],a[176:179],a[64:67]
ds_read_b64_tr_b16 v[184:185],v230 offset:32768
ds_read_b64_tr_b16 v[186:187],v233
v_accvgpr_read_b32 v100,a76
v_mfma_f32_16x16x32_bf16 a[96:99],v[200:203],v[164:167],0
ds_read_b64_tr_b16 v[188:189],v233 offset:49152
ds_read_b64_tr_b16 v[190:191],v230 offset:16384
v_mfma_f32_16x16x32_bf16 a[96:99],v[204:207],a[180:183],a[96:99]
buffer_load_dwordx4 v236,s[32:35],s62 offen offset:1024 lds
v_accvgpr_read_b32 v101,a77
v_accvgpr_read_b32 v102,a78
v_accvgpr_read_b32 v103,a79
v_mul_f32 v64,v106,v64
v_mfma_f32_16x16x32_bf16 a[8:11],v[200:203],v[168:171],0
v_mul_f32 v65,v106,v65
v_mul_f32 v66,v106,v66
v_mfma_f32_16x16x32_bf16 a[8:11],v[204:207],a[184:187],a[8:11]
buffer_load_dwordx4 v237,s[32:35],s62 offen offset:2048 lds
v_mul_f32 v67,v106,v67
v_mfma_f32_16x16x32_bf16 a[40:43],v[200:203],v[172:175],0
v_mul_f32 v100,v106,v100
v_mul_f32 v101,v106,v101
v_mfma_f32_16x16x32_bf16 a[40:43],v[204:207],a[188:191],a[40:43]
v_mul_f32 v102,v106,v102
v_mul_f32 v103,v106,v103
v_mfma_f32_16x16x32_bf16 a[4:7],v[208:211],v[152:155],0
v_cvt_pk_bf16_f32 v64,v64,v65
v_cvt_pk_bf16_f32 v65,v66,v67
v_mfma_f32_16x16x32_bf16 a[4:7],v[212:215],v[176:179],a[4:7]
v_cvt_pk_bf16_f32 v66,v100,v101
v_cvt_pk_bf16_f32 v67,v102,v103
v_mfma_f32_16x16x32_bf16 a[36:39],v[208:211],v[156:159],0
s_add_u32 s66,s29,30720
buffer_store_dwordx4 v[64:67],v239,s[52:55],s66 offen sc1
v_mfma_f32_16x16x32_bf16 a[36:39],v[212:215],v[180:183],a[36:39]
s_add_u32 s66,s81,30720
buffer_store_dwordx4 v[80:83],v239,s[52:55],s66 offen sc1
buffer_load_dwordx4 v238,s[32:35],s62 offen offset:3072 lds
v_accvgpr_read_b32 v148,a104
v_accvgpr_read_b32 v149,a105
v_mfma_f32_16x16x32_bf16 a[68:71],v[208:211],v[160:163],0
s_add_u32 s62,s62,20480
v_accvgpr_read_b32 v150,a106
v_accvgpr_read_b32 v151,a107
v_mfma_f32_16x16x32_bf16 a[68:71],v[212:215],a[176:179],a[68:71]
s_add_u32 m0,s63,147456
ds_read_b64_tr_b16 v[192:193],v231 offset:32768
ds_read_b64_tr_b16 v[194:195],v234
v_accvgpr_read_b32 v100,a108
v_mfma_f32_16x16x32_bf16 a[100:103],v[208:211],v[164:167],0
ds_read_b64_tr_b16 v[196:197],v234 offset:49152
ds_read_b64_tr_b16 v[198:199],v231 offset:16384
v_mfma_f32_16x16x32_bf16 a[100:103],v[212:215],a[180:183],a[100:103]
buffer_load_dwordx4 v235,s[32:35],s62 offen lds
v_accvgpr_read_b32 v101,a109
v_accvgpr_read_b32 v102,a110
v_accvgpr_read_b32 v103,a111
v_mul_f32 v148,v107,v148
v_mfma_f32_16x16x32_bf16 a[12:15],v[208:211],v[168:171],0
v_mul_f32 v149,v107,v149
v_mul_f32 v150,v107,v150
v_mfma_f32_16x16x32_bf16 a[12:15],v[212:215],a[184:187],a[12:15]
buffer_load_dwordx4 v236,s[32:35],s62 offen offset:1024 lds
v_mul_f32 v151,v107,v151
v_mfma_f32_16x16x32_bf16 a[44:47],v[208:211],v[172:175],0
v_mul_f32 v100,v107,v100
v_mul_f32 v101,v107,v101
v_mfma_f32_16x16x32_bf16 a[44:47],v[212:215],a[188:191],a[44:47]
s_waitcnt lgkmcnt(4)
v_mul_f32 v102,v107,v102
v_mul_f32 v103,v107,v103
v_mfma_f32_16x16x32_bf16 v[0:3],v[184:187],a[160:163],v[0:3]
buffer_load_dwordx4 v237,s[32:35],s62 offen offset:2048 lds
v_cvt_pk_bf16_f32 v148,v148,v149
v_mfma_f32_16x16x32_bf16 v[0:3],v[188:191],a[200:203],v[0:3]
v_cvt_pk_bf16_f32 v149,v150,v151
v_cvt_pk_bf16_f32 v150,v100,v101
v_mfma_f32_16x16x32_bf16 v[8:11],v[184:187],a[164:167],v[8:11]
v_cvt_pk_bf16_f32 v151,v102,v103
v_accvgpr_read_b32 v68,a16
v_mfma_f32_16x16x32_bf16 v[8:11],v[188:191],a[204:207],v[8:11]
s_add_u32 s66,s29,30720
buffer_store_dwordx4 v[148:151],v239,s[52:55],s66 offen offset:1024 sc1
v_accvgpr_read_b32 v69,a17
v_mfma_f32_16x16x32_bf16 v[16:19],v[184:187],a[168:171],v[16:19]
v_accvgpr_read_b32 v70,a18
v_accvgpr_read_b32 v71,a19
v_mfma_f32_16x16x32_bf16 v[16:19],v[188:191],a[208:211],v[16:19]
ds_read_b64_tr_b16 v[200:201],v230 offset:33024
ds_read_b64_tr_b16 v[202:203],v233 offset:256
v_accvgpr_read_b32 v100,a20
v_mfma_f32_16x16x32_bf16 v[24:27],v[184:187],a[172:175],v[24:27]
ds_read_b64_tr_b16 v[204:205],v233 offset:49408
ds_read_b64_tr_b16 v[206:207],v230 offset:16640
v_mfma_f32_16x16x32_bf16 v[24:27],v[188:191],a[212:215],v[24:27]
buffer_load_dwordx4 v238,s[32:35],s62 offen offset:3072 lds
v_accvgpr_read_b32 v101,a21
v_accvgpr_read_b32 v102,a22
v_accvgpr_read_b32 v103,a23
v_mul_f32 v68,v104,v68
v_mfma_f32_16x16x32_bf16 v[32:35],v[184:187],a[192:195],v[32:35]
s_add_u32 s62,s62,20480
s_mov_b32 m0,s63
v_mul_f32 v69,v104,v69
v_mfma_f32_16x16x32_bf16 v[32:35],v[188:191],a[216:219],v[32:35]
buffer_load_dwordx4 v235,s[32:35],s62 offen lds
v_mul_f32 v70,v104,v70
v_mul_f32 v71,v104,v71
v_mfma_f32_16x16x32_bf16 v[40:43],v[184:187],a[196:199],v[40:43]
v_mul_f32 v100,v104,v100
v_mul_f32 v101,v104,v101
v_mfma_f32_16x16x32_bf16 v[40:43],v[188:191],a[220:223],v[40:43]
s_waitcnt lgkmcnt(4)
v_mul_f32 v102,v104,v102
v_mul_f32 v103,v104,v103
v_mfma_f32_16x16x32_bf16 v[4:7],v[192:195],a[160:163],v[4:7]
buffer_load_dwordx4 v236,s[32:35],s62 offen offset:1024 lds
v_cvt_pk_bf16_f32 v68,v68,v69
v_mfma_f32_16x16x32_bf16 v[4:7],v[196:199],a[200:203],v[4:7]
v_cvt_pk_bf16_f32 v69,v70,v71
v_cvt_pk_bf16_f32 v70,v100,v101
v_mfma_f32_16x16x32_bf16 v[12:15],v[192:195],a[164:167],v[12:15]
v_cvt_pk_bf16_f32 v71,v102,v103
v_accvgpr_read_b32 v64,a48
v_mfma_f32_16x16x32_bf16 v[12:15],v[196:199],a[204:207],v[12:15]
s_add_u32 s66,s27,36864
buffer_store_dwordx4 v[68:71],v239,s[52:55],s66 offen sc1
v_accvgpr_read_b32 v65,a49
v_mfma_f32_16x16x32_bf16 v[20:23],v[192:195],a[168:171],v[20:23]
v_accvgpr_read_b32 v66,a50
v_accvgpr_read_b32 v67,a51
v_mfma_f32_16x16x32_bf16 v[20:23],v[196:199],a[208:211],v[20:23]
ds_read_b64_tr_b16 v[208:209],v231 offset:33024
ds_read_b64_tr_b16 v[210:211],v234 offset:256
v_accvgpr_read_b32 v100,a52
v_mfma_f32_16x16x32_bf16 v[28:31],v[192:195],a[172:175],v[28:31]
ds_read_b64_tr_b16 v[212:213],v234 offset:49408
ds_read_b64_tr_b16 v[214:215],v231 offset:16640
v_mfma_f32_16x16x32_bf16 v[28:31],v[196:199],a[212:215],v[28:31]
buffer_load_dwordx4 v237,s[32:35],s62 offen offset:2048 lds
v_accvgpr_read_b32 v101,a53
v_accvgpr_read_b32 v102,a54
v_accvgpr_read_b32 v103,a55
v_mul_f32 v64,v105,v64
v_mfma_f32_16x16x32_bf16 v[36:39],v[192:195],a[192:195],v[36:39]
v_mul_f32 v65,v105,v65
v_mul_f32 v66,v105,v66
v_mfma_f32_16x16x32_bf16 v[36:39],v[196:199],a[216:219],v[36:39]
buffer_load_dwordx4 v238,s[32:35],s62 offen offset:3072 lds
v_mul_f32 v67,v105,v67
v_mfma_f32_16x16x32_bf16 v[44:47],v[192:195],a[196:199],v[44:47]
s_add_u32 s62,s62,20480
v_mul_f32 v100,v105,v100
v_mul_f32 v101,v105,v101
v_mfma_f32_16x16x32_bf16 v[44:47],v[196:199],a[220:223],v[44:47]
s_waitcnt lgkmcnt(4)
v_mul_f32 v102,v105,v102
v_mul_f32 v103,v105,v103
v_mfma_f32_16x16x32_bf16 v[48:51],v[200:203],a[160:163],v[48:51]
buffer_load_dwordx4 v[152:155],v239,s[48:51],s61 offen sc1
v_cvt_pk_bf16_f32 v64,v64,v65
v_mfma_f32_16x16x32_bf16 v[48:51],v[204:207],a[200:203],v[48:51]
v_cvt_pk_bf16_f32 v65,v66,v67
v_cvt_pk_bf16_f32 v66,v100,v101
v_mfma_f32_16x16x32_bf16 v[56:59],v[200:203],a[164:167],v[56:59]
v_cvt_pk_bf16_f32 v67,v102,v103
v_accvgpr_read_b32 v148,a80
v_mfma_f32_16x16x32_bf16 v[56:59],v[204:207],a[204:207],v[56:59]
s_add_u32 s66,s27,36864
buffer_store_dwordx4 v[64:67],v239,s[52:55],s66 offen offset:1024 sc1
v_accvgpr_read_b32 v149,a81
v_mfma_f32_16x16x32_bf16 v[128:131],v[200:203],a[168:171],v[128:131]
v_accvgpr_read_b32 v150,a82
v_accvgpr_read_b32 v151,a83
v_mfma_f32_16x16x32_bf16 v[128:131],v[204:207],a[208:211],v[128:131]
ds_read_b64_tr_b16 v[184:185],v230 offset:33280
ds_read_b64_tr_b16 v[186:187],v233 offset:512
v_accvgpr_read_b32 v100,a84
v_mfma_f32_16x16x32_bf16 v[136:139],v[200:203],a[172:175],v[136:139]
ds_read_b64_tr_b16 v[188:189],v233 offset:49664
ds_read_b64_tr_b16 v[190:191],v230 offset:16896
v_mfma_f32_16x16x32_bf16 v[136:139],v[204:207],a[212:215],v[136:139]
buffer_load_dwordx4 v[156:159],v239,s[48:51],s61 offen offset:1024 sc1
v_accvgpr_read_b32 v101,a85
v_accvgpr_read_b32 v102,a86
v_accvgpr_read_b32 v103,a87
v_mul_f32 v148,v106,v148
v_mfma_f32_16x16x32_bf16 v[96:99],v[200:203],a[192:195],v[96:99]
v_mul_f32 v149,v106,v149
v_mul_f32 v150,v106,v150
v_mfma_f32_16x16x32_bf16 v[96:99],v[204:207],a[216:219],v[96:99]
buffer_load_dwordx4 v[160:163],v239,s[48:51],s61 offen offset:2048 sc1
v_mul_f32 v151,v106,v151
v_mfma_f32_16x16x32_bf16 v[216:219],v[200:203],a[196:199],v[216:219]
v_mul_f32 v100,v106,v100
v_mul_f32 v101,v106,v101
v_mfma_f32_16x16x32_bf16 v[216:219],v[204:207],a[220:223],v[216:219]
s_waitcnt lgkmcnt(4)
v_mul_f32 v102,v106,v102
v_mul_f32 v103,v106,v103
v_mfma_f32_16x16x32_bf16 v[52:55],v[208:211],a[160:163],v[52:55]
s_add_u32 m0,s63,81920
buffer_load_dwordx4 v[164:167],v239,s[48:51],s61 offen offset:3072 sc1
v_cvt_pk_bf16_f32 v148,v148,v149
v_mfma_f32_16x16x32_bf16 v[52:55],v[212:215],a[200:203],v[52:55]
v_cvt_pk_bf16_f32 v149,v150,v151
v_cvt_pk_bf16_f32 v150,v100,v101
v_mfma_f32_16x16x32_bf16 v[60:63],v[208:211],a[164:167],v[60:63]
v_cvt_pk_bf16_f32 v151,v102,v103
v_accvgpr_read_b32 v68,a112
v_mfma_f32_16x16x32_bf16 v[60:63],v[212:215],a[204:207],v[60:63]
s_add_u32 s66,s29,36864
buffer_store_dwordx4 v[148:151],v239,s[52:55],s66 offen sc1
v_accvgpr_read_b32 v69,a113
v_mfma_f32_16x16x32_bf16 v[132:135],v[208:211],a[168:171],v[132:135]
v_accvgpr_read_b32 v70,a114
v_accvgpr_read_b32 v71,a115
v_mfma_f32_16x16x32_bf16 v[132:135],v[212:215],a[208:211],v[132:135]
ds_read_b64_tr_b16 v[192:193],v231 offset:33280
ds_read_b64_tr_b16 v[194:195],v234 offset:512
v_accvgpr_read_b32 v100,a116
v_mfma_f32_16x16x32_bf16 v[140:143],v[208:211],a[172:175],v[140:143]
ds_read_b64_tr_b16 v[196:197],v234 offset:49664
ds_read_b64_tr_b16 v[198:199],v231 offset:16896
v_mfma_f32_16x16x32_bf16 v[140:143],v[212:215],a[212:215],v[140:143]
buffer_load_dwordx4 v[168:171],v252,s[48:51],s61 offen sc1
v_accvgpr_read_b32 v101,a117
v_accvgpr_read_b32 v102,a118
v_accvgpr_read_b32 v103,a119
v_mul_f32 v68,v107,v68
v_mfma_f32_16x16x32_bf16 v[144:147],v[208:211],a[192:195],v[144:147]
v_mul_f32 v69,v107,v69
v_mul_f32 v70,v107,v70
v_mfma_f32_16x16x32_bf16 v[144:147],v[212:215],a[216:219],v[144:147]
buffer_load_dwordx4 v[172:175],v252,s[48:51],s61 offen offset:1024 sc1
v_mul_f32 v71,v107,v71
v_mfma_f32_16x16x32_bf16 v[220:223],v[208:211],a[196:199],v[220:223]
s_add_u32 s61,s61,6144
v_mul_f32 v100,v107,v100
v_mul_f32 v101,v107,v101
v_mfma_f32_16x16x32_bf16 v[220:223],v[212:215],a[220:223],v[220:223]
s_waitcnt lgkmcnt(4)
v_mul_f32 v102,v107,v102
v_mul_f32 v103,v107,v103
v_mfma_f32_16x16x32_bf16 a[128:131],v[184:187],a[160:163],a[128:131]
buffer_load_dwordx4 v235,s[32:35],s62 offen lds
v_cvt_pk_bf16_f32 v68,v68,v69
v_mfma_f32_16x16x32_bf16 a[128:131],v[188:191],a[200:203],a[128:131]
v_cvt_pk_bf16_f32 v69,v70,v71
v_cvt_pk_bf16_f32 v70,v100,v101
v_mfma_f32_16x16x32_bf16 a[136:139],v[184:187],a[164:167],a[136:139]
v_cvt_pk_bf16_f32 v71,v102,v103
v_accvgpr_read_b32 v64,a24
v_mfma_f32_16x16x32_bf16 a[136:139],v[188:191],a[204:207],a[136:139]
s_add_u32 s66,s29,36864
buffer_store_dwordx4 v[68:71],v239,s[52:55],s66 offen offset:1024 sc1
v_accvgpr_read_b32 v65,a25
v_mfma_f32_16x16x32_bf16 a[144:147],v[184:187],a[168:171],a[144:147]
v_accvgpr_read_b32 v66,a26
v_accvgpr_read_b32 v67,a27
v_mfma_f32_16x16x32_bf16 a[144:147],v[188:191],a[208:211],a[144:147]
ds_read_b64_tr_b16 v[200:201],v230 offset:33536
ds_read_b64_tr_b16 v[202:203],v233 offset:768
v_accvgpr_read_b32 v100,a28
v_mfma_f32_16x16x32_bf16 a[152:155],v[184:187],a[172:175],a[152:155]
ds_read_b64_tr_b16 v[204:205],v233 offset:49920
ds_read_b64_tr_b16 v[206:207],v230 offset:17152
v_mfma_f32_16x16x32_bf16 a[152:155],v[188:191],a[212:215],a[152:155]
buffer_load_dwordx4 v236,s[32:35],s62 offen offset:1024 lds
v_accvgpr_read_b32 v101,a29
v_accvgpr_read_b32 v102,a30
v_accvgpr_read_b32 v103,a31
)A"
R"A(v_mul_f32 v64,v104,v64
v_mfma_f32_16x16x32_bf16 a[248:251],v[184:187],a[192:195],a[248:251]
v_mul_f32 v65,v104,v65
v_mul_f32 v66,v104,v66
v_mfma_f32_16x16x32_bf16 a[248:251],v[188:191],a[216:219],a[248:251]
buffer_load_dwordx4 v237,s[32:35],s62 offen offset:2048 lds
v_mul_f32 v67,v104,v67
v_mfma_f32_16x16x32_bf16 v[72:75],v[184:187],a[196:199],v[72:75]
v_mul_f32 v100,v104,v100
v_mul_f32 v101,v104,v101
v_mfma_f32_16x16x32_bf16 v[72:75],v[188:191],a[220:223],v[72:75]
s_waitcnt lgkmcnt(4)
v_mul_f32 v102,v104,v102
v_mul_f32 v103,v104,v103
v_mfma_f32_16x16x32_bf16 a[132:135],v[192:195],a[160:163],a[132:135]
buffer_load_dwordx4 v238,s[32:35],s62 offen offset:3072 lds
v_cvt_pk_bf16_f32 v64,v64,v65
v_mfma_f32_16x16x32_bf16 a[132:135],v[196:199],a[200:203],a[132:135]
s_add_u32 s62,s62,20480
v_cvt_pk_bf16_f32 v65,v66,v67
v_cvt_pk_bf16_f32 v66,v100,v101
v_mfma_f32_16x16x32_bf16 a[140:143],v[192:195],a[164:167],a[140:143]
v_cvt_pk_bf16_f32 v67,v102,v103
v_accvgpr_read_b32 v148,a56
v_mfma_f32_16x16x32_bf16 a[140:143],v[196:199],a[204:207],a[140:143]
s_add_u32 s66,s27,43008
buffer_store_dwordx4 v[64:67],v239,s[52:55],s66 offen sc1
v_accvgpr_read_b32 v149,a57
v_mfma_f32_16x16x32_bf16 a[148:151],v[192:195],a[168:171],a[148:151]
v_accvgpr_read_b32 v150,a58
v_accvgpr_read_b32 v151,a59
v_mfma_f32_16x16x32_bf16 a[148:151],v[196:199],a[208:211],a[148:151]
ds_read_b64_tr_b16 v[208:209],v231 offset:33536
ds_read_b64_tr_b16 v[210:211],v234 offset:768
v_accvgpr_read_b32 v100,a60
v_mfma_f32_16x16x32_bf16 a[156:159],v[192:195],a[172:175],a[156:159]
ds_read_b64_tr_b16 v[212:213],v234 offset:49920
ds_read_b64_tr_b16 v[214:215],v231 offset:17152
v_mfma_f32_16x16x32_bf16 a[156:159],v[196:199],a[212:215],a[156:159]
v_accvgpr_read_b32 v101,a61
v_accvgpr_read_b32 v102,a62
v_accvgpr_read_b32 v103,a63
v_mul_f32 v148,v105,v148
v_mfma_f32_16x16x32_bf16 a[252:255],v[192:195],a[192:195],a[252:255]
v_mul_f32 v149,v105,v149
v_mul_f32 v150,v105,v150
v_mfma_f32_16x16x32_bf16 a[252:255],v[196:199],a[216:219],a[252:255]
s_waitcnt lgkmcnt(2)
v_mul_f32 v151,v105,v151
v_mul_f32 v100,v105,v100
v_mfma_f32_16x16x32_bf16 v[76:79],v[192:195],a[196:199],v[76:79]
s_waitcnt lgkmcnt(0)
v_mul_f32 v101,v105,v101
v_mul_f32 v102,v105,v102
v_mfma_f32_16x16x32_bf16 v[76:79],v[196:199],a[220:223],v[76:79]
s_waitcnt vmcnt(24)
v_mul_f32 v103,v105,v103
v_mfma_f32_16x16x32_bf16 a[0:3],v[200:203],a[160:163],a[0:3]
s_waitcnt vmcnt(14)
v_cvt_pk_bf16_f32 v148,v148,v149
v_cvt_pk_bf16_f32 v149,v150,v151
v_mfma_f32_16x16x32_bf16 a[0:3],v[204:207],a[200:203],a[0:3]
s_add_u32 m0,s63,32768
v_cvt_pk_bf16_f32 v150,v100,v101
v_cvt_pk_bf16_f32 v151,v102,v103
v_mfma_f32_16x16x32_bf16 a[32:35],v[200:203],a[164:167],a[32:35]
s_add_u32 s66,s27,43008
buffer_store_dwordx4 v[148:151],v239,s[52:55],s66 offen offset:1024 sc1
v_mfma_f32_16x16x32_bf16 a[32:35],v[204:207],a[204:207],a[32:35]
buffer_load_dwordx4 v235,s[32:35],s62 offen lds
v_accvgpr_read_b32 v68,a88
v_accvgpr_read_b32 v69,a89
v_mfma_f32_16x16x32_bf16 a[64:67],v[200:203],a[168:171],a[64:67]
v_accvgpr_read_b32 v70,a90
v_accvgpr_read_b32 v71,a91
v_mfma_f32_16x16x32_bf16 a[64:67],v[204:207],a[208:211],a[64:67]
ds_read_b64_tr_b16 v[184:185],v233 offset:32768
ds_read_b64_tr_b16 v[186:187],v230 offset:49152
v_accvgpr_read_b32 v100,a92
v_mfma_f32_16x16x32_bf16 a[96:99],v[200:203],a[172:175],a[96:99]
ds_read_b64_tr_b16 v[188:189],v226 offset:16384
ds_read_b64_tr_b16 v[190:191],v230
v_mfma_f32_16x16x32_bf16 a[96:99],v[204:207],a[212:215],a[96:99]
buffer_load_dwordx4 v236,s[32:35],s62 offen offset:1024 lds
v_accvgpr_read_b32 v101,a93
v_accvgpr_read_b32 v102,a94
v_accvgpr_read_b32 v103,a95
v_mul_f32 v68,v106,v68
v_mfma_f32_16x16x32_bf16 a[8:11],v[200:203],a[192:195],a[8:11]
v_mul_f32 v69,v106,v69
v_mul_f32 v70,v106,v70
v_mfma_f32_16x16x32_bf16 a[8:11],v[204:207],a[216:219],a[8:11]
buffer_load_dwordx4 v237,s[32:35],s62 offen offset:2048 lds
v_mul_f32 v71,v106,v71
v_mfma_f32_16x16x32_bf16 a[40:43],v[200:203],a[196:199],a[40:43]
v_mul_f32 v100,v106,v100
v_mul_f32 v101,v106,v101
v_mfma_f32_16x16x32_bf16 a[40:43],v[204:207],a[220:223],a[40:43]
v_mul_f32 v102,v106,v102
v_mul_f32 v103,v106,v103
v_mfma_f32_16x16x32_bf16 a[4:7],v[208:211],a[160:163],a[4:7]
buffer_load_dwordx4 v238,s[32:35],s62 offen offset:3072 lds
v_cvt_pk_bf16_f32 v68,v68,v69
v_mfma_f32_16x16x32_bf16 a[4:7],v[212:215],a[200:203],a[4:7]
s_add_u32 s62,s62,20480
v_cvt_pk_bf16_f32 v69,v70,v71
v_cvt_pk_bf16_f32 v70,v100,v101
v_mfma_f32_16x16x32_bf16 a[36:39],v[208:211],a[164:167],a[36:39]
s_add_u32 m0,s63,65536
v_cvt_pk_bf16_f32 v71,v102,v103
v_accvgpr_read_b32 v64,a120
v_mfma_f32_16x16x32_bf16 a[36:39],v[212:215],a[204:207],a[36:39]
s_add_u32 s66,s29,43008
buffer_store_dwordx4 v[68:71],v239,s[52:55],s66 offen sc1
v_accvgpr_read_b32 v65,a121
v_mfma_f32_16x16x32_bf16 a[68:71],v[208:211],a[168:171],a[68:71]
v_accvgpr_read_b32 v66,a122
v_accvgpr_read_b32 v67,a123
v_mfma_f32_16x16x32_bf16 a[68:71],v[212:215],a[208:211],a[68:71]
ds_read_b64_tr_b16 v[192:193],v234 offset:32768
ds_read_b64_tr_b16 v[194:195],v231 offset:49152
v_accvgpr_read_b32 v100,a124
v_mfma_f32_16x16x32_bf16 a[100:103],v[208:211],a[172:175],a[100:103]
ds_read_b64_tr_b16 v[196:197],v232 offset:16384
ds_read_b64_tr_b16 v[198:199],v231
v_mfma_f32_16x16x32_bf16 a[100:103],v[212:215],a[212:215],a[100:103]
buffer_load_dwordx4 v235,s[32:35],s62 offen lds
v_accvgpr_read_b32 v101,a125
v_accvgpr_read_b32 v102,a126
v_accvgpr_read_b32 v103,a127
v_mul_f32 v64,v107,v64
v_mfma_f32_16x16x32_bf16 a[12:15],v[208:211],a[192:195],a[12:15]
v_mul_f32 v65,v107,v65
v_mul_f32 v66,v107,v66
v_mfma_f32_16x16x32_bf16 a[12:15],v[212:215],a[216:219],a[12:15]
buffer_load_dwordx4 v236,s[32:35],s62 offen offset:1024 lds
v_mul_f32 v67,v107,v67
v_mfma_f32_16x16x32_bf16 a[44:47],v[208:211],a[196:199],a[44:47]
v_mul_f32 v100,v107,v100
v_mul_f32 v101,v107,v101
v_mfma_f32_16x16x32_bf16 a[44:47],v[212:215],a[220:223],a[44:47]
s_waitcnt lgkmcnt(4)
v_mul_f32 v102,v107,v102
v_mul_f32 v103,v107,v103
v_mfma_f32_16x16x32_bf16 v[0:3],v[184:187],a[224:227],v[0:3]
buffer_load_dwordx4 v237,s[32:35],s62 offen offset:2048 lds
v_cvt_pk_bf16_f32 v64,v64,v65
s_waitcnt vmcnt(22)
v_mfma_f32_16x16x32_bf16 v[0:3],v[188:191],v[152:155],v[0:3]
v_cvt_pk_bf16_f32 v65,v66,v67
v_cvt_pk_bf16_f32 v66,v100,v101
v_mfma_f32_16x16x32_bf16 v[8:11],v[184:187],a[228:231],v[8:11]
ds_read_b64_tr_b16 v[200:201],v233 offset:33024
v_cvt_pk_bf16_f32 v67,v102,v103
s_waitcnt vmcnt(20)
v_mfma_f32_16x16x32_bf16 v[8:11],v[188:191],v[156:159],v[8:11]
s_add_u32 s66,s29,43008
buffer_store_dwordx4 v[64:67],v239,s[52:55],s66 offen offset:1024 sc1
v_mfma_f32_16x16x32_bf16 v[16:19],v[184:187],a[232:235],v[16:19]
ds_read_b64_tr_b16 v[202:203],v230 offset:49408
ds_read_b64_tr_b16 v[204:205],v226 offset:16640
s_waitcnt vmcnt(20)
v_mfma_f32_16x16x32_bf16 v[16:19],v[188:191],v[160:163],v[16:19]
ds_read_b64_tr_b16 v[206:207],v230 offset:256
buffer_load_dwordx4 v238,s[32:35],s62 offen offset:3072 lds
v_mfma_f32_16x16x32_bf16 v[24:27],v[184:187],a[236:239],v[24:27]
s_add_u32 s62,s62,20480
s_add_u32 m0,s63,114688
s_waitcnt vmcnt(20)
v_mfma_f32_16x16x32_bf16 v[24:27],v[188:191],v[164:167],v[24:27]
buffer_load_dwordx4 v235,s[32:35],s62 offen lds
buffer_load_dwordx4 v236,s[32:35],s62 offen offset:1024 lds
v_mfma_f32_16x16x32_bf16 v[32:35],v[184:187],a[240:243],v[32:35]
buffer_load_dwordx4 v237,s[32:35],s62 offen offset:2048 lds
buffer_load_dwordx4 v238,s[32:35],s62 offen offset:3072 lds
s_waitcnt vmcnt(22)
v_mfma_f32_16x16x32_bf16 v[32:35],v[188:191],v[168:171],v[32:35]
s_add_u32 s62,s62,184320
v_mfma_f32_16x16x32_bf16 v[40:43],v[184:187],a[244:247],v[40:43]
s_waitcnt vmcnt(21)
v_mfma_f32_16x16x32_bf16 v[40:43],v[188:191],v[172:175],v[40:43]
s_waitcnt lgkmcnt(4)
buffer_load_dwordx4 v[176:179],v239,s[48:51],s61 offen sc1
v_mfma_f32_16x16x32_bf16 v[4:7],v[192:195],a[224:227],v[4:7]
ds_read_b64_tr_b16 v[208:209],v234 offset:33024
ds_read_b64_tr_b16 v[210:211],v231 offset:49408
v_mfma_f32_16x16x32_bf16 v[4:7],v[196:199],v[152:155],v[4:7]
ds_read_b64_tr_b16 v[212:213],v232 offset:16640
ds_read_b64_tr_b16 v[214:215],v231 offset:256
v_mfma_f32_16x16x32_bf16 v[12:15],v[192:195],a[228:231],v[12:15]
buffer_load_dwordx4 v[180:183],v239,s[48:51],s61 offen offset:1024 sc1
v_mfma_f32_16x16x32_bf16 v[12:15],v[196:199],v[156:159],v[12:15]
v_mfma_f32_16x16x32_bf16 v[20:23],v[192:195],a[232:235],v[20:23]
v_mfma_f32_16x16x32_bf16 v[20:23],v[196:199],v[160:163],v[20:23]
buffer_load_dwordx4 a[176:179],v239,s[48:51],s61 offen offset:2048 sc1
v_mfma_f32_16x16x32_bf16 v[28:31],v[192:195],a[236:239],v[28:31]
v_mfma_f32_16x16x32_bf16 v[28:31],v[196:199],v[164:167],v[28:31]
v_mfma_f32_16x16x32_bf16 v[36:39],v[192:195],a[240:243],v[36:39]
buffer_load_dwordx4 a[180:183],v239,s[48:51],s61 offen offset:3072 sc1
v_mfma_f32_16x16x32_bf16 v[36:39],v[196:199],v[168:171],v[36:39]
v_mfma_f32_16x16x32_bf16 v[44:47],v[192:195],a[244:247],v[44:47]
v_mfma_f32_16x16x32_bf16 v[44:47],v[196:199],v[172:175],v[44:47]
s_waitcnt lgkmcnt(4)
buffer_load_dwordx4 a[184:187],v252,s[48:51],s61 offen sc1
v_mfma_f32_16x16x32_bf16 v[48:51],v[200:203],a[224:227],v[48:51]
ds_read_b64_tr_b16 v[184:185],v233 offset:33280
ds_read_b64_tr_b16 v[186:187],v230 offset:49664
v_mfma_f32_16x16x32_bf16 v[48:51],v[204:207],v[152:155],v[48:51]
ds_read_b64_tr_b16 v[188:189],v226 offset:16896
ds_read_b64_tr_b16 v[190:191],v230 offset:512
v_mfma_f32_16x16x32_bf16 v[56:59],v[200:203],a[228:231],v[56:59]
buffer_load_dwordx4 a[188:191],v252,s[48:51],s61 offen offset:1024 sc1
v_mfma_f32_16x16x32_bf16 v[56:59],v[204:207],v[156:159],v[56:59]
s_add_u32 s61,s61,6144
v_mfma_f32_16x16x32_bf16 v[128:131],v[200:203],a[232:235],v[128:131]
v_mfma_f32_16x16x32_bf16 v[128:131],v[204:207],v[160:163],v[128:131]
buffer_load_dwordx4 a[160:163],v239,s[48:51],s61 offen sc1
v_mfma_f32_16x16x32_bf16 v[136:139],v[200:203],a[236:239],v[136:139]
v_mfma_f32_16x16x32_bf16 v[136:139],v[204:207],v[164:167],v[136:139]
v_mfma_f32_16x16x32_bf16 v[96:99],v[200:203],a[240:243],v[96:99]
buffer_load_dwordx4 a[164:167],v239,s[48:51],s61 offen offset:1024 sc1
v_mfma_f32_16x16x32_bf16 v[96:99],v[204:207],v[168:171],v[96:99]
v_mfma_f32_16x16x32_bf16 v[216:219],v[200:203],a[244:247],v[216:219]
v_mfma_f32_16x16x32_bf16 v[216:219],v[204:207],v[172:175],v[216:219]
s_waitcnt lgkmcnt(4)
buffer_load_dwordx4 a[168:171],v239,s[48:51],s61 offen offset:2048 sc1
v_mfma_f32_16x16x32_bf16 v[52:55],v[208:211],a[224:227],v[52:55]
ds_read_b64_tr_b16 v[192:193],v234 offset:33280
ds_read_b64_tr_b16 v[194:195],v231 offset:49664
v_mfma_f32_16x16x32_bf16 v[52:55],v[212:215],v[152:155],v[52:55]
ds_read_b64_tr_b16 v[196:197],v232 offset:16896
ds_read_b64_tr_b16 v[198:199],v231 offset:512
v_mfma_f32_16x16x32_bf16 v[60:63],v[208:211],a[228:231],v[60:63]
buffer_load_dwordx4 a[172:175],v239,s[48:51],s61 offen offset:3072 sc1
v_mfma_f32_16x16x32_bf16 v[60:63],v[212:215],v[156:159],v[60:63]
v_mfma_f32_16x16x32_bf16 v[132:135],v[208:211],a[232:235],v[132:135]
v_mfma_f32_16x16x32_bf16 v[132:135],v[212:215],v[160:163],v[132:135]
buffer_load_dwordx4 a[192:195],v252,s[48:51],s61 offen sc1
v_mfma_f32_16x16x32_bf16 v[140:143],v[208:211],a[236:239],v[140:143]
v_mfma_f32_16x16x32_bf16 v[140:143],v[212:215],v[164:167],v[140:143]
v_mfma_f32_16x16x32_bf16 v[144:147],v[208:211],a[240:243],v[144:147]
buffer_load_dwordx4 a[196:199],v252,s[48:51],s61 offen offset:1024 sc1
v_mfma_f32_16x16x32_bf16 v[144:147],v[212:215],v[168:171],v[144:147]
s_add_u32 s61,s61,30720
s_add_u32 m0,s63,16384
v_mfma_f32_16x16x32_bf16 v[220:223],v[208:211],a[244:247],v[220:223]
v_mfma_f32_16x16x32_bf16 v[220:223],v[212:215],v[172:175],v[220:223]
s_waitcnt lgkmcnt(4)
buffer_load_dwordx4 v235,s[32:35],s62 offen lds
v_mfma_f32_16x16x32_bf16 a[128:131],v[184:187],a[224:227],a[128:131]
ds_read_b64_tr_b16 v[200:201],v233 offset:33536
ds_read_b64_tr_b16 v[202:203],v230 offset:49920
v_mfma_f32_16x16x32_bf16 a[128:131],v[188:191],v[152:155],a[128:131]
ds_read_b64_tr_b16 v[204:205],v226 offset:17152
ds_read_b64_tr_b16 v[206:207],v230 offset:768
v_mfma_f32_16x16x32_bf16 a[136:139],v[184:187],a[228:231],a[136:139]
)A"
R"A(buffer_load_dwordx4 v236,s[32:35],s62 offen offset:1024 lds
v_mfma_f32_16x16x32_bf16 a[136:139],v[188:191],v[156:159],a[136:139]
v_mfma_f32_16x16x32_bf16 a[144:147],v[184:187],a[232:235],a[144:147]
v_mfma_f32_16x16x32_bf16 a[144:147],v[188:191],v[160:163],a[144:147]
buffer_load_dwordx4 v237,s[32:35],s62 offen offset:2048 lds
v_mfma_f32_16x16x32_bf16 a[152:155],v[184:187],a[236:239],a[152:155]
v_mfma_f32_16x16x32_bf16 a[152:155],v[188:191],v[164:167],a[152:155]
v_mfma_f32_16x16x32_bf16 a[248:251],v[184:187],a[240:243],a[248:251]
buffer_load_dwordx4 v238,s[32:35],s62 offen offset:3072 lds
v_mfma_f32_16x16x32_bf16 a[248:251],v[188:191],v[168:171],a[248:251]
s_add_u32 s62,s62,20480
v_mfma_f32_16x16x32_bf16 v[72:75],v[184:187],a[244:247],v[72:75]
v_mfma_f32_16x16x32_bf16 v[72:75],v[188:191],v[172:175],v[72:75]
s_waitcnt lgkmcnt(4)
v_mfma_f32_16x16x32_bf16 a[132:135],v[192:195],a[224:227],a[132:135]
ds_read_b64_tr_b16 v[208:209],v234 offset:33536
ds_read_b64_tr_b16 v[210:211],v231 offset:49920
v_mfma_f32_16x16x32_bf16 a[132:135],v[196:199],v[152:155],a[132:135]
ds_read_b64_tr_b16 v[212:213],v232 offset:17152
ds_read_b64_tr_b16 v[214:215],v231 offset:768
v_mfma_f32_16x16x32_bf16 a[140:143],v[192:195],a[228:231],a[140:143]
v_mfma_f32_16x16x32_bf16 a[140:143],v[196:199],v[156:159],a[140:143]
v_mfma_f32_16x16x32_bf16 a[148:151],v[192:195],a[232:235],a[148:151]
v_mfma_f32_16x16x32_bf16 a[148:151],v[196:199],v[160:163],a[148:151]
v_mfma_f32_16x16x32_bf16 a[156:159],v[192:195],a[236:239],a[156:159]
v_mfma_f32_16x16x32_bf16 a[156:159],v[196:199],v[164:167],a[156:159]
v_mfma_f32_16x16x32_bf16 a[252:255],v[192:195],a[240:243],a[252:255]
v_mfma_f32_16x16x32_bf16 a[252:255],v[196:199],v[168:171],a[252:255]
s_waitcnt lgkmcnt(2)
v_mfma_f32_16x16x32_bf16 v[76:79],v[192:195],a[244:247],v[76:79]
s_waitcnt lgkmcnt(0)
s_add_u32 m0,s63,98304
v_mfma_f32_16x16x32_bf16 v[76:79],v[196:199],v[172:175],v[76:79]
s_waitcnt vmcnt(26)
buffer_load_dwordx4 v235,s[32:35],s62 offen lds
v_mfma_f32_16x16x32_bf16 a[0:3],v[200:203],a[224:227],a[0:3]
s_waitcnt vmcnt(17)
ds_read_b64_tr_b16 v[184:185],v233 offset:16384
ds_read_b64_tr_b16 v[186:187],v230 offset:32768
v_mfma_f32_16x16x32_bf16 a[0:3],v[204:207],v[152:155],a[0:3]
ds_read_b64_tr_b16 v[188:189],v233
ds_read_b64_tr_b16 v[190:191],v233 offset:49152
v_mfma_f32_16x16x32_bf16 a[32:35],v[200:203],a[228:231],a[32:35]
buffer_load_dwordx4 v236,s[32:35],s62 offen offset:1024 lds
buffer_load_dwordx4 v237,s[32:35],s62 offen offset:2048 lds
v_mfma_f32_16x16x32_bf16 a[32:35],v[204:207],v[156:159],a[32:35]
buffer_load_dwordx4 v238,s[32:35],s62 offen offset:3072 lds
v_mfma_f32_16x16x32_bf16 a[64:67],v[200:203],a[232:235],a[64:67]
s_add_u32 s62,s62,20480
s_add_u32 m0,s63,49152
v_mfma_f32_16x16x32_bf16 a[64:67],v[204:207],v[160:163],a[64:67]
buffer_load_dwordx4 v235,s[32:35],s62 offen lds
v_mfma_f32_16x16x32_bf16 a[96:99],v[200:203],a[236:239],a[96:99]
v_mfma_f32_16x16x32_bf16 a[96:99],v[204:207],v[164:167],a[96:99]
v_mfma_f32_16x16x32_bf16 a[8:11],v[200:203],a[240:243],a[8:11]
buffer_load_dwordx4 v236,s[32:35],s62 offen offset:1024 lds
v_mfma_f32_16x16x32_bf16 a[8:11],v[204:207],v[168:171],a[8:11]
v_mfma_f32_16x16x32_bf16 a[40:43],v[200:203],a[244:247],a[40:43]
v_mfma_f32_16x16x32_bf16 a[40:43],v[204:207],v[172:175],a[40:43]
buffer_load_dwordx4 v237,s[32:35],s62 offen offset:2048 lds
v_mfma_f32_16x16x32_bf16 a[4:7],v[208:211],a[224:227],a[4:7]
ds_read_b64_tr_b16 v[192:193],v234 offset:16384
ds_read_b64_tr_b16 v[194:195],v231 offset:32768
v_mfma_f32_16x16x32_bf16 a[4:7],v[212:215],v[152:155],a[4:7]
ds_read_b64_tr_b16 v[196:197],v234
ds_read_b64_tr_b16 v[198:199],v234 offset:49152
v_mfma_f32_16x16x32_bf16 a[36:39],v[208:211],a[228:231],a[36:39]
buffer_load_dwordx4 v238,s[32:35],s62 offen offset:3072 lds
v_mfma_f32_16x16x32_bf16 a[36:39],v[212:215],v[156:159],a[36:39]
s_add_u32 s62,s62,20480
s_add_u32 m0,s63,147456
v_mfma_f32_16x16x32_bf16 a[68:71],v[208:211],a[232:235],a[68:71]
v_mfma_f32_16x16x32_bf16 a[68:71],v[212:215],v[160:163],a[68:71]
buffer_load_dwordx4 v235,s[32:35],s62 offen lds
v_mfma_f32_16x16x32_bf16 a[100:103],v[208:211],a[236:239],a[100:103]
v_mfma_f32_16x16x32_bf16 a[100:103],v[212:215],v[164:167],a[100:103]
v_mfma_f32_16x16x32_bf16 a[12:15],v[208:211],a[240:243],a[12:15]
buffer_load_dwordx4 v236,s[32:35],s62 offen offset:1024 lds
v_mfma_f32_16x16x32_bf16 a[12:15],v[212:215],v[168:171],a[12:15]
v_mfma_f32_16x16x32_bf16 a[44:47],v[208:211],a[244:247],a[44:47]
v_mfma_f32_16x16x32_bf16 a[44:47],v[212:215],v[172:175],a[44:47]
s_waitcnt lgkmcnt(4)
buffer_load_dwordx4 v237,s[32:35],s62 offen offset:2048 lds
s_waitcnt vmcnt(26)
v_mfma_f32_16x16x32_bf16 v[0:3],v[184:187],v[176:179],v[0:3]
ds_read_b64_tr_b16 v[200:201],v233 offset:16640
ds_read_b64_tr_b16 v[202:203],v230 offset:33024
s_waitcnt vmcnt(20)
v_mfma_f32_16x16x32_bf16 v[0:3],v[188:191],a[160:163],v[0:3]
ds_read_b64_tr_b16 v[204:205],v233 offset:256
ds_read_b64_tr_b16 v[206:207],v233 offset:49408
v_mfma_f32_16x16x32_bf16 v[8:11],v[184:187],v[180:183],v[8:11]
buffer_load_dwordx4 v238,s[32:35],s62 offen offset:3072 lds
s_waitcnt vmcnt(20)
v_mfma_f32_16x16x32_bf16 v[8:11],v[188:191],a[164:167],v[8:11]
s_add_u32 s62,s62,20480
v_mfma_f32_16x16x32_bf16 v[16:19],v[184:187],a[176:179],v[16:19]
s_waitcnt vmcnt(19)
v_mfma_f32_16x16x32_bf16 v[16:19],v[188:191],a[168:171],v[16:19]
buffer_load_dwordx4 a[200:203],v239,s[48:51],s61 offen sc1
v_mfma_f32_16x16x32_bf16 v[24:27],v[184:187],a[180:183],v[24:27]
s_waitcnt vmcnt(19)
v_mfma_f32_16x16x32_bf16 v[24:27],v[188:191],a[172:175],v[24:27]
v_mov_b32 v228,1
ds_add_rtn_u32 v249,v229,v228
v_mfma_f32_16x16x32_bf16 v[32:35],v[184:187],a[184:187],v[32:35]
buffer_load_dwordx4 a[204:207],v239,s[48:51],s61 offen offset:1024 sc1
s_waitcnt vmcnt(19)
v_mfma_f32_16x16x32_bf16 v[32:35],v[188:191],a[192:195],v[32:35]
v_mfma_f32_16x16x32_bf16 v[40:43],v[184:187],a[188:191],v[40:43]
s_waitcnt vmcnt(18)
v_mfma_f32_16x16x32_bf16 v[40:43],v[188:191],a[196:199],v[40:43]
s_waitcnt lgkmcnt(5)
buffer_load_dwordx4 a[208:211],v239,s[48:51],s61 offen offset:2048 sc1
v_mfma_f32_16x16x32_bf16 v[4:7],v[192:195],v[176:179],v[4:7]
ds_read_b64_tr_b16 v[208:209],v234 offset:16640
ds_read_b64_tr_b16 v[210:211],v231 offset:33024
v_mfma_f32_16x16x32_bf16 v[4:7],v[196:199],a[160:163],v[4:7]
ds_read_b64_tr_b16 v[212:213],v234 offset:256
ds_read_b64_tr_b16 v[214:215],v234 offset:49408
v_mfma_f32_16x16x32_bf16 v[12:15],v[192:195],v[180:183],v[12:15]
buffer_load_dwordx4 a[212:215],v239,s[48:51],s61 offen offset:3072 sc1
v_mfma_f32_16x16x32_bf16 v[12:15],v[196:199],a[164:167],v[12:15]
v_mfma_f32_16x16x32_bf16 v[20:23],v[192:195],a[176:179],v[20:23]
v_mfma_f32_16x16x32_bf16 v[20:23],v[196:199],a[168:171],v[20:23]
buffer_load_dwordx4 a[216:219],v252,s[48:51],s61 offen sc1
v_mfma_f32_16x16x32_bf16 v[28:31],v[192:195],a[180:183],v[28:31]
v_mfma_f32_16x16x32_bf16 v[28:31],v[196:199],a[172:175],v[28:31]
s_waitcnt lgkmcnt(4)
v_readfirstlane_b32 s73,v249
s_nop 0
s_cmp_eq_u32 s73,3
s_cbranch_scc0 .Lnopub
v_lshlrev_b32 v229,13,v239
v_mov_b32 v228,s59
buffer_store_dword v228,v229,s[40:43],s68 offen offset:512 sc1
.Lnopub:
s_lshl_b32 s73,%1,2
s_add_u32 s73,s73,s60
s_cmp_eq_u32 s73,1020
s_cbranch_scc1 .Lep0
.Lepb:
s_cmp_ge_u32 s28,30
s_cbranch_scc1 .Lidle
v_mfma_f32_16x16x32_bf16 v[36:39],v[192:195],a[184:187],v[36:39]
buffer_load_dwordx4 a[220:223],v252,s[48:51],s61 offen offset:1024 sc1
v_mfma_f32_16x16x32_bf16 v[36:39],v[196:199],a[192:195],v[36:39]
s_add_u32 s61,s61,6144
v_mfma_f32_16x16x32_bf16 v[44:47],v[192:195],a[188:191],v[44:47]
v_mfma_f32_16x16x32_bf16 v[44:47],v[196:199],a[196:199],v[44:47]
buffer_load_dwordx4 a[224:227],v239,s[48:51],s61 offen sc1
v_mfma_f32_16x16x32_bf16 v[48:51],v[200:203],v[176:179],v[48:51]
ds_read_b64_tr_b16 v[184:185],v233 offset:16896
ds_read_b64_tr_b16 v[186:187],v230 offset:33280
v_mfma_f32_16x16x32_bf16 v[48:51],v[204:207],a[160:163],v[48:51]
ds_read_b64_tr_b16 v[188:189],v233 offset:512
ds_read_b64_tr_b16 v[190:191],v233 offset:49664
v_mfma_f32_16x16x32_bf16 v[56:59],v[200:203],v[180:183],v[56:59]
buffer_load_dwordx4 a[228:231],v239,s[48:51],s61 offen offset:1024 sc1
v_mfma_f32_16x16x32_bf16 v[56:59],v[204:207],a[164:167],v[56:59]
v_mfma_f32_16x16x32_bf16 v[128:131],v[200:203],a[176:179],v[128:131]
v_mfma_f32_16x16x32_bf16 v[128:131],v[204:207],a[168:171],v[128:131]
buffer_load_dwordx4 a[232:235],v239,s[48:51],s61 offen offset:2048 sc1
v_mfma_f32_16x16x32_bf16 v[136:139],v[200:203],a[180:183],v[136:139]
v_mfma_f32_16x16x32_bf16 v[136:139],v[204:207],a[172:175],v[136:139]
v_mfma_f32_16x16x32_bf16 v[96:99],v[200:203],a[184:187],v[96:99]
buffer_load_dwordx4 a[236:239],v239,s[48:51],s61 offen offset:3072 sc1
v_mfma_f32_16x16x32_bf16 v[96:99],v[204:207],a[192:195],v[96:99]
v_mfma_f32_16x16x32_bf16 v[216:219],v[200:203],a[188:191],v[216:219]
v_mfma_f32_16x16x32_bf16 v[216:219],v[204:207],a[196:199],v[216:219]
s_waitcnt lgkmcnt(4)
buffer_load_dwordx4 a[240:243],v252,s[48:51],s61 offen sc1
v_mfma_f32_16x16x32_bf16 v[52:55],v[208:211],v[176:179],v[52:55]
ds_read_b64_tr_b16 v[192:193],v234 offset:16896
ds_read_b64_tr_b16 v[194:195],v231 offset:33280
v_mfma_f32_16x16x32_bf16 v[52:55],v[212:215],a[160:163],v[52:55]
ds_read_b64_tr_b16 v[196:197],v234 offset:512
ds_read_b64_tr_b16 v[198:199],v234 offset:49664
v_mfma_f32_16x16x32_bf16 v[60:63],v[208:211],v[180:183],v[60:63]
buffer_load_dwordx4 a[244:247],v252,s[48:51],s61 offen offset:1024 sc1
v_mfma_f32_16x16x32_bf16 v[60:63],v[212:215],a[164:167],v[60:63]
s_add_u32 s61,s61,6144
s_mov_b32 m0,s63
v_mfma_f32_16x16x32_bf16 v[132:135],v[208:211],a[176:179],v[132:135]
v_mfma_f32_16x16x32_bf16 v[132:135],v[212:215],a[168:171],v[132:135]
buffer_load_dwordx4 v235,s[32:35],s62 offen lds
v_mfma_f32_16x16x32_bf16 v[140:143],v[208:211],a[180:183],v[140:143]
v_mfma_f32_16x16x32_bf16 v[140:143],v[212:215],a[172:175],v[140:143]
v_mfma_f32_16x16x32_bf16 v[144:147],v[208:211],a[184:187],v[144:147]
buffer_load_dwordx4 v236,s[32:35],s62 offen offset:1024 lds
v_mfma_f32_16x16x32_bf16 v[144:147],v[212:215],a[192:195],v[144:147]
v_mfma_f32_16x16x32_bf16 v[220:223],v[208:211],a[188:191],v[220:223]
v_mfma_f32_16x16x32_bf16 v[220:223],v[212:215],a[196:199],v[220:223]
s_waitcnt lgkmcnt(4)
buffer_load_dwordx4 v237,s[32:35],s62 offen offset:2048 lds
v_mfma_f32_16x16x32_bf16 a[128:131],v[184:187],v[176:179],a[128:131]
ds_read_b64_tr_b16 v[200:201],v233 offset:17152
ds_read_b64_tr_b16 v[202:203],v230 offset:33536
v_mfma_f32_16x16x32_bf16 a[128:131],v[188:191],a[160:163],a[128:131]
ds_read_b64_tr_b16 v[204:205],v233 offset:768
ds_read_b64_tr_b16 v[206:207],v233 offset:49920
v_mfma_f32_16x16x32_bf16 a[136:139],v[184:187],v[180:183],a[136:139]
buffer_load_dwordx4 v238,s[32:35],s62 offen offset:3072 lds
v_mfma_f32_16x16x32_bf16 a[136:139],v[188:191],a[164:167],a[136:139]
s_add_u32 s62,s62,20480
v_mfma_f32_16x16x32_bf16 a[144:147],v[184:187],a[176:179],a[144:147]
v_mfma_f32_16x16x32_bf16 a[144:147],v[188:191],a[168:171],a[144:147]
v_mfma_f32_16x16x32_bf16 a[152:155],v[184:187],a[180:183],a[152:155]
v_mfma_f32_16x16x32_bf16 a[152:155],v[188:191],a[172:175],a[152:155]
v_mfma_f32_16x16x32_bf16 a[248:251],v[184:187],a[184:187],a[248:251]
v_mfma_f32_16x16x32_bf16 a[248:251],v[188:191],a[192:195],a[248:251]
v_mfma_f32_16x16x32_bf16 v[72:75],v[184:187],a[188:191],v[72:75]
v_mfma_f32_16x16x32_bf16 v[72:75],v[188:191],a[196:199],v[72:75]
s_waitcnt lgkmcnt(4)
v_mfma_f32_16x16x32_bf16 a[132:135],v[192:195],v[176:179],a[132:135]
ds_read_b64_tr_b16 v[208:209],v234 offset:17152
ds_read_b64_tr_b16 v[210:211],v231 offset:33536
v_mfma_f32_16x16x32_bf16 a[132:135],v[196:199],a[160:163],a[132:135]
ds_read_b64_tr_b16 v[212:213],v234 offset:768
ds_read_b64_tr_b16 v[214:215],v234 offset:49920
v_mfma_f32_16x16x32_bf16 a[140:143],v[192:195],v[180:183],a[140:143]
v_mfma_f32_16x16x32_bf16 a[140:143],v[196:199],a[164:167],a[140:143]
v_mfma_f32_16x16x32_bf16 a[148:151],v[192:195],a[176:179],a[148:151]
v_mfma_f32_16x16x32_bf16 a[148:151],v[196:199],a[168:171],a[148:151]
v_mfma_f32_16x16x32_bf16 a[156:159],v[192:195],a[180:183],a[156:159]
)A"
R"A(v_mfma_f32_16x16x32_bf16 a[156:159],v[196:199],a[172:175],a[156:159]
v_mfma_f32_16x16x32_bf16 a[252:255],v[192:195],a[184:187],a[252:255]
v_mfma_f32_16x16x32_bf16 a[252:255],v[196:199],a[192:195],a[252:255]
s_waitcnt lgkmcnt(2)
v_mfma_f32_16x16x32_bf16 v[76:79],v[192:195],a[188:191],v[76:79]
s_waitcnt lgkmcnt(0)
s_add_u32 m0,s63,81920
v_mfma_f32_16x16x32_bf16 v[76:79],v[196:199],a[196:199],v[76:79]
s_waitcnt vmcnt(24)
buffer_load_dwordx4 v235,s[32:35],s62 offen lds
v_mfma_f32_16x16x32_bf16 a[0:3],v[200:203],v[176:179],a[0:3]
s_waitcnt vmcnt(17)
ds_read_b64_tr_b16 v[184:185],v230 offset:16384
ds_read_b64_tr_b16 v[186:187],v233 offset:32768
v_mfma_f32_16x16x32_bf16 a[0:3],v[204:207],a[160:163],a[0:3]
ds_read_b64_tr_b16 v[188:189],v230 offset:49152
ds_read_b64_tr_b16 v[190:191],v226 offset:16384
v_mfma_f32_16x16x32_bf16 a[32:35],v[200:203],v[180:183],a[32:35]
buffer_load_dwordx4 v236,s[32:35],s62 offen offset:1024 lds
buffer_load_dwordx4 v237,s[32:35],s62 offen offset:2048 lds
v_mfma_f32_16x16x32_bf16 a[32:35],v[204:207],a[164:167],a[32:35]
buffer_load_dwordx4 v238,s[32:35],s62 offen offset:3072 lds
v_mfma_f32_16x16x32_bf16 a[64:67],v[200:203],a[176:179],a[64:67]
s_add_u32 s62,s62,20480
s_add_u32 m0,s63,32768
v_mfma_f32_16x16x32_bf16 a[64:67],v[204:207],a[168:171],a[64:67]
buffer_load_dwordx4 v235,s[32:35],s62 offen lds
v_mfma_f32_16x16x32_bf16 a[96:99],v[200:203],a[180:183],a[96:99]
v_mfma_f32_16x16x32_bf16 a[96:99],v[204:207],a[172:175],a[96:99]
v_mfma_f32_16x16x32_bf16 a[8:11],v[200:203],a[184:187],a[8:11]
buffer_load_dwordx4 v236,s[32:35],s62 offen offset:1024 lds
v_mfma_f32_16x16x32_bf16 a[8:11],v[204:207],a[192:195],a[8:11]
v_mfma_f32_16x16x32_bf16 a[40:43],v[200:203],a[188:191],a[40:43]
v_mfma_f32_16x16x32_bf16 a[40:43],v[204:207],a[196:199],a[40:43]
buffer_load_dwordx4 v237,s[32:35],s62 offen offset:2048 lds
v_mfma_f32_16x16x32_bf16 a[4:7],v[208:211],v[176:179],a[4:7]
ds_read_b64_tr_b16 v[192:193],v231 offset:16384
ds_read_b64_tr_b16 v[194:195],v234 offset:32768
v_mfma_f32_16x16x32_bf16 a[4:7],v[212:215],a[160:163],a[4:7]
ds_read_b64_tr_b16 v[196:197],v231 offset:49152
ds_read_b64_tr_b16 v[198:199],v232 offset:16384
v_mfma_f32_16x16x32_bf16 a[36:39],v[208:211],v[180:183],a[36:39]
buffer_load_dwordx4 v238,s[32:35],s62 offen offset:3072 lds
v_mfma_f32_16x16x32_bf16 a[36:39],v[212:215],a[164:167],a[36:39]
s_add_u32 s62,s62,20480
s_add_u32 m0,s63,65536
v_mfma_f32_16x16x32_bf16 a[68:71],v[208:211],a[176:179],a[68:71]
v_mfma_f32_16x16x32_bf16 a[68:71],v[212:215],a[168:171],a[68:71]
buffer_load_dwordx4 v235,s[32:35],s62 offen lds
v_mfma_f32_16x16x32_bf16 a[100:103],v[208:211],a[180:183],a[100:103]
v_mfma_f32_16x16x32_bf16 a[100:103],v[212:215],a[172:175],a[100:103]
v_mfma_f32_16x16x32_bf16 a[12:15],v[208:211],a[184:187],a[12:15]
buffer_load_dwordx4 v236,s[32:35],s62 offen offset:1024 lds
v_mfma_f32_16x16x32_bf16 a[12:15],v[212:215],a[192:195],a[12:15]
v_mfma_f32_16x16x32_bf16 a[44:47],v[208:211],a[188:191],a[44:47]
v_mfma_f32_16x16x32_bf16 a[44:47],v[212:215],a[196:199],a[44:47]
s_waitcnt lgkmcnt(4)
buffer_load_dwordx4 v237,s[32:35],s62 offen offset:2048 lds
s_waitcnt vmcnt(26)
v_mfma_f32_16x16x32_bf16 v[0:3],v[184:187],a[200:203],v[0:3]
ds_read_b64_tr_b16 v[200:201],v230 offset:16640
ds_read_b64_tr_b16 v[202:203],v233 offset:33024
s_waitcnt vmcnt(20)
v_mfma_f32_16x16x32_bf16 v[0:3],v[188:191],a[224:227],v[0:3]
ds_read_b64_tr_b16 v[204:205],v230 offset:49408
ds_read_b64_tr_b16 v[206:207],v226 offset:16640
v_mfma_f32_16x16x32_bf16 v[8:11],v[184:187],a[204:207],v[8:11]
buffer_load_dwordx4 v238,s[32:35],s62 offen offset:3072 lds
s_waitcnt vmcnt(20)
v_mfma_f32_16x16x32_bf16 v[8:11],v[188:191],a[228:231],v[8:11]
s_add_u32 s62,s62,184320
v_mfma_f32_16x16x32_bf16 v[16:19],v[184:187],a[208:211],v[16:19]
s_waitcnt vmcnt(19)
v_mfma_f32_16x16x32_bf16 v[16:19],v[188:191],a[232:235],v[16:19]
buffer_load_dwordx4 v[152:155],v239,s[48:51],s61 offen sc1
v_mfma_f32_16x16x32_bf16 v[24:27],v[184:187],a[212:215],v[24:27]
s_waitcnt vmcnt(19)
v_mfma_f32_16x16x32_bf16 v[24:27],v[188:191],a[236:239],v[24:27]
v_mfma_f32_16x16x32_bf16 v[32:35],v[184:187],a[216:219],v[32:35]
buffer_load_dwordx4 v[156:159],v239,s[48:51],s61 offen offset:1024 sc1
s_waitcnt vmcnt(19)
v_mfma_f32_16x16x32_bf16 v[32:35],v[188:191],a[240:243],v[32:35]
v_mfma_f32_16x16x32_bf16 v[40:43],v[184:187],a[220:223],v[40:43]
s_waitcnt vmcnt(18)
v_mfma_f32_16x16x32_bf16 v[40:43],v[188:191],a[244:247],v[40:43]
s_waitcnt lgkmcnt(4)
buffer_load_dwordx4 v[160:163],v239,s[48:51],s61 offen offset:2048 sc1
v_mfma_f32_16x16x32_bf16 v[4:7],v[192:195],a[200:203],v[4:7]
ds_read_b64_tr_b16 v[208:209],v231 offset:16640
ds_read_b64_tr_b16 v[210:211],v234 offset:33024
v_mfma_f32_16x16x32_bf16 v[4:7],v[196:199],a[224:227],v[4:7]
ds_read_b64_tr_b16 v[212:213],v231 offset:49408
ds_read_b64_tr_b16 v[214:215],v232 offset:16640
v_mfma_f32_16x16x32_bf16 v[12:15],v[192:195],a[204:207],v[12:15]
buffer_load_dwordx4 v[164:167],v239,s[48:51],s61 offen offset:3072 sc1
v_mfma_f32_16x16x32_bf16 v[12:15],v[196:199],a[228:231],v[12:15]
v_mfma_f32_16x16x32_bf16 v[20:23],v[192:195],a[208:211],v[20:23]
v_mfma_f32_16x16x32_bf16 v[20:23],v[196:199],a[232:235],v[20:23]
buffer_load_dwordx4 v[168:171],v252,s[48:51],s61 offen sc1
v_mfma_f32_16x16x32_bf16 v[28:31],v[192:195],a[212:215],v[28:31]
v_mfma_f32_16x16x32_bf16 v[28:31],v[196:199],a[236:239],v[28:31]
v_mfma_f32_16x16x32_bf16 v[36:39],v[192:195],a[216:219],v[36:39]
buffer_load_dwordx4 v[172:175],v252,s[48:51],s61 offen offset:1024 sc1
v_mfma_f32_16x16x32_bf16 v[36:39],v[196:199],a[240:243],v[36:39]
s_add_u32 s61,s61,6144
v_mfma_f32_16x16x32_bf16 v[44:47],v[192:195],a[220:223],v[44:47]
v_mfma_f32_16x16x32_bf16 v[44:47],v[196:199],a[244:247],v[44:47]
s_waitcnt lgkmcnt(4)
buffer_load_dwordx4 v[176:179],v239,s[48:51],s61 offen sc1
v_mfma_f32_16x16x32_bf16 v[48:51],v[200:203],a[200:203],v[48:51]
ds_read_b64_tr_b16 v[184:185],v230 offset:16896
ds_read_b64_tr_b16 v[186:187],v233 offset:33280
v_mfma_f32_16x16x32_bf16 v[48:51],v[204:207],a[224:227],v[48:51]
ds_read_b64_tr_b16 v[188:189],v230 offset:49664
ds_read_b64_tr_b16 v[190:191],v226 offset:16896
v_mfma_f32_16x16x32_bf16 v[56:59],v[200:203],a[204:207],v[56:59]
buffer_load_dwordx4 v[180:183],v239,s[48:51],s61 offen offset:1024 sc1
v_mfma_f32_16x16x32_bf16 v[56:59],v[204:207],a[228:231],v[56:59]
v_mfma_f32_16x16x32_bf16 v[128:131],v[200:203],a[208:211],v[128:131]
v_mfma_f32_16x16x32_bf16 v[128:131],v[204:207],a[232:235],v[128:131]
buffer_load_dwordx4 a[176:179],v239,s[48:51],s61 offen offset:2048 sc1
v_mfma_f32_16x16x32_bf16 v[136:139],v[200:203],a[212:215],v[136:139]
v_mfma_f32_16x16x32_bf16 v[136:139],v[204:207],a[236:239],v[136:139]
v_mfma_f32_16x16x32_bf16 v[96:99],v[200:203],a[216:219],v[96:99]
buffer_load_dwordx4 a[180:183],v239,s[48:51],s61 offen offset:3072 sc1
v_mfma_f32_16x16x32_bf16 v[96:99],v[204:207],a[240:243],v[96:99]
v_mfma_f32_16x16x32_bf16 v[216:219],v[200:203],a[220:223],v[216:219]
v_mfma_f32_16x16x32_bf16 v[216:219],v[204:207],a[244:247],v[216:219]
s_waitcnt lgkmcnt(4)
buffer_load_dwordx4 a[184:187],v252,s[48:51],s61 offen sc1
v_mfma_f32_16x16x32_bf16 v[52:55],v[208:211],a[200:203],v[52:55]
ds_read_b64_tr_b16 v[192:193],v231 offset:16896
ds_read_b64_tr_b16 v[194:195],v234 offset:33280
v_mfma_f32_16x16x32_bf16 v[52:55],v[212:215],a[224:227],v[52:55]
ds_read_b64_tr_b16 v[196:197],v231 offset:49664
ds_read_b64_tr_b16 v[198:199],v232 offset:16896
v_mfma_f32_16x16x32_bf16 v[60:63],v[208:211],a[204:207],v[60:63]
buffer_load_dwordx4 a[188:191],v252,s[48:51],s61 offen offset:1024 sc1
v_mfma_f32_16x16x32_bf16 v[60:63],v[212:215],a[228:231],v[60:63]
s_add_u32 s61,s61,30720
s_add_u32 m0,s63,114688
v_mfma_f32_16x16x32_bf16 v[132:135],v[208:211],a[208:211],v[132:135]
v_mfma_f32_16x16x32_bf16 v[132:135],v[212:215],a[232:235],v[132:135]
buffer_load_dwordx4 v235,s[32:35],s62 offen lds
v_mfma_f32_16x16x32_bf16 v[140:143],v[208:211],a[212:215],v[140:143]
v_mfma_f32_16x16x32_bf16 v[140:143],v[212:215],a[236:239],v[140:143]
v_mfma_f32_16x16x32_bf16 v[144:147],v[208:211],a[216:219],v[144:147]
buffer_load_dwordx4 v236,s[32:35],s62 offen offset:1024 lds
v_mfma_f32_16x16x32_bf16 v[144:147],v[212:215],a[240:243],v[144:147]
v_mfma_f32_16x16x32_bf16 v[220:223],v[208:211],a[220:223],v[220:223]
v_mfma_f32_16x16x32_bf16 v[220:223],v[212:215],a[244:247],v[220:223]
s_waitcnt lgkmcnt(4)
buffer_load_dwordx4 v237,s[32:35],s62 offen offset:2048 lds
v_mfma_f32_16x16x32_bf16 a[128:131],v[184:187],a[200:203],a[128:131]
ds_read_b64_tr_b16 v[200:201],v230 offset:17152
ds_read_b64_tr_b16 v[202:203],v233 offset:33536
v_mfma_f32_16x16x32_bf16 a[128:131],v[188:191],a[224:227],a[128:131]
ds_read_b64_tr_b16 v[204:205],v230 offset:49920
ds_read_b64_tr_b16 v[206:207],v226 offset:17152
v_mfma_f32_16x16x32_bf16 a[136:139],v[184:187],a[204:207],a[136:139]
buffer_load_dwordx4 v238,s[32:35],s62 offen offset:3072 lds
v_mfma_f32_16x16x32_bf16 a[136:139],v[188:191],a[228:231],a[136:139]
s_add_u32 s62,s62,20480
v_mfma_f32_16x16x32_bf16 a[144:147],v[184:187],a[208:211],a[144:147]
v_mfma_f32_16x16x32_bf16 a[144:147],v[188:191],a[232:235],a[144:147]
v_mfma_f32_16x16x32_bf16 a[152:155],v[184:187],a[212:215],a[152:155]
v_mfma_f32_16x16x32_bf16 a[152:155],v[188:191],a[236:239],a[152:155]
v_mfma_f32_16x16x32_bf16 a[248:251],v[184:187],a[216:219],a[248:251]
v_mfma_f32_16x16x32_bf16 a[248:251],v[188:191],a[240:243],a[248:251]
v_mfma_f32_16x16x32_bf16 v[72:75],v[184:187],a[220:223],v[72:75]
v_mfma_f32_16x16x32_bf16 v[72:75],v[188:191],a[244:247],v[72:75]
s_waitcnt lgkmcnt(4)
v_mfma_f32_16x16x32_bf16 a[132:135],v[192:195],a[200:203],a[132:135]
ds_read_b64_tr_b16 v[208:209],v231 offset:17152
ds_read_b64_tr_b16 v[210:211],v234 offset:33536
v_mfma_f32_16x16x32_bf16 a[132:135],v[196:199],a[224:227],a[132:135]
ds_read_b64_tr_b16 v[212:213],v231 offset:49920
ds_read_b64_tr_b16 v[214:215],v232 offset:17152
v_mfma_f32_16x16x32_bf16 a[140:143],v[192:195],a[204:207],a[140:143]
v_mfma_f32_16x16x32_bf16 a[140:143],v[196:199],a[228:231],a[140:143]
v_mfma_f32_16x16x32_bf16 a[148:151],v[192:195],a[208:211],a[148:151]
v_mfma_f32_16x16x32_bf16 a[148:151],v[196:199],a[232:235],a[148:151]
v_mfma_f32_16x16x32_bf16 a[156:159],v[192:195],a[212:215],a[156:159]
v_mfma_f32_16x16x32_bf16 a[156:159],v[196:199],a[236:239],a[156:159]
v_mfma_f32_16x16x32_bf16 a[252:255],v[192:195],a[216:219],a[252:255]
v_mfma_f32_16x16x32_bf16 a[252:255],v[196:199],a[240:243],a[252:255]
s_waitcnt lgkmcnt(2)
v_mfma_f32_16x16x32_bf16 v[76:79],v[192:195],a[220:223],v[76:79]
s_waitcnt lgkmcnt(0)
s_add_u32 m0,s63,16384
v_mfma_f32_16x16x32_bf16 v[76:79],v[196:199],a[244:247],v[76:79]
s_waitcnt vmcnt(24)
buffer_load_dwordx4 v235,s[32:35],s62 offen lds
v_mfma_f32_16x16x32_bf16 a[0:3],v[200:203],a[200:203],a[0:3]
s_waitcnt vmcnt(17)
ds_read_b64_tr_b16 v[184:185],v230
ds_read_b64_tr_b16 v[186:187],v233 offset:16384
v_mfma_f32_16x16x32_bf16 a[0:3],v[204:207],a[224:227],a[0:3]
ds_read_b64_tr_b16 v[188:189],v230 offset:32768
ds_read_b64_tr_b16 v[190:191],v233
v_mfma_f32_16x16x32_bf16 a[32:35],v[200:203],a[204:207],a[32:35]
buffer_load_dwordx4 v236,s[32:35],s62 offen offset:1024 lds
buffer_load_dwordx4 v237,s[32:35],s62 offen offset:2048 lds
v_mfma_f32_16x16x32_bf16 a[32:35],v[204:207],a[228:231],a[32:35]
buffer_load_dwordx4 v238,s[32:35],s62 offen offset:3072 lds
v_mfma_f32_16x16x32_bf16 a[64:67],v[200:203],a[208:211],a[64:67]
s_add_u32 s62,s62,20480
s_add_u32 m0,s63,98304
v_mfma_f32_16x16x32_bf16 a[64:67],v[204:207],a[232:235],a[64:67]
buffer_load_dwordx4 v235,s[32:35],s62 offen lds
v_mfma_f32_16x16x32_bf16 a[96:99],v[200:203],a[212:215],a[96:99]
v_mfma_f32_16x16x32_bf16 a[96:99],v[204:207],a[236:239],a[96:99]
v_mfma_f32_16x16x32_bf16 a[8:11],v[200:203],a[216:219],a[8:11]
buffer_load_dwordx4 v236,s[32:35],s62 offen offset:1024 lds
v_mfma_f32_16x16x32_bf16 a[8:11],v[204:207],a[240:243],a[8:11]
v_mfma_f32_16x16x32_bf16 a[40:43],v[200:203],a[220:223],a[40:43]
v_mfma_f32_16x16x32_bf16 a[40:43],v[204:207],a[244:247],a[40:43]
buffer_load_dwordx4 v237,s[32:35],s62 offen offset:2048 lds
v_mfma_f32_16x16x32_bf16 a[4:7],v[208:211],a[200:203],a[4:7]
)A"
R"A(ds_read_b64_tr_b16 v[192:193],v231
ds_read_b64_tr_b16 v[194:195],v234 offset:16384
v_mfma_f32_16x16x32_bf16 a[4:7],v[212:215],a[224:227],a[4:7]
ds_read_b64_tr_b16 v[196:197],v231 offset:32768
ds_read_b64_tr_b16 v[198:199],v234
v_mfma_f32_16x16x32_bf16 a[36:39],v[208:211],a[204:207],a[36:39]
buffer_load_dwordx4 v238,s[32:35],s62 offen offset:3072 lds
v_mfma_f32_16x16x32_bf16 a[36:39],v[212:215],a[228:231],a[36:39]
s_add_u32 s62,s62,20480
s_add_u32 m0,s63,49152
v_mfma_f32_16x16x32_bf16 a[68:71],v[208:211],a[208:211],a[68:71]
v_mfma_f32_16x16x32_bf16 a[68:71],v[212:215],a[232:235],a[68:71]
buffer_load_dwordx4 v235,s[32:35],s62 offen lds
v_mfma_f32_16x16x32_bf16 a[100:103],v[208:211],a[212:215],a[100:103]
v_mfma_f32_16x16x32_bf16 a[100:103],v[212:215],a[236:239],a[100:103]
v_mfma_f32_16x16x32_bf16 a[12:15],v[208:211],a[216:219],a[12:15]
buffer_load_dwordx4 v236,s[32:35],s62 offen offset:1024 lds
v_mfma_f32_16x16x32_bf16 a[12:15],v[212:215],a[240:243],a[12:15]
v_mfma_f32_16x16x32_bf16 a[44:47],v[208:211],a[220:223],a[44:47]
v_mfma_f32_16x16x32_bf16 a[44:47],v[212:215],a[244:247],a[44:47]
s_waitcnt lgkmcnt(4)
buffer_load_dwordx4 v237,s[32:35],s62 offen offset:2048 lds
s_waitcnt vmcnt(26)
v_mfma_f32_16x16x32_bf16 v[0:3],v[184:187],v[152:155],v[0:3]
ds_read_b64_tr_b16 v[200:201],v230 offset:256
ds_read_b64_tr_b16 v[202:203],v233 offset:16640
s_waitcnt vmcnt(20)
v_mfma_f32_16x16x32_bf16 v[0:3],v[188:191],v[176:179],v[0:3]
ds_read_b64_tr_b16 v[204:205],v230 offset:33024
ds_read_b64_tr_b16 v[206:207],v233 offset:256
v_mfma_f32_16x16x32_bf16 v[8:11],v[184:187],v[156:159],v[8:11]
buffer_load_dwordx4 v238,s[32:35],s62 offen offset:3072 lds
s_waitcnt vmcnt(20)
v_mfma_f32_16x16x32_bf16 v[8:11],v[188:191],v[180:183],v[8:11]
s_add_u32 s62,s62,20480
v_mfma_f32_16x16x32_bf16 v[16:19],v[184:187],v[160:163],v[16:19]
s_waitcnt vmcnt(19)
v_mfma_f32_16x16x32_bf16 v[16:19],v[188:191],a[176:179],v[16:19]
buffer_load_dwordx4 a[160:163],v239,s[48:51],s61 offen sc1
v_mfma_f32_16x16x32_bf16 v[24:27],v[184:187],v[164:167],v[24:27]
s_waitcnt vmcnt(19)
v_mfma_f32_16x16x32_bf16 v[24:27],v[188:191],a[180:183],v[24:27]
v_mfma_f32_16x16x32_bf16 v[32:35],v[184:187],v[168:171],v[32:35]
buffer_load_dwordx4 a[164:167],v239,s[48:51],s61 offen offset:1024 sc1
s_waitcnt vmcnt(19)
v_mfma_f32_16x16x32_bf16 v[32:35],v[188:191],a[184:187],v[32:35]
v_mfma_f32_16x16x32_bf16 v[40:43],v[184:187],v[172:175],v[40:43]
s_waitcnt vmcnt(18)
v_mfma_f32_16x16x32_bf16 v[40:43],v[188:191],a[188:191],v[40:43]
s_waitcnt lgkmcnt(4)
buffer_load_dwordx4 a[168:171],v239,s[48:51],s61 offen offset:2048 sc1
v_mfma_f32_16x16x32_bf16 v[4:7],v[192:195],v[152:155],v[4:7]
ds_read_b64_tr_b16 v[208:209],v231 offset:256
ds_read_b64_tr_b16 v[210:211],v234 offset:16640
v_mfma_f32_16x16x32_bf16 v[4:7],v[196:199],v[176:179],v[4:7]
ds_read_b64_tr_b16 v[212:213],v231 offset:33024
ds_read_b64_tr_b16 v[214:215],v234 offset:256
v_mfma_f32_16x16x32_bf16 v[12:15],v[192:195],v[156:159],v[12:15]
buffer_load_dwordx4 a[172:175],v239,s[48:51],s61 offen offset:3072 sc1
v_mfma_f32_16x16x32_bf16 v[12:15],v[196:199],v[180:183],v[12:15]
v_mfma_f32_16x16x32_bf16 v[20:23],v[192:195],v[160:163],v[20:23]
v_mfma_f32_16x16x32_bf16 v[20:23],v[196:199],a[176:179],v[20:23]
buffer_load_dwordx4 a[192:195],v252,s[48:51],s61 offen sc1
v_mfma_f32_16x16x32_bf16 v[28:31],v[192:195],v[164:167],v[28:31]
v_mfma_f32_16x16x32_bf16 v[28:31],v[196:199],a[180:183],v[28:31]
v_mfma_f32_16x16x32_bf16 v[36:39],v[192:195],v[168:171],v[36:39]
buffer_load_dwordx4 a[196:199],v252,s[48:51],s61 offen offset:1024 sc1
v_mfma_f32_16x16x32_bf16 v[36:39],v[196:199],a[184:187],v[36:39]
s_add_u32 s61,s61,6144
v_mfma_f32_16x16x32_bf16 v[44:47],v[192:195],v[172:175],v[44:47]
v_mfma_f32_16x16x32_bf16 v[44:47],v[196:199],a[188:191],v[44:47]
s_waitcnt lgkmcnt(4)
buffer_load_dwordx4 a[200:203],v239,s[48:51],s61 offen sc1
v_mfma_f32_16x16x32_bf16 v[48:51],v[200:203],v[152:155],v[48:51]
ds_read_b64_tr_b16 v[184:185],v230 offset:512
ds_read_b64_tr_b16 v[186:187],v233 offset:16896
v_mfma_f32_16x16x32_bf16 v[48:51],v[204:207],v[176:179],v[48:51]
ds_read_b64_tr_b16 v[188:189],v230 offset:33280
ds_read_b64_tr_b16 v[190:191],v233 offset:512
v_mfma_f32_16x16x32_bf16 v[56:59],v[200:203],v[156:159],v[56:59]
buffer_load_dwordx4 a[204:207],v239,s[48:51],s61 offen offset:1024 sc1
v_mfma_f32_16x16x32_bf16 v[56:59],v[204:207],v[180:183],v[56:59]
v_mfma_f32_16x16x32_bf16 v[128:131],v[200:203],v[160:163],v[128:131]
v_mfma_f32_16x16x32_bf16 v[128:131],v[204:207],a[176:179],v[128:131]
buffer_load_dwordx4 a[208:211],v239,s[48:51],s61 offen offset:2048 sc1
v_mfma_f32_16x16x32_bf16 v[136:139],v[200:203],v[164:167],v[136:139]
v_mfma_f32_16x16x32_bf16 v[136:139],v[204:207],a[180:183],v[136:139]
v_mfma_f32_16x16x32_bf16 v[96:99],v[200:203],v[168:171],v[96:99]
buffer_load_dwordx4 a[212:215],v239,s[48:51],s61 offen offset:3072 sc1
v_mfma_f32_16x16x32_bf16 v[96:99],v[204:207],a[184:187],v[96:99]
v_mfma_f32_16x16x32_bf16 v[216:219],v[200:203],v[172:175],v[216:219]
v_mfma_f32_16x16x32_bf16 v[216:219],v[204:207],a[188:191],v[216:219]
s_waitcnt lgkmcnt(4)
buffer_load_dwordx4 a[216:219],v252,s[48:51],s61 offen sc1
v_mfma_f32_16x16x32_bf16 v[52:55],v[208:211],v[152:155],v[52:55]
ds_read_b64_tr_b16 v[192:193],v231 offset:512
ds_read_b64_tr_b16 v[194:195],v234 offset:16896
v_mfma_f32_16x16x32_bf16 v[52:55],v[212:215],v[176:179],v[52:55]
ds_read_b64_tr_b16 v[196:197],v231 offset:33280
ds_read_b64_tr_b16 v[198:199],v234 offset:512
v_mfma_f32_16x16x32_bf16 v[60:63],v[208:211],v[156:159],v[60:63]
buffer_load_dwordx4 a[220:223],v252,s[48:51],s61 offen offset:1024 sc1
v_mfma_f32_16x16x32_bf16 v[60:63],v[212:215],v[180:183],v[60:63]
s_add_u32 s61,s61,6144
s_add_u32 m0,s63,147456
v_mfma_f32_16x16x32_bf16 v[132:135],v[208:211],v[160:163],v[132:135]
v_mfma_f32_16x16x32_bf16 v[132:135],v[212:215],a[176:179],v[132:135]
buffer_load_dwordx4 v235,s[32:35],s62 offen lds
v_mfma_f32_16x16x32_bf16 v[140:143],v[208:211],v[164:167],v[140:143]
v_mfma_f32_16x16x32_bf16 v[140:143],v[212:215],a[180:183],v[140:143]
v_mfma_f32_16x16x32_bf16 v[144:147],v[208:211],v[168:171],v[144:147]
buffer_load_dwordx4 v236,s[32:35],s62 offen offset:1024 lds
v_mfma_f32_16x16x32_bf16 v[144:147],v[212:215],a[184:187],v[144:147]
v_mfma_f32_16x16x32_bf16 v[220:223],v[208:211],v[172:175],v[220:223]
v_mfma_f32_16x16x32_bf16 v[220:223],v[212:215],a[188:191],v[220:223]
s_waitcnt lgkmcnt(4)
buffer_load_dwordx4 v237,s[32:35],s62 offen offset:2048 lds
v_mfma_f32_16x16x32_bf16 a[128:131],v[184:187],v[152:155],a[128:131]
ds_read_b64_tr_b16 v[200:201],v230 offset:768
ds_read_b64_tr_b16 v[202:203],v233 offset:17152
v_mfma_f32_16x16x32_bf16 a[128:131],v[188:191],v[176:179],a[128:131]
ds_read_b64_tr_b16 v[204:205],v230 offset:33536
ds_read_b64_tr_b16 v[206:207],v233 offset:768
v_mfma_f32_16x16x32_bf16 a[136:139],v[184:187],v[156:159],a[136:139]
buffer_load_dwordx4 v238,s[32:35],s62 offen offset:3072 lds
v_mfma_f32_16x16x32_bf16 a[136:139],v[188:191],v[180:183],a[136:139]
s_add_u32 s62,s62,20480
v_mfma_f32_16x16x32_bf16 a[144:147],v[184:187],v[160:163],a[144:147]
v_mfma_f32_16x16x32_bf16 a[144:147],v[188:191],a[176:179],a[144:147]
v_mfma_f32_16x16x32_bf16 a[152:155],v[184:187],v[164:167],a[152:155]
v_mfma_f32_16x16x32_bf16 a[152:155],v[188:191],a[180:183],a[152:155]
v_mfma_f32_16x16x32_bf16 a[248:251],v[184:187],v[168:171],a[248:251]
v_mfma_f32_16x16x32_bf16 a[248:251],v[188:191],a[184:187],a[248:251]
v_mfma_f32_16x16x32_bf16 v[72:75],v[184:187],v[172:175],v[72:75]
v_mfma_f32_16x16x32_bf16 v[72:75],v[188:191],a[188:191],v[72:75]
s_waitcnt lgkmcnt(4)
v_mfma_f32_16x16x32_bf16 a[132:135],v[192:195],v[152:155],a[132:135]
ds_read_b64_tr_b16 v[208:209],v231 offset:768
ds_read_b64_tr_b16 v[210:211],v234 offset:17152
v_mfma_f32_16x16x32_bf16 a[132:135],v[196:199],v[176:179],a[132:135]
ds_read_b64_tr_b16 v[212:213],v231 offset:33536
ds_read_b64_tr_b16 v[214:215],v234 offset:768
v_mfma_f32_16x16x32_bf16 a[140:143],v[192:195],v[156:159],a[140:143]
v_mfma_f32_16x16x32_bf16 a[140:143],v[196:199],v[180:183],a[140:143]
v_mfma_f32_16x16x32_bf16 a[148:151],v[192:195],v[160:163],a[148:151]
v_mfma_f32_16x16x32_bf16 a[148:151],v[196:199],a[176:179],a[148:151]
v_mfma_f32_16x16x32_bf16 a[156:159],v[192:195],v[164:167],a[156:159]
v_mfma_f32_16x16x32_bf16 a[156:159],v[196:199],a[180:183],a[156:159]
v_mfma_f32_16x16x32_bf16 a[252:255],v[192:195],v[168:171],a[252:255]
v_mfma_f32_16x16x32_bf16 a[252:255],v[196:199],a[184:187],a[252:255]
s_waitcnt lgkmcnt(2)
v_mfma_f32_16x16x32_bf16 v[76:79],v[192:195],v[172:175],v[76:79]
s_waitcnt lgkmcnt(0)
s_mov_b32 m0,s63
v_mfma_f32_16x16x32_bf16 v[76:79],v[196:199],a[188:191],v[76:79]
s_waitcnt vmcnt(24)
buffer_load_dwordx4 v235,s[32:35],s62 offen lds
v_mfma_f32_16x16x32_bf16 a[0:3],v[200:203],v[152:155],a[0:3]
s_waitcnt vmcnt(17)
ds_read_b64_tr_b16 v[184:185],v233 offset:49152
ds_read_b64_tr_b16 v[186:187],v230 offset:16384
v_mfma_f32_16x16x32_bf16 a[0:3],v[204:207],v[176:179],a[0:3]
ds_read_b64_tr_b16 v[188:189],v233 offset:32768
ds_read_b64_tr_b16 v[190:191],v230 offset:49152
v_mfma_f32_16x16x32_bf16 a[32:35],v[200:203],v[156:159],a[32:35]
buffer_load_dwordx4 v236,s[32:35],s62 offen offset:1024 lds
buffer_load_dwordx4 v237,s[32:35],s62 offen offset:2048 lds
v_mfma_f32_16x16x32_bf16 a[32:35],v[204:207],v[180:183],a[32:35]
buffer_load_dwordx4 v238,s[32:35],s62 offen offset:3072 lds
v_mfma_f32_16x16x32_bf16 a[64:67],v[200:203],v[160:163],a[64:67]
s_add_u32 s62,s62,20480
s_add_u32 m0,s63,81920
v_mfma_f32_16x16x32_bf16 a[64:67],v[204:207],a[176:179],a[64:67]
buffer_load_dwordx4 v235,s[32:35],s62 offen lds
v_mfma_f32_16x16x32_bf16 a[96:99],v[200:203],v[164:167],a[96:99]
v_mfma_f32_16x16x32_bf16 a[96:99],v[204:207],a[180:183],a[96:99]
v_mfma_f32_16x16x32_bf16 a[8:11],v[200:203],v[168:171],a[8:11]
buffer_load_dwordx4 v236,s[32:35],s62 offen offset:1024 lds
v_mfma_f32_16x16x32_bf16 a[8:11],v[204:207],a[184:187],a[8:11]
v_mfma_f32_16x16x32_bf16 a[40:43],v[200:203],v[172:175],a[40:43]
v_mfma_f32_16x16x32_bf16 a[40:43],v[204:207],a[188:191],a[40:43]
buffer_load_dwordx4 v237,s[32:35],s62 offen offset:2048 lds
v_mfma_f32_16x16x32_bf16 a[4:7],v[208:211],v[152:155],a[4:7]
ds_read_b64_tr_b16 v[192:193],v234 offset:49152
ds_read_b64_tr_b16 v[194:195],v231 offset:16384
v_mfma_f32_16x16x32_bf16 a[4:7],v[212:215],v[176:179],a[4:7]
ds_read_b64_tr_b16 v[196:197],v234 offset:32768
ds_read_b64_tr_b16 v[198:199],v231 offset:49152
v_mfma_f32_16x16x32_bf16 a[36:39],v[208:211],v[156:159],a[36:39]
buffer_load_dwordx4 v238,s[32:35],s62 offen offset:3072 lds
v_mfma_f32_16x16x32_bf16 a[36:39],v[212:215],v[180:183],a[36:39]
s_add_u32 s62,s62,20480
s_add_u32 m0,s63,32768
v_mfma_f32_16x16x32_bf16 a[68:71],v[208:211],v[160:163],a[68:71]
v_mfma_f32_16x16x32_bf16 a[68:71],v[212:215],a[176:179],a[68:71]
buffer_load_dwordx4 v235,s[32:35],s62 offen lds
v_mfma_f32_16x16x32_bf16 a[100:103],v[208:211],v[164:167],a[100:103]
v_mfma_f32_16x16x32_bf16 a[100:103],v[212:215],a[180:183],a[100:103]
v_mfma_f32_16x16x32_bf16 a[12:15],v[208:211],v[168:171],a[12:15]
buffer_load_dwordx4 v236,s[32:35],s62 offen offset:1024 lds
v_mfma_f32_16x16x32_bf16 a[12:15],v[212:215],a[184:187],a[12:15]
v_mfma_f32_16x16x32_bf16 a[44:47],v[208:211],v[172:175],a[44:47]
v_mfma_f32_16x16x32_bf16 a[44:47],v[212:215],a[188:191],a[44:47]
s_waitcnt lgkmcnt(4)
buffer_load_dwordx4 v237,s[32:35],s62 offen offset:2048 lds
s_waitcnt vmcnt(26)
v_mfma_f32_16x16x32_bf16 v[0:3],v[184:187],a[160:163],v[0:3]
ds_read_b64_tr_b16 v[200:201],v233 offset:49408
ds_read_b64_tr_b16 v[202:203],v230 offset:16640
s_waitcnt vmcnt(20)
v_mfma_f32_16x16x32_bf16 v[0:3],v[188:191],a[200:203],v[0:3]
ds_read_b64_tr_b16 v[204:205],v233 offset:33024
ds_read_b64_tr_b16 v[206:207],v230 offset:49408
v_mfma_f32_16x16x32_bf16 v[8:11],v[184:187],a[164:167],v[8:11]
buffer_load_dwordx4 v238,s[32:35],s62 offen offset:3072 lds
s_waitcnt vmcnt(20)
v_mfma_f32_16x16x32_bf16 v[8:11],v[188:191],a[204:207],v[8:11]
s_add_u32 s62,s62,0xfff15000
v_mfma_f32_16x16x32_bf16 v[16:19],v[184:187],a[168:171],v[16:19]
s_waitcnt vmcnt(19)
)A"
R"A(v_mfma_f32_16x16x32_bf16 v[16:19],v[188:191],a[208:211],v[16:19]
buffer_load_dwordx4 a[224:227],v239,s[48:51],s61 offen sc1
v_mfma_f32_16x16x32_bf16 v[24:27],v[184:187],a[172:175],v[24:27]
s_waitcnt vmcnt(19)
v_mfma_f32_16x16x32_bf16 v[24:27],v[188:191],a[212:215],v[24:27]
v_mfma_f32_16x16x32_bf16 v[32:35],v[184:187],a[192:195],v[32:35]
buffer_load_dwordx4 a[228:231],v239,s[48:51],s61 offen offset:1024 sc1
s_waitcnt vmcnt(19)
v_mfma_f32_16x16x32_bf16 v[32:35],v[188:191],a[216:219],v[32:35]
v_mfma_f32_16x16x32_bf16 v[40:43],v[184:187],a[196:199],v[40:43]
s_waitcnt vmcnt(18)
v_mfma_f32_16x16x32_bf16 v[40:43],v[188:191],a[220:223],v[40:43]
s_waitcnt lgkmcnt(4)
buffer_load_dwordx4 a[232:235],v239,s[48:51],s61 offen offset:2048 sc1
v_mfma_f32_16x16x32_bf16 v[4:7],v[192:195],a[160:163],v[4:7]
ds_read_b64_tr_b16 v[208:209],v234 offset:49408
ds_read_b64_tr_b16 v[210:211],v231 offset:16640
v_mfma_f32_16x16x32_bf16 v[4:7],v[196:199],a[200:203],v[4:7]
ds_read_b64_tr_b16 v[212:213],v234 offset:33024
ds_read_b64_tr_b16 v[214:215],v231 offset:49408
v_mfma_f32_16x16x32_bf16 v[12:15],v[192:195],a[164:167],v[12:15]
buffer_load_dwordx4 a[236:239],v239,s[48:51],s61 offen offset:3072 sc1
v_mfma_f32_16x16x32_bf16 v[12:15],v[196:199],a[204:207],v[12:15]
v_mfma_f32_16x16x32_bf16 v[20:23],v[192:195],a[168:171],v[20:23]
v_mfma_f32_16x16x32_bf16 v[20:23],v[196:199],a[208:211],v[20:23]
buffer_load_dwordx4 a[240:243],v252,s[48:51],s61 offen sc1
v_mfma_f32_16x16x32_bf16 v[28:31],v[192:195],a[172:175],v[28:31]
v_mfma_f32_16x16x32_bf16 v[28:31],v[196:199],a[212:215],v[28:31]
v_mfma_f32_16x16x32_bf16 v[36:39],v[192:195],a[192:195],v[36:39]
buffer_load_dwordx4 a[244:247],v252,s[48:51],s61 offen offset:1024 sc1
v_mfma_f32_16x16x32_bf16 v[36:39],v[196:199],a[216:219],v[36:39]
s_add_u32 s61,s61,6144
v_and_b32 v255,0x380,v251
v_mfma_f32_16x16x32_bf16 v[44:47],v[192:195],a[196:199],v[44:47]
buffer_load_dword v253,v255,s[40:43],s69 offen sc1
v_mfma_f32_16x16x32_bf16 v[44:47],v[196:199],a[220:223],v[44:47]
s_waitcnt lgkmcnt(4)
buffer_load_dwordx4 v[152:155],v239,s[48:51],s61 offen sc1
v_mfma_f32_16x16x32_bf16 v[48:51],v[200:203],a[160:163],v[48:51]
ds_read_b64_tr_b16 v[184:185],v233 offset:49664
ds_read_b64_tr_b16 v[186:187],v230 offset:16896
v_mfma_f32_16x16x32_bf16 v[48:51],v[204:207],a[200:203],v[48:51]
ds_read_b64_tr_b16 v[188:189],v233 offset:33280
ds_read_b64_tr_b16 v[190:191],v230 offset:49664
v_mfma_f32_16x16x32_bf16 v[56:59],v[200:203],a[164:167],v[56:59]
buffer_load_dwordx4 v[156:159],v239,s[48:51],s61 offen offset:1024 sc1
v_mfma_f32_16x16x32_bf16 v[56:59],v[204:207],a[204:207],v[56:59]
v_mfma_f32_16x16x32_bf16 v[128:131],v[200:203],a[168:171],v[128:131]
v_mfma_f32_16x16x32_bf16 v[128:131],v[204:207],a[208:211],v[128:131]
buffer_load_dwordx4 v[160:163],v239,s[48:51],s61 offen offset:2048 sc1
v_mfma_f32_16x16x32_bf16 v[136:139],v[200:203],a[172:175],v[136:139]
v_mfma_f32_16x16x32_bf16 v[136:139],v[204:207],a[212:215],v[136:139]
v_mfma_f32_16x16x32_bf16 v[96:99],v[200:203],a[192:195],v[96:99]
buffer_load_dwordx4 v[164:167],v239,s[48:51],s61 offen offset:3072 sc1
v_mfma_f32_16x16x32_bf16 v[96:99],v[204:207],a[216:219],v[96:99]
v_mfma_f32_16x16x32_bf16 v[216:219],v[200:203],a[196:199],v[216:219]
v_mfma_f32_16x16x32_bf16 v[216:219],v[204:207],a[220:223],v[216:219]
s_waitcnt lgkmcnt(4)
buffer_load_dwordx4 v[168:171],v252,s[48:51],s61 offen sc1
v_mfma_f32_16x16x32_bf16 v[52:55],v[208:211],a[160:163],v[52:55]
ds_read_b64_tr_b16 v[192:193],v234 offset:49664
ds_read_b64_tr_b16 v[194:195],v231 offset:16896
v_mfma_f32_16x16x32_bf16 v[52:55],v[212:215],a[200:203],v[52:55]
ds_read_b64_tr_b16 v[196:197],v234 offset:33280
ds_read_b64_tr_b16 v[198:199],v231 offset:49664
v_mfma_f32_16x16x32_bf16 v[60:63],v[208:211],a[164:167],v[60:63]
buffer_load_dwordx4 v[172:175],v252,s[48:51],s61 offen offset:1024 sc1
v_mfma_f32_16x16x32_bf16 v[60:63],v[212:215],a[204:207],v[60:63]
s_add_u32 s61,s61,0xfffdd800
s_add_u32 m0,s63,65536
v_mfma_f32_16x16x32_bf16 v[132:135],v[208:211],a[168:171],v[132:135]
v_mfma_f32_16x16x32_bf16 v[132:135],v[212:215],a[208:211],v[132:135]
buffer_load_dwordx4 v235,s[32:35],s62 offen lds
v_mfma_f32_16x16x32_bf16 v[140:143],v[208:211],a[172:175],v[140:143]
v_mfma_f32_16x16x32_bf16 v[140:143],v[212:215],a[212:215],v[140:143]
v_mfma_f32_16x16x32_bf16 v[144:147],v[208:211],a[192:195],v[144:147]
buffer_load_dwordx4 v236,s[32:35],s62 offen offset:1024 lds
v_mfma_f32_16x16x32_bf16 v[144:147],v[212:215],a[216:219],v[144:147]
v_mfma_f32_16x16x32_bf16 v[220:223],v[208:211],a[196:199],v[220:223]
v_mfma_f32_16x16x32_bf16 v[220:223],v[212:215],a[220:223],v[220:223]
s_waitcnt lgkmcnt(4)
buffer_load_dwordx4 v237,s[32:35],s62 offen offset:2048 lds
v_mfma_f32_16x16x32_bf16 a[128:131],v[184:187],a[160:163],a[128:131]
ds_read_b64_tr_b16 v[200:201],v233 offset:49920
ds_read_b64_tr_b16 v[202:203],v230 offset:17152
v_mfma_f32_16x16x32_bf16 a[128:131],v[188:191],a[200:203],a[128:131]
ds_read_b64_tr_b16 v[204:205],v233 offset:33536
ds_read_b64_tr_b16 v[206:207],v230 offset:49920
v_mfma_f32_16x16x32_bf16 a[136:139],v[184:187],a[164:167],a[136:139]
buffer_load_dwordx4 v238,s[32:35],s62 offen offset:3072 lds
v_mfma_f32_16x16x32_bf16 a[136:139],v[188:191],a[204:207],a[136:139]
s_add_u32 s62,s62,20480
v_mfma_f32_16x16x32_bf16 a[144:147],v[184:187],a[168:171],a[144:147]
v_mfma_f32_16x16x32_bf16 a[144:147],v[188:191],a[208:211],a[144:147]
v_mfma_f32_16x16x32_bf16 a[152:155],v[184:187],a[172:175],a[152:155]
v_mfma_f32_16x16x32_bf16 a[152:155],v[188:191],a[212:215],a[152:155]
v_mfma_f32_16x16x32_bf16 a[248:251],v[184:187],a[192:195],a[248:251]
v_mfma_f32_16x16x32_bf16 a[248:251],v[188:191],a[216:219],a[248:251]
v_mfma_f32_16x16x32_bf16 v[72:75],v[184:187],a[196:199],v[72:75]
v_mfma_f32_16x16x32_bf16 v[72:75],v[188:191],a[220:223],v[72:75]
s_waitcnt lgkmcnt(4)
v_mfma_f32_16x16x32_bf16 a[132:135],v[192:195],a[160:163],a[132:135]
ds_read_b64_tr_b16 v[208:209],v234 offset:49920
ds_read_b64_tr_b16 v[210:211],v231 offset:17152
v_mfma_f32_16x16x32_bf16 a[132:135],v[196:199],a[200:203],a[132:135]
ds_read_b64_tr_b16 v[212:213],v234 offset:33536
ds_read_b64_tr_b16 v[214:215],v231 offset:49920
v_mfma_f32_16x16x32_bf16 a[140:143],v[192:195],a[164:167],a[140:143]
v_mfma_f32_16x16x32_bf16 a[140:143],v[196:199],a[204:207],a[140:143]
v_mfma_f32_16x16x32_bf16 a[148:151],v[192:195],a[168:171],a[148:151]
v_mfma_f32_16x16x32_bf16 a[148:151],v[196:199],a[208:211],a[148:151]
v_mfma_f32_16x16x32_bf16 a[156:159],v[192:195],a[172:175],a[156:159]
v_mfma_f32_16x16x32_bf16 a[156:159],v[196:199],a[212:215],a[156:159]
v_mfma_f32_16x16x32_bf16 a[252:255],v[192:195],a[192:195],a[252:255]
v_mfma_f32_16x16x32_bf16 a[252:255],v[196:199],a[216:219],a[252:255]
s_waitcnt lgkmcnt(2)
v_mfma_f32_16x16x32_bf16 v[76:79],v[192:195],a[196:199],v[76:79]
s_waitcnt lgkmcnt(0)
s_add_u32 m0,s63,114688
v_mfma_f32_16x16x32_bf16 v[76:79],v[196:199],a[220:223],v[76:79]
s_waitcnt vmcnt(25)
buffer_load_dwordx4 v235,s[32:35],s62 offen lds
v_mfma_f32_16x16x32_bf16 a[0:3],v[200:203],a[160:163],a[0:3]
s_waitcnt vmcnt(18)
ds_read_b64_tr_b16 v[184:185],v226 offset:16384
ds_read_b64_tr_b16 v[186:187],v230
v_mfma_f32_16x16x32_bf16 a[0:3],v[204:207],a[200:203],a[0:3]
ds_read_b64_tr_b16 v[188:189],v233 offset:16384
ds_read_b64_tr_b16 v[190:191],v230 offset:32768
v_mfma_f32_16x16x32_bf16 a[32:35],v[200:203],a[164:167],a[32:35]
buffer_load_dwordx4 v236,s[32:35],s62 offen offset:1024 lds
buffer_load_dwordx4 v237,s[32:35],s62 offen offset:2048 lds
v_mfma_f32_16x16x32_bf16 a[32:35],v[204:207],a[204:207],a[32:35]
buffer_load_dwordx4 v238,s[32:35],s62 offen offset:3072 lds
v_mfma_f32_16x16x32_bf16 a[64:67],v[200:203],a[168:171],a[64:67]
s_add_u32 s62,s62,20480
s_add_u32 m0,s63,16384
v_mfma_f32_16x16x32_bf16 a[64:67],v[204:207],a[208:211],a[64:67]
buffer_load_dwordx4 v235,s[32:35],s62 offen lds
v_mfma_f32_16x16x32_bf16 a[96:99],v[200:203],a[172:175],a[96:99]
v_mfma_f32_16x16x32_bf16 a[96:99],v[204:207],a[212:215],a[96:99]
v_mfma_f32_16x16x32_bf16 a[8:11],v[200:203],a[192:195],a[8:11]
buffer_load_dwordx4 v236,s[32:35],s62 offen offset:1024 lds
v_mfma_f32_16x16x32_bf16 a[8:11],v[204:207],a[216:219],a[8:11]
v_mfma_f32_16x16x32_bf16 a[40:43],v[200:203],a[196:199],a[40:43]
v_mfma_f32_16x16x32_bf16 a[40:43],v[204:207],a[220:223],a[40:43]
buffer_load_dwordx4 v237,s[32:35],s62 offen offset:2048 lds
v_mfma_f32_16x16x32_bf16 a[4:7],v[208:211],a[160:163],a[4:7]
ds_read_b64_tr_b16 v[192:193],v232 offset:16384
ds_read_b64_tr_b16 v[194:195],v231
v_mfma_f32_16x16x32_bf16 a[4:7],v[212:215],a[200:203],a[4:7]
ds_read_b64_tr_b16 v[196:197],v234 offset:16384
ds_read_b64_tr_b16 v[198:199],v231 offset:32768
v_mfma_f32_16x16x32_bf16 a[36:39],v[208:211],a[164:167],a[36:39]
buffer_load_dwordx4 v238,s[32:35],s62 offen offset:3072 lds
v_mfma_f32_16x16x32_bf16 a[36:39],v[212:215],a[204:207],a[36:39]
s_add_u32 s62,s62,20480
s_add_u32 m0,s63,98304
v_mfma_f32_16x16x32_bf16 a[68:71],v[208:211],a[168:171],a[68:71]
v_mfma_f32_16x16x32_bf16 a[68:71],v[212:215],a[208:211],a[68:71]
buffer_load_dwordx4 v235,s[32:35],s62 offen lds
v_mfma_f32_16x16x32_bf16 a[100:103],v[208:211],a[172:175],a[100:103]
v_mfma_f32_16x16x32_bf16 a[100:103],v[212:215],a[212:215],a[100:103]
v_mfma_f32_16x16x32_bf16 a[12:15],v[208:211],a[192:195],a[12:15]
buffer_load_dwordx4 v236,s[32:35],s62 offen offset:1024 lds
v_mfma_f32_16x16x32_bf16 a[12:15],v[212:215],a[216:219],a[12:15]
v_mfma_f32_16x16x32_bf16 a[44:47],v[208:211],a[196:199],a[44:47]
v_mfma_f32_16x16x32_bf16 a[44:47],v[212:215],a[220:223],a[44:47]
s_waitcnt lgkmcnt(4)
buffer_load_dwordx4 v237,s[32:35],s62 offen offset:2048 lds
s_waitcnt vmcnt(27)
v_mfma_f32_16x16x32_bf16 v[0:3],v[184:187],a[224:227],v[0:3]
ds_read_b64_tr_b16 v[200:201],v226 offset:16640
ds_read_b64_tr_b16 v[202:203],v230 offset:256
s_waitcnt vmcnt(20)
v_mfma_f32_16x16x32_bf16 v[0:3],v[188:191],v[152:155],v[0:3]
ds_read_b64_tr_b16 v[204:205],v233 offset:16640
ds_read_b64_tr_b16 v[206:207],v230 offset:33024
v_mfma_f32_16x16x32_bf16 v[8:11],v[184:187],a[228:231],v[8:11]
buffer_load_dwordx4 v238,s[32:35],s62 offen offset:3072 lds
s_waitcnt vmcnt(20)
v_mfma_f32_16x16x32_bf16 v[8:11],v[188:191],v[156:159],v[8:11]
s_add_u32 s62,s62,20480
v_mfma_f32_16x16x32_bf16 v[16:19],v[184:187],a[232:235],v[16:19]
s_waitcnt vmcnt(19)
v_mfma_f32_16x16x32_bf16 v[16:19],v[188:191],v[160:163],v[16:19]
v_mfma_f32_16x16x32_bf16 v[24:27],v[184:187],a[236:239],v[24:27]
s_waitcnt vmcnt(18)
v_mfma_f32_16x16x32_bf16 v[24:27],v[188:191],v[164:167],v[24:27]
v_mfma_f32_16x16x32_bf16 v[32:35],v[184:187],a[240:243],v[32:35]
s_waitcnt vmcnt(17)
v_mfma_f32_16x16x32_bf16 v[32:35],v[188:191],v[168:171],v[32:35]
v_cmp_eq_u32 s[72:73],s59,v253
s_nop 0
s_bitcmp1_b32 s72,4
s_cbranch_scc0 .Lspb16
.Lsbb16:
v_mfma_f32_16x16x32_bf16 v[40:43],v[184:187],a[244:247],v[40:43]
buffer_load_dwordx4 v[176:179],v239,s[48:51],s61 offen sc1
buffer_load_dwordx4 v[180:183],v239,s[48:51],s61 offen offset:1024 sc1
s_waitcnt vmcnt(18)
v_mfma_f32_16x16x32_bf16 v[40:43],v[188:191],v[172:175],v[40:43]
s_waitcnt lgkmcnt(4)
buffer_load_dwordx4 a[176:179],v239,s[48:51],s61 offen offset:2048 sc1
v_mfma_f32_16x16x32_bf16 v[4:7],v[192:195],a[224:227],v[4:7]
ds_read_b64_tr_b16 v[208:209],v232 offset:16640
ds_read_b64_tr_b16 v[210:211],v231 offset:256
v_mfma_f32_16x16x32_bf16 v[4:7],v[196:199],v[152:155],v[4:7]
ds_read_b64_tr_b16 v[212:213],v234 offset:16640
ds_read_b64_tr_b16 v[214:215],v231 offset:33024
v_mfma_f32_16x16x32_bf16 v[12:15],v[192:195],a[228:231],v[12:15]
buffer_load_dwordx4 a[180:183],v239,s[48:51],s61 offen offset:3072 sc1
v_mfma_f32_16x16x32_bf16 v[12:15],v[196:199],v[156:159],v[12:15]
v_mfma_f32_16x16x32_bf16 v[20:23],v[192:195],a[232:235],v[20:23]
v_mfma_f32_16x16x32_bf16 v[20:23],v[196:199],v[160:163],v[20:23]
buffer_load_dwordx4 a[184:187],v252,s[48:51],s61 offen sc1
v_mfma_f32_16x16x32_bf16 v[28:31],v[192:195],a[236:239],v[28:31]
v_mfma_f32_16x16x32_bf16 v[28:31],v[196:199],v[164:167],v[28:31]
v_mfma_f32_16x16x32_bf16 v[36:39],v[192:195],a[240:243],v[36:39]
buffer_load_dwordx4 a[188:191],v252,s[48:51],s61 offen offset:1024 sc1
)A"
R"A(v_mfma_f32_16x16x32_bf16 v[36:39],v[196:199],v[168:171],v[36:39]
s_add_u32 s61,s61,6144
v_mfma_f32_16x16x32_bf16 v[44:47],v[192:195],a[244:247],v[44:47]
v_mfma_f32_16x16x32_bf16 v[44:47],v[196:199],v[172:175],v[44:47]
s_waitcnt lgkmcnt(4)
buffer_load_dwordx4 a[160:163],v239,s[48:51],s61 offen sc1
v_mfma_f32_16x16x32_bf16 v[48:51],v[200:203],a[224:227],v[48:51]
ds_read_b64_tr_b16 v[184:185],v226 offset:16896
ds_read_b64_tr_b16 v[186:187],v230 offset:512
v_mfma_f32_16x16x32_bf16 v[48:51],v[204:207],v[152:155],v[48:51]
ds_read_b64_tr_b16 v[188:189],v233 offset:16896
ds_read_b64_tr_b16 v[190:191],v230 offset:33280
v_mfma_f32_16x16x32_bf16 v[56:59],v[200:203],a[228:231],v[56:59]
buffer_load_dwordx4 a[164:167],v239,s[48:51],s61 offen offset:1024 sc1
v_mfma_f32_16x16x32_bf16 v[56:59],v[204:207],v[156:159],v[56:59]
v_mfma_f32_16x16x32_bf16 v[128:131],v[200:203],a[232:235],v[128:131]
v_mfma_f32_16x16x32_bf16 v[128:131],v[204:207],v[160:163],v[128:131]
buffer_load_dwordx4 a[168:171],v239,s[48:51],s61 offen offset:2048 sc1
v_mfma_f32_16x16x32_bf16 v[136:139],v[200:203],a[236:239],v[136:139]
v_mfma_f32_16x16x32_bf16 v[136:139],v[204:207],v[164:167],v[136:139]
v_mfma_f32_16x16x32_bf16 v[96:99],v[200:203],a[240:243],v[96:99]
buffer_load_dwordx4 a[172:175],v239,s[48:51],s61 offen offset:3072 sc1
v_mfma_f32_16x16x32_bf16 v[96:99],v[204:207],v[168:171],v[96:99]
v_mfma_f32_16x16x32_bf16 v[216:219],v[200:203],a[244:247],v[216:219]
v_mfma_f32_16x16x32_bf16 v[216:219],v[204:207],v[172:175],v[216:219]
s_waitcnt lgkmcnt(4)
buffer_load_dwordx4 a[192:195],v252,s[48:51],s61 offen sc1
v_mfma_f32_16x16x32_bf16 v[52:55],v[208:211],a[224:227],v[52:55]
ds_read_b64_tr_b16 v[192:193],v232 offset:16896
ds_read_b64_tr_b16 v[194:195],v231 offset:512
v_mfma_f32_16x16x32_bf16 v[52:55],v[212:215],v[152:155],v[52:55]
ds_read_b64_tr_b16 v[196:197],v234 offset:16896
ds_read_b64_tr_b16 v[198:199],v231 offset:33280
v_mfma_f32_16x16x32_bf16 v[60:63],v[208:211],a[228:231],v[60:63]
buffer_load_dwordx4 a[196:199],v252,s[48:51],s61 offen offset:1024 sc1
v_mfma_f32_16x16x32_bf16 v[60:63],v[212:215],v[156:159],v[60:63]
s_add_u32 s61,s61,6144
s_add_u32 m0,s63,49152
v_mfma_f32_16x16x32_bf16 v[132:135],v[208:211],a[232:235],v[132:135]
v_mfma_f32_16x16x32_bf16 v[132:135],v[212:215],v[160:163],v[132:135]
buffer_load_dwordx4 v235,s[32:35],s62 offen lds
v_mfma_f32_16x16x32_bf16 v[140:143],v[208:211],a[236:239],v[140:143]
v_mfma_f32_16x16x32_bf16 v[140:143],v[212:215],v[164:167],v[140:143]
v_mfma_f32_16x16x32_bf16 v[144:147],v[208:211],a[240:243],v[144:147]
buffer_load_dwordx4 v236,s[32:35],s62 offen offset:1024 lds
v_mfma_f32_16x16x32_bf16 v[144:147],v[212:215],v[168:171],v[144:147]
v_mfma_f32_16x16x32_bf16 v[220:223],v[208:211],a[244:247],v[220:223]
v_mfma_f32_16x16x32_bf16 v[220:223],v[212:215],v[172:175],v[220:223]
s_waitcnt lgkmcnt(4)
buffer_load_dwordx4 v237,s[32:35],s62 offen offset:2048 lds
v_mfma_f32_16x16x32_bf16 a[128:131],v[184:187],a[224:227],a[128:131]
ds_read_b64_tr_b16 v[200:201],v226 offset:17152
ds_read_b64_tr_b16 v[202:203],v230 offset:768
v_mfma_f32_16x16x32_bf16 a[128:131],v[188:191],v[152:155],a[128:131]
ds_read_b64_tr_b16 v[204:205],v233 offset:17152
ds_read_b64_tr_b16 v[206:207],v230 offset:33536
v_mfma_f32_16x16x32_bf16 a[136:139],v[184:187],a[228:231],a[136:139]
buffer_load_dwordx4 v238,s[32:35],s62 offen offset:3072 lds
v_mfma_f32_16x16x32_bf16 a[136:139],v[188:191],v[156:159],a[136:139]
s_add_u32 s62,s62,20480
v_mfma_f32_16x16x32_bf16 a[144:147],v[184:187],a[232:235],a[144:147]
v_mfma_f32_16x16x32_bf16 a[144:147],v[188:191],v[160:163],a[144:147]
v_mfma_f32_16x16x32_bf16 a[152:155],v[184:187],a[236:239],a[152:155]
v_mfma_f32_16x16x32_bf16 a[152:155],v[188:191],v[164:167],a[152:155]
v_mfma_f32_16x16x32_bf16 a[248:251],v[184:187],a[240:243],a[248:251]
v_mfma_f32_16x16x32_bf16 a[248:251],v[188:191],v[168:171],a[248:251]
v_mfma_f32_16x16x32_bf16 v[72:75],v[184:187],a[244:247],v[72:75]
v_mfma_f32_16x16x32_bf16 v[72:75],v[188:191],v[172:175],v[72:75]
s_waitcnt lgkmcnt(4)
v_mfma_f32_16x16x32_bf16 a[132:135],v[192:195],a[224:227],a[132:135]
ds_read_b64_tr_b16 v[208:209],v232 offset:17152
ds_read_b64_tr_b16 v[210:211],v231 offset:768
v_mfma_f32_16x16x32_bf16 a[132:135],v[196:199],v[152:155],a[132:135]
ds_read_b64_tr_b16 v[212:213],v234 offset:17152
ds_read_b64_tr_b16 v[214:215],v231 offset:33536
v_mfma_f32_16x16x32_bf16 a[140:143],v[192:195],a[228:231],a[140:143]
v_mfma_f32_16x16x32_bf16 a[140:143],v[196:199],v[156:159],a[140:143]
v_mfma_f32_16x16x32_bf16 a[148:151],v[192:195],a[232:235],a[148:151]
v_mfma_f32_16x16x32_bf16 a[148:151],v[196:199],v[160:163],a[148:151]
v_mfma_f32_16x16x32_bf16 a[156:159],v[192:195],a[236:239],a[156:159]
v_mfma_f32_16x16x32_bf16 a[156:159],v[196:199],v[164:167],a[156:159]
v_mfma_f32_16x16x32_bf16 a[252:255],v[192:195],a[240:243],a[252:255]
v_mfma_f32_16x16x32_bf16 a[252:255],v[196:199],v[168:171],a[252:255]
s_waitcnt lgkmcnt(2)
v_mfma_f32_16x16x32_bf16 v[76:79],v[192:195],a[244:247],v[76:79]
s_waitcnt lgkmcnt(0)
s_add_u32 m0,s63,147456
v_mfma_f32_16x16x32_bf16 v[76:79],v[196:199],v[172:175],v[76:79]
s_waitcnt vmcnt(24)
buffer_load_dwordx4 v235,s[32:35],s62 offen lds
v_mfma_f32_16x16x32_bf16 a[0:3],v[200:203],a[224:227],a[0:3]
s_waitcnt vmcnt(17)
ds_read_b64_tr_b16 v[184:185],v233
ds_read_b64_tr_b16 v[186:187],v233 offset:49152
v_mfma_f32_16x16x32_bf16 a[0:3],v[204:207],v[152:155],a[0:3]
ds_read_b64_tr_b16 v[188:189],v230 offset:16384
ds_read_b64_tr_b16 v[190:191],v233 offset:32768
v_mfma_f32_16x16x32_bf16 a[32:35],v[200:203],a[228:231],a[32:35]
buffer_load_dwordx4 v236,s[32:35],s62 offen offset:1024 lds
buffer_load_dwordx4 v237,s[32:35],s62 offen offset:2048 lds
v_mfma_f32_16x16x32_bf16 a[32:35],v[204:207],v[156:159],a[32:35]
buffer_load_dwordx4 v238,s[32:35],s62 offen offset:3072 lds
v_mfma_f32_16x16x32_bf16 a[64:67],v[200:203],a[232:235],a[64:67]
s_add_u32 s62,s62,20480
s_mov_b32 m0,s63
v_mfma_f32_16x16x32_bf16 a[64:67],v[204:207],v[160:163],a[64:67]
buffer_load_dwordx4 v235,s[32:35],s62 offen lds
v_mfma_f32_16x16x32_bf16 a[96:99],v[200:203],a[236:239],a[96:99]
v_mfma_f32_16x16x32_bf16 a[96:99],v[204:207],v[164:167],a[96:99]
v_mfma_f32_16x16x32_bf16 a[8:11],v[200:203],a[240:243],a[8:11]
buffer_load_dwordx4 v236,s[32:35],s62 offen offset:1024 lds
v_mfma_f32_16x16x32_bf16 a[8:11],v[204:207],v[168:171],a[8:11]
v_mfma_f32_16x16x32_bf16 a[40:43],v[200:203],a[244:247],a[40:43]
v_mfma_f32_16x16x32_bf16 a[40:43],v[204:207],v[172:175],a[40:43]
buffer_load_dwordx4 v237,s[32:35],s62 offen offset:2048 lds
v_mfma_f32_16x16x32_bf16 a[4:7],v[208:211],a[224:227],a[4:7]
ds_read_b64_tr_b16 v[192:193],v234
ds_read_b64_tr_b16 v[194:195],v234 offset:49152
v_mfma_f32_16x16x32_bf16 a[4:7],v[212:215],v[152:155],a[4:7]
ds_read_b64_tr_b16 v[196:197],v231 offset:16384
ds_read_b64_tr_b16 v[198:199],v234 offset:32768
v_mfma_f32_16x16x32_bf16 a[36:39],v[208:211],a[228:231],a[36:39]
buffer_load_dwordx4 v238,s[32:35],s62 offen offset:3072 lds
v_mfma_f32_16x16x32_bf16 a[36:39],v[212:215],v[156:159],a[36:39]
s_add_u32 s62,s62,20480
s_add_u32 m0,s63,81920
v_mfma_f32_16x16x32_bf16 a[68:71],v[208:211],a[232:235],a[68:71]
v_mfma_f32_16x16x32_bf16 a[68:71],v[212:215],v[160:163],a[68:71]
buffer_load_dwordx4 v235,s[32:35],s62 offen lds
v_mfma_f32_16x16x32_bf16 a[100:103],v[208:211],a[236:239],a[100:103]
v_mfma_f32_16x16x32_bf16 a[100:103],v[212:215],v[164:167],a[100:103]
v_mfma_f32_16x16x32_bf16 a[12:15],v[208:211],a[240:243],a[12:15]
buffer_load_dwordx4 v236,s[32:35],s62 offen offset:1024 lds
v_mfma_f32_16x16x32_bf16 a[12:15],v[212:215],v[168:171],a[12:15]
v_mfma_f32_16x16x32_bf16 a[44:47],v[208:211],a[244:247],a[44:47]
v_mfma_f32_16x16x32_bf16 a[44:47],v[212:215],v[172:175],a[44:47]
s_waitcnt lgkmcnt(4)
buffer_load_dwordx4 v237,s[32:35],s62 offen offset:2048 lds
s_waitcnt vmcnt(26)
v_mfma_f32_16x16x32_bf16 v[0:3],v[184:187],v[176:179],v[0:3]
ds_read_b64_tr_b16 v[200:201],v233 offset:256
ds_read_b64_tr_b16 v[202:203],v233 offset:49408
s_waitcnt vmcnt(20)
v_mfma_f32_16x16x32_bf16 v[0:3],v[188:191],a[160:163],v[0:3]
ds_read_b64_tr_b16 v[204:205],v230 offset:16640
ds_read_b64_tr_b16 v[206:207],v233 offset:33024
v_mfma_f32_16x16x32_bf16 v[8:11],v[184:187],v[180:183],v[8:11]
buffer_load_dwordx4 v238,s[32:35],s62 offen offset:3072 lds
s_waitcnt vmcnt(20)
v_mfma_f32_16x16x32_bf16 v[8:11],v[188:191],a[164:167],v[8:11]
s_add_u32 s62,s62,184320
v_mfma_f32_16x16x32_bf16 v[16:19],v[184:187],a[176:179],v[16:19]
s_waitcnt vmcnt(19)
v_mfma_f32_16x16x32_bf16 v[16:19],v[188:191],a[168:171],v[16:19]
buffer_load_dwordx4 a[200:203],v239,s[48:51],s61 offen sc1
v_mfma_f32_16x16x32_bf16 v[24:27],v[184:187],a[180:183],v[24:27]
s_waitcnt vmcnt(19)
v_mfma_f32_16x16x32_bf16 v[24:27],v[188:191],a[172:175],v[24:27]
v_mfma_f32_16x16x32_bf16 v[32:35],v[184:187],a[184:187],v[32:35]
buffer_load_dwordx4 a[204:207],v239,s[48:51],s61 offen offset:1024 sc1
s_waitcnt vmcnt(19)
v_mfma_f32_16x16x32_bf16 v[32:35],v[188:191],a[192:195],v[32:35]
v_mfma_f32_16x16x32_bf16 v[40:43],v[184:187],a[188:191],v[40:43]
s_waitcnt vmcnt(18)
v_mfma_f32_16x16x32_bf16 v[40:43],v[188:191],a[196:199],v[40:43]
s_waitcnt lgkmcnt(4)
buffer_load_dwordx4 a[208:211],v239,s[48:51],s61 offen offset:2048 sc1
v_mfma_f32_16x16x32_bf16 v[4:7],v[192:195],v[176:179],v[4:7]
ds_read_b64_tr_b16 v[208:209],v234 offset:256
ds_read_b64_tr_b16 v[210:211],v234 offset:49408
v_mfma_f32_16x16x32_bf16 v[4:7],v[196:199],a[160:163],v[4:7]
ds_read_b64_tr_b16 v[212:213],v231 offset:16640
ds_read_b64_tr_b16 v[214:215],v234 offset:33024
v_mfma_f32_16x16x32_bf16 v[12:15],v[192:195],v[180:183],v[12:15]
buffer_load_dwordx4 a[212:215],v239,s[48:51],s61 offen offset:3072 sc1
v_mfma_f32_16x16x32_bf16 v[12:15],v[196:199],a[164:167],v[12:15]
v_mfma_f32_16x16x32_bf16 v[20:23],v[192:195],a[176:179],v[20:23]
v_mfma_f32_16x16x32_bf16 v[20:23],v[196:199],a[168:171],v[20:23]
buffer_load_dwordx4 a[216:219],v252,s[48:51],s61 offen sc1
v_mfma_f32_16x16x32_bf16 v[28:31],v[192:195],a[180:183],v[28:31]
v_mfma_f32_16x16x32_bf16 v[28:31],v[196:199],a[172:175],v[28:31]
v_mfma_f32_16x16x32_bf16 v[36:39],v[192:195],a[184:187],v[36:39]
buffer_load_dwordx4 a[220:223],v252,s[48:51],s61 offen offset:1024 sc1
v_mfma_f32_16x16x32_bf16 v[36:39],v[196:199],a[192:195],v[36:39]
s_add_u32 s61,s61,6144
v_and_b32 v255,0x380,v251
v_mfma_f32_16x16x32_bf16 v[44:47],v[192:195],a[188:191],v[44:47]
buffer_load_dword v253,v255,s[40:43],s69 offen sc1
v_mfma_f32_16x16x32_bf16 v[44:47],v[196:199],a[196:199],v[44:47]
s_waitcnt lgkmcnt(4)
buffer_load_dwordx4 a[224:227],v239,s[48:51],s61 offen sc1
v_mfma_f32_16x16x32_bf16 v[48:51],v[200:203],v[176:179],v[48:51]
ds_read_b64_tr_b16 v[184:185],v233 offset:512
ds_read_b64_tr_b16 v[186:187],v233 offset:49664
v_mfma_f32_16x16x32_bf16 v[48:51],v[204:207],a[160:163],v[48:51]
ds_read_b64_tr_b16 v[188:189],v230 offset:16896
ds_read_b64_tr_b16 v[190:191],v233 offset:33280
v_mfma_f32_16x16x32_bf16 v[56:59],v[200:203],v[180:183],v[56:59]
buffer_load_dwordx4 a[228:231],v239,s[48:51],s61 offen offset:1024 sc1
v_mfma_f32_16x16x32_bf16 v[56:59],v[204:207],a[164:167],v[56:59]
v_mfma_f32_16x16x32_bf16 v[128:131],v[200:203],a[176:179],v[128:131]
v_mfma_f32_16x16x32_bf16 v[128:131],v[204:207],a[168:171],v[128:131]
buffer_load_dwordx4 a[232:235],v239,s[48:51],s61 offen offset:2048 sc1
v_mfma_f32_16x16x32_bf16 v[136:139],v[200:203],a[180:183],v[136:139]
v_mfma_f32_16x16x32_bf16 v[136:139],v[204:207],a[172:175],v[136:139]
v_mfma_f32_16x16x32_bf16 v[96:99],v[200:203],a[184:187],v[96:99]
buffer_load_dwordx4 a[236:239],v239,s[48:51],s61 offen offset:3072 sc1
v_mfma_f32_16x16x32_bf16 v[96:99],v[204:207],a[192:195],v[96:99]
v_mfma_f32_16x16x32_bf16 v[216:219],v[200:203],a[188:191],v[216:219]
v_mfma_f32_16x16x32_bf16 v[216:219],v[204:207],a[196:199],v[216:219]
s_waitcnt lgkmcnt(4)
buffer_load_dwordx4 a[240:243],v252,s[48:51],s61 offen sc1
v_mfma_f32_16x16x32_bf16 v[52:55],v[208:211],v[176:179],v[52:55]
ds_read_b64_tr_b16 v[192:193],v234 offset:512
ds_read_b64_tr_b16 v[194:195],v234 offset:49664
v_mfma_f32_16x16x32_bf16 v[52:55],v[212:215],a[160:163],v[52:55]
)A"
R"A(ds_read_b64_tr_b16 v[196:197],v231 offset:16896
ds_read_b64_tr_b16 v[198:199],v234 offset:33280
v_mfma_f32_16x16x32_bf16 v[60:63],v[208:211],v[180:183],v[60:63]
buffer_load_dwordx4 a[244:247],v252,s[48:51],s61 offen offset:1024 sc1
v_mfma_f32_16x16x32_bf16 v[60:63],v[212:215],a[164:167],v[60:63]
s_add_u32 s61,s61,30720
s_add_u32 m0,s63,32768
v_mfma_f32_16x16x32_bf16 v[132:135],v[208:211],a[176:179],v[132:135]
v_mfma_f32_16x16x32_bf16 v[132:135],v[212:215],a[168:171],v[132:135]
buffer_load_dwordx4 v235,s[32:35],s62 offen lds
v_mfma_f32_16x16x32_bf16 v[140:143],v[208:211],a[180:183],v[140:143]
v_mfma_f32_16x16x32_bf16 v[140:143],v[212:215],a[172:175],v[140:143]
v_mfma_f32_16x16x32_bf16 v[144:147],v[208:211],a[184:187],v[144:147]
buffer_load_dwordx4 v236,s[32:35],s62 offen offset:1024 lds
v_mfma_f32_16x16x32_bf16 v[144:147],v[212:215],a[192:195],v[144:147]
v_mfma_f32_16x16x32_bf16 v[220:223],v[208:211],a[188:191],v[220:223]
v_mfma_f32_16x16x32_bf16 v[220:223],v[212:215],a[196:199],v[220:223]
s_waitcnt lgkmcnt(4)
buffer_load_dwordx4 v237,s[32:35],s62 offen offset:2048 lds
v_mfma_f32_16x16x32_bf16 a[128:131],v[184:187],v[176:179],a[128:131]
ds_read_b64_tr_b16 v[200:201],v233 offset:768
ds_read_b64_tr_b16 v[202:203],v233 offset:49920
v_mfma_f32_16x16x32_bf16 a[128:131],v[188:191],a[160:163],a[128:131]
ds_read_b64_tr_b16 v[204:205],v230 offset:17152
ds_read_b64_tr_b16 v[206:207],v233 offset:33536
v_mfma_f32_16x16x32_bf16 a[136:139],v[184:187],v[180:183],a[136:139]
buffer_load_dwordx4 v238,s[32:35],s62 offen offset:3072 lds
v_mfma_f32_16x16x32_bf16 a[136:139],v[188:191],a[164:167],a[136:139]
s_add_u32 s62,s62,20480
v_mfma_f32_16x16x32_bf16 a[144:147],v[184:187],a[176:179],a[144:147]
v_mfma_f32_16x16x32_bf16 a[144:147],v[188:191],a[168:171],a[144:147]
v_mfma_f32_16x16x32_bf16 a[152:155],v[184:187],a[180:183],a[152:155]
v_mfma_f32_16x16x32_bf16 a[152:155],v[188:191],a[172:175],a[152:155]
v_mfma_f32_16x16x32_bf16 a[248:251],v[184:187],a[184:187],a[248:251]
v_mfma_f32_16x16x32_bf16 a[248:251],v[188:191],a[192:195],a[248:251]
v_mfma_f32_16x16x32_bf16 v[72:75],v[184:187],a[188:191],v[72:75]
v_mfma_f32_16x16x32_bf16 v[72:75],v[188:191],a[196:199],v[72:75]
s_waitcnt lgkmcnt(4)
v_mfma_f32_16x16x32_bf16 a[132:135],v[192:195],v[176:179],a[132:135]
ds_read_b64_tr_b16 v[208:209],v234 offset:768
ds_read_b64_tr_b16 v[210:211],v234 offset:49920
v_mfma_f32_16x16x32_bf16 a[132:135],v[196:199],a[160:163],a[132:135]
ds_read_b64_tr_b16 v[212:213],v231 offset:17152
ds_read_b64_tr_b16 v[214:215],v234 offset:33536
v_mfma_f32_16x16x32_bf16 a[140:143],v[192:195],v[180:183],a[140:143]
v_mfma_f32_16x16x32_bf16 a[140:143],v[196:199],a[164:167],a[140:143]
v_mfma_f32_16x16x32_bf16 a[148:151],v[192:195],a[176:179],a[148:151]
v_mfma_f32_16x16x32_bf16 a[148:151],v[196:199],a[168:171],a[148:151]
v_mfma_f32_16x16x32_bf16 a[156:159],v[192:195],a[180:183],a[156:159]
v_mfma_f32_16x16x32_bf16 a[156:159],v[196:199],a[172:175],a[156:159]
v_mfma_f32_16x16x32_bf16 a[252:255],v[192:195],a[184:187],a[252:255]
v_mfma_f32_16x16x32_bf16 a[252:255],v[196:199],a[192:195],a[252:255]
s_waitcnt lgkmcnt(2)
v_mfma_f32_16x16x32_bf16 v[76:79],v[192:195],a[188:191],v[76:79]
s_waitcnt lgkmcnt(0)
s_add_u32 m0,s63,65536
v_mfma_f32_16x16x32_bf16 v[76:79],v[196:199],a[196:199],v[76:79]
s_waitcnt vmcnt(25)
buffer_load_dwordx4 v235,s[32:35],s62 offen lds
v_mfma_f32_16x16x32_bf16 a[0:3],v[200:203],v[176:179],a[0:3]
s_waitcnt vmcnt(18)
ds_read_b64_tr_b16 v[184:185],v230 offset:49152
ds_read_b64_tr_b16 v[186:187],v226 offset:16384
v_mfma_f32_16x16x32_bf16 a[0:3],v[204:207],a[160:163],a[0:3]
ds_read_b64_tr_b16 v[188:189],v230
ds_read_b64_tr_b16 v[190:191],v233 offset:16384
v_mfma_f32_16x16x32_bf16 a[32:35],v[200:203],v[180:183],a[32:35]
buffer_load_dwordx4 v236,s[32:35],s62 offen offset:1024 lds
buffer_load_dwordx4 v237,s[32:35],s62 offen offset:2048 lds
v_mfma_f32_16x16x32_bf16 a[32:35],v[204:207],a[164:167],a[32:35]
buffer_load_dwordx4 v238,s[32:35],s62 offen offset:3072 lds
v_mfma_f32_16x16x32_bf16 a[64:67],v[200:203],a[176:179],a[64:67]
s_add_u32 s62,s62,20480
s_add_u32 m0,s63,114688
v_mfma_f32_16x16x32_bf16 a[64:67],v[204:207],a[168:171],a[64:67]
buffer_load_dwordx4 v235,s[32:35],s62 offen lds
v_mfma_f32_16x16x32_bf16 a[96:99],v[200:203],a[180:183],a[96:99]
v_mfma_f32_16x16x32_bf16 a[96:99],v[204:207],a[172:175],a[96:99]
v_mfma_f32_16x16x32_bf16 a[8:11],v[200:203],a[184:187],a[8:11]
buffer_load_dwordx4 v236,s[32:35],s62 offen offset:1024 lds
v_mfma_f32_16x16x32_bf16 a[8:11],v[204:207],a[192:195],a[8:11]
v_mfma_f32_16x16x32_bf16 a[40:43],v[200:203],a[188:191],a[40:43]
v_mfma_f32_16x16x32_bf16 a[40:43],v[204:207],a[196:199],a[40:43]
buffer_load_dwordx4 v237,s[32:35],s62 offen offset:2048 lds
v_mfma_f32_16x16x32_bf16 a[4:7],v[208:211],v[176:179],a[4:7]
ds_read_b64_tr_b16 v[192:193],v231 offset:49152
ds_read_b64_tr_b16 v[194:195],v232 offset:16384
v_mfma_f32_16x16x32_bf16 a[4:7],v[212:215],a[160:163],a[4:7]
ds_read_b64_tr_b16 v[196:197],v231
ds_read_b64_tr_b16 v[198:199],v234 offset:16384
v_mfma_f32_16x16x32_bf16 a[36:39],v[208:211],v[180:183],a[36:39]
buffer_load_dwordx4 v238,s[32:35],s62 offen offset:3072 lds
v_mfma_f32_16x16x32_bf16 a[36:39],v[212:215],a[164:167],a[36:39]
s_add_u32 s62,s62,20480
s_add_u32 m0,s63,16384
v_mfma_f32_16x16x32_bf16 a[68:71],v[208:211],a[176:179],a[68:71]
v_mfma_f32_16x16x32_bf16 a[68:71],v[212:215],a[168:171],a[68:71]
buffer_load_dwordx4 v235,s[32:35],s62 offen lds
v_mfma_f32_16x16x32_bf16 a[100:103],v[208:211],a[180:183],a[100:103]
v_mfma_f32_16x16x32_bf16 a[100:103],v[212:215],a[172:175],a[100:103]
v_mfma_f32_16x16x32_bf16 a[12:15],v[208:211],a[184:187],a[12:15]
buffer_load_dwordx4 v236,s[32:35],s62 offen offset:1024 lds
v_mfma_f32_16x16x32_bf16 a[12:15],v[212:215],a[192:195],a[12:15]
v_mfma_f32_16x16x32_bf16 a[44:47],v[208:211],a[188:191],a[44:47]
v_mfma_f32_16x16x32_bf16 a[44:47],v[212:215],a[196:199],a[44:47]
s_waitcnt lgkmcnt(4)
buffer_load_dwordx4 v237,s[32:35],s62 offen offset:2048 lds
s_waitcnt vmcnt(27)
v_mfma_f32_16x16x32_bf16 v[0:3],v[184:187],a[200:203],v[0:3]
ds_read_b64_tr_b16 v[200:201],v230 offset:49408
ds_read_b64_tr_b16 v[202:203],v226 offset:16640
s_waitcnt vmcnt(20)
v_mfma_f32_16x16x32_bf16 v[0:3],v[188:191],a[224:227],v[0:3]
ds_read_b64_tr_b16 v[204:205],v230 offset:256
ds_read_b64_tr_b16 v[206:207],v233 offset:16640
v_mfma_f32_16x16x32_bf16 v[8:11],v[184:187],a[204:207],v[8:11]
buffer_load_dwordx4 v238,s[32:35],s62 offen offset:3072 lds
s_waitcnt vmcnt(20)
v_mfma_f32_16x16x32_bf16 v[8:11],v[188:191],a[228:231],v[8:11]
s_add_u32 s62,s62,20480
v_mfma_f32_16x16x32_bf16 v[16:19],v[184:187],a[208:211],v[16:19]
s_waitcnt vmcnt(19)
v_mfma_f32_16x16x32_bf16 v[16:19],v[188:191],a[232:235],v[16:19]
v_mfma_f32_16x16x32_bf16 v[24:27],v[184:187],a[212:215],v[24:27]
s_waitcnt vmcnt(18)
v_mfma_f32_16x16x32_bf16 v[24:27],v[188:191],a[236:239],v[24:27]
v_mfma_f32_16x16x32_bf16 v[32:35],v[184:187],a[216:219],v[32:35]
s_waitcnt vmcnt(17)
v_mfma_f32_16x16x32_bf16 v[32:35],v[188:191],a[240:243],v[32:35]
v_cmp_eq_u32 s[72:73],s59,v253
s_nop 0
s_bitcmp1_b32 s72,5
s_cbranch_scc0 .Lspb20
.Lsbb20:
v_mfma_f32_16x16x32_bf16 v[40:43],v[184:187],a[220:223],v[40:43]
buffer_load_dwordx4 v[152:155],v239,s[48:51],s61 offen sc1
buffer_load_dwordx4 v[156:159],v239,s[48:51],s61 offen offset:1024 sc1
s_waitcnt vmcnt(18)
v_mfma_f32_16x16x32_bf16 v[40:43],v[188:191],a[244:247],v[40:43]
s_waitcnt lgkmcnt(4)
buffer_load_dwordx4 v[160:163],v239,s[48:51],s61 offen offset:2048 sc1
v_mfma_f32_16x16x32_bf16 v[4:7],v[192:195],a[200:203],v[4:7]
ds_read_b64_tr_b16 v[208:209],v231 offset:49408
ds_read_b64_tr_b16 v[210:211],v232 offset:16640
v_mfma_f32_16x16x32_bf16 v[4:7],v[196:199],a[224:227],v[4:7]
ds_read_b64_tr_b16 v[212:213],v231 offset:256
ds_read_b64_tr_b16 v[214:215],v234 offset:16640
v_mfma_f32_16x16x32_bf16 v[12:15],v[192:195],a[204:207],v[12:15]
buffer_load_dwordx4 v[164:167],v239,s[48:51],s61 offen offset:3072 sc1
v_mfma_f32_16x16x32_bf16 v[12:15],v[196:199],a[228:231],v[12:15]
v_mfma_f32_16x16x32_bf16 v[20:23],v[192:195],a[208:211],v[20:23]
v_mfma_f32_16x16x32_bf16 v[20:23],v[196:199],a[232:235],v[20:23]
buffer_load_dwordx4 v[168:171],v252,s[48:51],s61 offen sc1
v_mfma_f32_16x16x32_bf16 v[28:31],v[192:195],a[212:215],v[28:31]
v_mfma_f32_16x16x32_bf16 v[28:31],v[196:199],a[236:239],v[28:31]
v_mfma_f32_16x16x32_bf16 v[36:39],v[192:195],a[216:219],v[36:39]
buffer_load_dwordx4 v[172:175],v252,s[48:51],s61 offen offset:1024 sc1
v_mfma_f32_16x16x32_bf16 v[36:39],v[196:199],a[240:243],v[36:39]
s_add_u32 s61,s61,6144
v_mfma_f32_16x16x32_bf16 v[44:47],v[192:195],a[220:223],v[44:47]
v_mfma_f32_16x16x32_bf16 v[44:47],v[196:199],a[244:247],v[44:47]
s_waitcnt lgkmcnt(4)
buffer_load_dwordx4 v[176:179],v239,s[48:51],s61 offen sc1
v_mfma_f32_16x16x32_bf16 v[48:51],v[200:203],a[200:203],v[48:51]
ds_read_b64_tr_b16 v[184:185],v230 offset:49664
ds_read_b64_tr_b16 v[186:187],v226 offset:16896
v_mfma_f32_16x16x32_bf16 v[48:51],v[204:207],a[224:227],v[48:51]
ds_read_b64_tr_b16 v[188:189],v230 offset:512
ds_read_b64_tr_b16 v[190:191],v233 offset:16896
v_mfma_f32_16x16x32_bf16 v[56:59],v[200:203],a[204:207],v[56:59]
buffer_load_dwordx4 v[180:183],v239,s[48:51],s61 offen offset:1024 sc1
v_mfma_f32_16x16x32_bf16 v[56:59],v[204:207],a[228:231],v[56:59]
v_mfma_f32_16x16x32_bf16 v[128:131],v[200:203],a[208:211],v[128:131]
v_mfma_f32_16x16x32_bf16 v[128:131],v[204:207],a[232:235],v[128:131]
buffer_load_dwordx4 a[176:179],v239,s[48:51],s61 offen offset:2048 sc1
v_mfma_f32_16x16x32_bf16 v[136:139],v[200:203],a[212:215],v[136:139]
v_mfma_f32_16x16x32_bf16 v[136:139],v[204:207],a[236:239],v[136:139]
v_mfma_f32_16x16x32_bf16 v[96:99],v[200:203],a[216:219],v[96:99]
buffer_load_dwordx4 a[180:183],v239,s[48:51],s61 offen offset:3072 sc1
v_mfma_f32_16x16x32_bf16 v[96:99],v[204:207],a[240:243],v[96:99]
v_mfma_f32_16x16x32_bf16 v[216:219],v[200:203],a[220:223],v[216:219]
v_mfma_f32_16x16x32_bf16 v[216:219],v[204:207],a[244:247],v[216:219]
s_waitcnt lgkmcnt(4)
buffer_load_dwordx4 a[184:187],v252,s[48:51],s61 offen sc1
v_mfma_f32_16x16x32_bf16 v[52:55],v[208:211],a[200:203],v[52:55]
ds_read_b64_tr_b16 v[192:193],v231 offset:49664
ds_read_b64_tr_b16 v[194:195],v232 offset:16896
v_mfma_f32_16x16x32_bf16 v[52:55],v[212:215],a[224:227],v[52:55]
ds_read_b64_tr_b16 v[196:197],v231 offset:512
ds_read_b64_tr_b16 v[198:199],v234 offset:16896
v_mfma_f32_16x16x32_bf16 v[60:63],v[208:211],a[204:207],v[60:63]
buffer_load_dwordx4 a[188:191],v252,s[48:51],s61 offen offset:1024 sc1
v_mfma_f32_16x16x32_bf16 v[60:63],v[212:215],a[228:231],v[60:63]
s_add_u32 s61,s61,6144
s_add_u32 m0,s63,98304
v_mfma_f32_16x16x32_bf16 v[132:135],v[208:211],a[208:211],v[132:135]
v_mfma_f32_16x16x32_bf16 v[132:135],v[212:215],a[232:235],v[132:135]
buffer_load_dwordx4 v235,s[32:35],s62 offen lds
v_mfma_f32_16x16x32_bf16 v[140:143],v[208:211],a[212:215],v[140:143]
v_mfma_f32_16x16x32_bf16 v[140:143],v[212:215],a[236:239],v[140:143]
v_mfma_f32_16x16x32_bf16 v[144:147],v[208:211],a[216:219],v[144:147]
buffer_load_dwordx4 v236,s[32:35],s62 offen offset:1024 lds
v_mfma_f32_16x16x32_bf16 v[144:147],v[212:215],a[240:243],v[144:147]
v_mfma_f32_16x16x32_bf16 v[220:223],v[208:211],a[220:223],v[220:223]
v_mfma_f32_16x16x32_bf16 v[220:223],v[212:215],a[244:247],v[220:223]
s_waitcnt lgkmcnt(4)
buffer_load_dwordx4 v237,s[32:35],s62 offen offset:2048 lds
v_mfma_f32_16x16x32_bf16 a[128:131],v[184:187],a[200:203],a[128:131]
ds_read_b64_tr_b16 v[200:201],v230 offset:49920
ds_read_b64_tr_b16 v[202:203],v226 offset:17152
v_mfma_f32_16x16x32_bf16 a[128:131],v[188:191],a[224:227],a[128:131]
ds_read_b64_tr_b16 v[204:205],v230 offset:768
ds_read_b64_tr_b16 v[206:207],v233 offset:17152
v_mfma_f32_16x16x32_bf16 a[136:139],v[184:187],a[204:207],a[136:139]
buffer_load_dwordx4 v238,s[32:35],s62 offen offset:3072 lds
v_mfma_f32_16x16x32_bf16 a[136:139],v[188:191],a[228:231],a[136:139]
s_add_u32 s62,s62,20480
v_mfma_f32_16x16x32_bf16 a[144:147],v[184:187],a[208:211],a[144:147]
v_mfma_f32_16x16x32_bf16 a[144:147],v[188:191],a[232:235],a[144:147]
)A"
R"A(v_mfma_f32_16x16x32_bf16 a[152:155],v[184:187],a[212:215],a[152:155]
v_mfma_f32_16x16x32_bf16 a[152:155],v[188:191],a[236:239],a[152:155]
v_mfma_f32_16x16x32_bf16 a[248:251],v[184:187],a[216:219],a[248:251]
v_mfma_f32_16x16x32_bf16 a[248:251],v[188:191],a[240:243],a[248:251]
v_mfma_f32_16x16x32_bf16 v[72:75],v[184:187],a[220:223],v[72:75]
v_mfma_f32_16x16x32_bf16 v[72:75],v[188:191],a[244:247],v[72:75]
s_waitcnt lgkmcnt(4)
v_mfma_f32_16x16x32_bf16 a[132:135],v[192:195],a[200:203],a[132:135]
ds_read_b64_tr_b16 v[208:209],v231 offset:49920
ds_read_b64_tr_b16 v[210:211],v232 offset:17152
v_mfma_f32_16x16x32_bf16 a[132:135],v[196:199],a[224:227],a[132:135]
ds_read_b64_tr_b16 v[212:213],v231 offset:768
ds_read_b64_tr_b16 v[214:215],v234 offset:17152
v_mfma_f32_16x16x32_bf16 a[140:143],v[192:195],a[204:207],a[140:143]
v_mfma_f32_16x16x32_bf16 a[140:143],v[196:199],a[228:231],a[140:143]
v_mfma_f32_16x16x32_bf16 a[148:151],v[192:195],a[208:211],a[148:151]
v_mfma_f32_16x16x32_bf16 a[148:151],v[196:199],a[232:235],a[148:151]
v_mfma_f32_16x16x32_bf16 a[156:159],v[192:195],a[212:215],a[156:159]
v_mfma_f32_16x16x32_bf16 a[156:159],v[196:199],a[236:239],a[156:159]
v_mfma_f32_16x16x32_bf16 a[252:255],v[192:195],a[216:219],a[252:255]
v_mfma_f32_16x16x32_bf16 a[252:255],v[196:199],a[240:243],a[252:255]
s_waitcnt lgkmcnt(2)
v_mfma_f32_16x16x32_bf16 v[76:79],v[192:195],a[220:223],v[76:79]
s_waitcnt lgkmcnt(0)
s_add_u32 m0,s63,49152
v_mfma_f32_16x16x32_bf16 v[76:79],v[196:199],a[244:247],v[76:79]
s_waitcnt vmcnt(24)
buffer_load_dwordx4 v235,s[32:35],s62 offen lds
v_mfma_f32_16x16x32_bf16 a[0:3],v[200:203],a[200:203],a[0:3]
s_waitcnt vmcnt(17)
ds_read_b64_tr_b16 v[184:185],v230 offset:32768
ds_read_b64_tr_b16 v[186:187],v233
v_mfma_f32_16x16x32_bf16 a[0:3],v[204:207],a[224:227],a[0:3]
ds_read_b64_tr_b16 v[188:189],v233 offset:49152
ds_read_b64_tr_b16 v[190:191],v230 offset:16384
v_mfma_f32_16x16x32_bf16 a[32:35],v[200:203],a[204:207],a[32:35]
buffer_load_dwordx4 v236,s[32:35],s62 offen offset:1024 lds
buffer_load_dwordx4 v237,s[32:35],s62 offen offset:2048 lds
v_mfma_f32_16x16x32_bf16 a[32:35],v[204:207],a[228:231],a[32:35]
buffer_load_dwordx4 v238,s[32:35],s62 offen offset:3072 lds
v_mfma_f32_16x16x32_bf16 a[64:67],v[200:203],a[208:211],a[64:67]
s_add_u32 s62,s62,20480
s_add_u32 m0,s63,147456
v_mfma_f32_16x16x32_bf16 a[64:67],v[204:207],a[232:235],a[64:67]
buffer_load_dwordx4 v235,s[32:35],s62 offen lds
v_mfma_f32_16x16x32_bf16 a[96:99],v[200:203],a[212:215],a[96:99]
v_mfma_f32_16x16x32_bf16 a[96:99],v[204:207],a[236:239],a[96:99]
v_mfma_f32_16x16x32_bf16 a[8:11],v[200:203],a[216:219],a[8:11]
buffer_load_dwordx4 v236,s[32:35],s62 offen offset:1024 lds
v_mfma_f32_16x16x32_bf16 a[8:11],v[204:207],a[240:243],a[8:11]
v_mfma_f32_16x16x32_bf16 a[40:43],v[200:203],a[220:223],a[40:43]
v_mfma_f32_16x16x32_bf16 a[40:43],v[204:207],a[244:247],a[40:43]
buffer_load_dwordx4 v237,s[32:35],s62 offen offset:2048 lds
v_mfma_f32_16x16x32_bf16 a[4:7],v[208:211],a[200:203],a[4:7]
ds_read_b64_tr_b16 v[192:193],v231 offset:32768
ds_read_b64_tr_b16 v[194:195],v234
v_mfma_f32_16x16x32_bf16 a[4:7],v[212:215],a[224:227],a[4:7]
ds_read_b64_tr_b16 v[196:197],v234 offset:49152
ds_read_b64_tr_b16 v[198:199],v231 offset:16384
v_mfma_f32_16x16x32_bf16 a[36:39],v[208:211],a[204:207],a[36:39]
buffer_load_dwordx4 v238,s[32:35],s62 offen offset:3072 lds
v_mfma_f32_16x16x32_bf16 a[36:39],v[212:215],a[228:231],a[36:39]
s_add_u32 s62,s62,20480
s_mov_b32 m0,s63
v_mfma_f32_16x16x32_bf16 a[68:71],v[208:211],a[208:211],a[68:71]
v_mfma_f32_16x16x32_bf16 a[68:71],v[212:215],a[232:235],a[68:71]
buffer_load_dwordx4 v235,s[32:35],s62 offen lds
v_mfma_f32_16x16x32_bf16 a[100:103],v[208:211],a[212:215],a[100:103]
v_mfma_f32_16x16x32_bf16 a[100:103],v[212:215],a[236:239],a[100:103]
v_mfma_f32_16x16x32_bf16 a[12:15],v[208:211],a[216:219],a[12:15]
buffer_load_dwordx4 v236,s[32:35],s62 offen offset:1024 lds
v_mfma_f32_16x16x32_bf16 a[12:15],v[212:215],a[240:243],a[12:15]
v_mfma_f32_16x16x32_bf16 a[44:47],v[208:211],a[220:223],a[44:47]
v_mfma_f32_16x16x32_bf16 a[44:47],v[212:215],a[244:247],a[44:47]
s_waitcnt lgkmcnt(4)
buffer_load_dwordx4 v237,s[32:35],s62 offen offset:2048 lds
s_waitcnt vmcnt(26)
v_mfma_f32_16x16x32_bf16 v[0:3],v[184:187],v[152:155],v[0:3]
ds_read_b64_tr_b16 v[200:201],v230 offset:33024
ds_read_b64_tr_b16 v[202:203],v233 offset:256
s_waitcnt vmcnt(20)
v_mfma_f32_16x16x32_bf16 v[0:3],v[188:191],v[176:179],v[0:3]
ds_read_b64_tr_b16 v[204:205],v233 offset:49408
ds_read_b64_tr_b16 v[206:207],v230 offset:16640
v_mfma_f32_16x16x32_bf16 v[8:11],v[184:187],v[156:159],v[8:11]
buffer_load_dwordx4 v238,s[32:35],s62 offen offset:3072 lds
s_waitcnt vmcnt(20)
v_mfma_f32_16x16x32_bf16 v[8:11],v[188:191],v[180:183],v[8:11]
s_add_u32 s62,s62,184320
v_mfma_f32_16x16x32_bf16 v[16:19],v[184:187],v[160:163],v[16:19]
s_waitcnt vmcnt(19)
v_mfma_f32_16x16x32_bf16 v[16:19],v[188:191],a[176:179],v[16:19]
buffer_load_dwordx4 a[160:163],v239,s[48:51],s61 offen sc1
v_mfma_f32_16x16x32_bf16 v[24:27],v[184:187],v[164:167],v[24:27]
s_waitcnt vmcnt(19)
v_mfma_f32_16x16x32_bf16 v[24:27],v[188:191],a[180:183],v[24:27]
v_mfma_f32_16x16x32_bf16 v[32:35],v[184:187],v[168:171],v[32:35]
buffer_load_dwordx4 a[164:167],v239,s[48:51],s61 offen offset:1024 sc1
s_waitcnt vmcnt(19)
v_mfma_f32_16x16x32_bf16 v[32:35],v[188:191],a[184:187],v[32:35]
v_mfma_f32_16x16x32_bf16 v[40:43],v[184:187],v[172:175],v[40:43]
s_waitcnt vmcnt(18)
v_mfma_f32_16x16x32_bf16 v[40:43],v[188:191],a[188:191],v[40:43]
s_waitcnt lgkmcnt(4)
buffer_load_dwordx4 a[168:171],v239,s[48:51],s61 offen offset:2048 sc1
v_mfma_f32_16x16x32_bf16 v[4:7],v[192:195],v[152:155],v[4:7]
ds_read_b64_tr_b16 v[208:209],v231 offset:33024
ds_read_b64_tr_b16 v[210:211],v234 offset:256
v_mfma_f32_16x16x32_bf16 v[4:7],v[196:199],v[176:179],v[4:7]
ds_read_b64_tr_b16 v[212:213],v234 offset:49408
ds_read_b64_tr_b16 v[214:215],v231 offset:16640
v_mfma_f32_16x16x32_bf16 v[12:15],v[192:195],v[156:159],v[12:15]
buffer_load_dwordx4 a[172:175],v239,s[48:51],s61 offen offset:3072 sc1
v_mfma_f32_16x16x32_bf16 v[12:15],v[196:199],v[180:183],v[12:15]
v_mfma_f32_16x16x32_bf16 v[20:23],v[192:195],v[160:163],v[20:23]
v_mfma_f32_16x16x32_bf16 v[20:23],v[196:199],a[176:179],v[20:23]
buffer_load_dwordx4 a[192:195],v252,s[48:51],s61 offen sc1
v_mfma_f32_16x16x32_bf16 v[28:31],v[192:195],v[164:167],v[28:31]
v_mfma_f32_16x16x32_bf16 v[28:31],v[196:199],a[180:183],v[28:31]
v_mfma_f32_16x16x32_bf16 v[36:39],v[192:195],v[168:171],v[36:39]
buffer_load_dwordx4 a[196:199],v252,s[48:51],s61 offen offset:1024 sc1
v_mfma_f32_16x16x32_bf16 v[36:39],v[196:199],a[184:187],v[36:39]
s_add_u32 s61,s61,6144
v_and_b32 v255,0x380,v251
v_mfma_f32_16x16x32_bf16 v[44:47],v[192:195],v[172:175],v[44:47]
buffer_load_dword v253,v255,s[40:43],s69 offen sc1
v_mfma_f32_16x16x32_bf16 v[44:47],v[196:199],a[188:191],v[44:47]
s_waitcnt lgkmcnt(4)
buffer_load_dwordx4 a[200:203],v239,s[48:51],s61 offen sc1
v_mfma_f32_16x16x32_bf16 v[48:51],v[200:203],v[152:155],v[48:51]
ds_read_b64_tr_b16 v[184:185],v230 offset:33280
ds_read_b64_tr_b16 v[186:187],v233 offset:512
v_mfma_f32_16x16x32_bf16 v[48:51],v[204:207],v[176:179],v[48:51]
ds_read_b64_tr_b16 v[188:189],v233 offset:49664
ds_read_b64_tr_b16 v[190:191],v230 offset:16896
v_mfma_f32_16x16x32_bf16 v[56:59],v[200:203],v[156:159],v[56:59]
buffer_load_dwordx4 a[204:207],v239,s[48:51],s61 offen offset:1024 sc1
v_mfma_f32_16x16x32_bf16 v[56:59],v[204:207],v[180:183],v[56:59]
v_mfma_f32_16x16x32_bf16 v[128:131],v[200:203],v[160:163],v[128:131]
v_mfma_f32_16x16x32_bf16 v[128:131],v[204:207],a[176:179],v[128:131]
buffer_load_dwordx4 a[208:211],v239,s[48:51],s61 offen offset:2048 sc1
v_mfma_f32_16x16x32_bf16 v[136:139],v[200:203],v[164:167],v[136:139]
v_mfma_f32_16x16x32_bf16 v[136:139],v[204:207],a[180:183],v[136:139]
v_mfma_f32_16x16x32_bf16 v[96:99],v[200:203],v[168:171],v[96:99]
buffer_load_dwordx4 a[212:215],v239,s[48:51],s61 offen offset:3072 sc1
v_mfma_f32_16x16x32_bf16 v[96:99],v[204:207],a[184:187],v[96:99]
v_mfma_f32_16x16x32_bf16 v[216:219],v[200:203],v[172:175],v[216:219]
v_mfma_f32_16x16x32_bf16 v[216:219],v[204:207],a[188:191],v[216:219]
s_waitcnt lgkmcnt(4)
buffer_load_dwordx4 a[216:219],v252,s[48:51],s61 offen sc1
v_mfma_f32_16x16x32_bf16 v[52:55],v[208:211],v[152:155],v[52:55]
ds_read_b64_tr_b16 v[192:193],v231 offset:33280
ds_read_b64_tr_b16 v[194:195],v234 offset:512
v_mfma_f32_16x16x32_bf16 v[52:55],v[212:215],v[176:179],v[52:55]
ds_read_b64_tr_b16 v[196:197],v234 offset:49664
ds_read_b64_tr_b16 v[198:199],v231 offset:16896
v_mfma_f32_16x16x32_bf16 v[60:63],v[208:211],v[156:159],v[60:63]
buffer_load_dwordx4 a[220:223],v252,s[48:51],s61 offen offset:1024 sc1
v_mfma_f32_16x16x32_bf16 v[60:63],v[212:215],v[180:183],v[60:63]
s_add_u32 s61,s61,30720
s_add_u32 m0,s63,81920
v_mfma_f32_16x16x32_bf16 v[132:135],v[208:211],v[160:163],v[132:135]
s_mov_b64 s[44:45],s[18:19]
s_cmp_lt_u32 s28,30
s_cselect_b32 s46,0x3c000,0
v_mfma_f32_16x16x32_bf16 v[132:135],v[212:215],a[176:179],v[132:135]
buffer_load_dwordx4 v235,s[32:35],s62 offen lds
v_mfma_f32_16x16x32_bf16 v[140:143],v[208:211],v[164:167],v[140:143]
v_mfma_f32_16x16x32_bf16 v[140:143],v[212:215],a[180:183],v[140:143]
v_mfma_f32_16x16x32_bf16 v[144:147],v[208:211],v[168:171],v[144:147]
buffer_load_dwordx4 v236,s[32:35],s62 offen offset:1024 lds
v_mfma_f32_16x16x32_bf16 v[144:147],v[212:215],a[184:187],v[144:147]
v_mfma_f32_16x16x32_bf16 v[220:223],v[208:211],v[172:175],v[220:223]
v_mfma_f32_16x16x32_bf16 v[220:223],v[212:215],a[188:191],v[220:223]
s_waitcnt lgkmcnt(4)
buffer_load_dwordx4 v237,s[32:35],s62 offen offset:2048 lds
v_mfma_f32_16x16x32_bf16 a[128:131],v[184:187],v[152:155],a[128:131]
ds_read_b64_tr_b16 v[200:201],v230 offset:33536
ds_read_b64_tr_b16 v[202:203],v233 offset:768
v_mfma_f32_16x16x32_bf16 a[128:131],v[188:191],v[176:179],a[128:131]
ds_read_b64_tr_b16 v[204:205],v233 offset:49920
ds_read_b64_tr_b16 v[206:207],v230 offset:17152
v_mfma_f32_16x16x32_bf16 a[136:139],v[184:187],v[156:159],a[136:139]
buffer_load_dwordx4 v238,s[32:35],s62 offen offset:3072 lds
v_mfma_f32_16x16x32_bf16 a[136:139],v[188:191],v[180:183],a[136:139]
s_add_u32 s62,s62,20480
s_xor_b32 s66,s60,1
s_lshl_b32 s66,s66,12
s_add_u32 s66,s66,32768
v_add_u32 v245,s66,v239
v_mfma_f32_16x16x32_bf16 a[144:147],v[184:187],v[160:163],a[144:147]
s_add_u32 s66,s63,32768
v_add_u32 v240,s66,v239
v_mfma_f32_16x16x32_bf16 a[144:147],v[188:191],a[176:179],a[144:147]
v_mfma_f32_16x16x32_bf16 a[152:155],v[184:187],v[164:167],a[152:155]
v_mfma_f32_16x16x32_bf16 a[152:155],v[188:191],a[180:183],a[152:155]
v_mfma_f32_16x16x32_bf16 a[248:251],v[184:187],v[168:171],a[248:251]
v_mfma_f32_16x16x32_bf16 a[248:251],v[188:191],a[184:187],a[248:251]
s_xor_b32 s66,s60,2
s_lshl_b32 s66,s66,12
s_add_u32 s66,s66,0
v_add_u32 v246,s66,v239
v_mfma_f32_16x16x32_bf16 v[72:75],v[184:187],v[172:175],v[72:75]
s_add_u32 s66,s63,0
v_add_u32 v241,s66,v239
v_mfma_f32_16x16x32_bf16 v[72:75],v[188:191],a[188:191],v[72:75]
s_waitcnt lgkmcnt(4)
v_mfma_f32_16x16x32_bf16 a[132:135],v[192:195],v[152:155],a[132:135]
ds_read_b64_tr_b16 v[208:209],v231 offset:33536
ds_read_b64_tr_b16 v[210:211],v234 offset:768
v_mfma_f32_16x16x32_bf16 a[132:135],v[196:199],v[176:179],a[132:135]
ds_read_b64_tr_b16 v[212:213],v234 offset:49920
ds_read_b64_tr_b16 v[214:215],v231 offset:17152
v_mfma_f32_16x16x32_bf16 a[140:143],v[192:195],v[156:159],a[140:143]
v_mfma_f32_16x16x32_bf16 a[140:143],v[196:199],v[180:183],a[140:143]
v_mfma_f32_16x16x32_bf16 a[148:151],v[192:195],v[160:163],a[148:151]
v_mfma_f32_16x16x32_bf16 a[148:151],v[196:199],a[176:179],a[148:151]
s_xor_b32 s66,s60,2
s_lshl_b32 s66,s66,12
s_add_u32 s66,s66,147456
v_add_u32 v247,s66,v239
v_mfma_f32_16x16x32_bf16 a[156:159],v[192:195],v[164:167],a[156:159]
s_add_u32 s66,s63,147456
v_add_u32 v242,s66,v239
v_mfma_f32_16x16x32_bf16 a[156:159],v[196:199],a[180:183],a[156:159]
v_mfma_f32_16x16x32_bf16 a[252:255],v[192:195],v[168:171],a[252:255]
v_mfma_f32_16x16x32_bf16 a[252:255],v[196:199],a[184:187],a[252:255]
)A"
R"A(s_waitcnt lgkmcnt(2)
v_mfma_f32_16x16x32_bf16 v[76:79],v[192:195],v[172:175],v[76:79]
s_waitcnt lgkmcnt(0)
s_add_u32 m0,s63,32768
v_mfma_f32_16x16x32_bf16 v[76:79],v[196:199],a[188:191],v[76:79]
s_waitcnt vmcnt(25)
buffer_load_dwordx4 v235,s[32:35],s62 offen lds
v_mfma_f32_16x16x32_bf16 a[0:3],v[200:203],v[152:155],a[0:3]
s_waitcnt vmcnt(18)
ds_read_b64_tr_b16 v[184:185],v233 offset:32768
ds_read_b64_tr_b16 v[186:187],v230 offset:49152
v_mfma_f32_16x16x32_bf16 a[0:3],v[204:207],v[176:179],a[0:3]
ds_read_b64_tr_b16 v[188:189],v226 offset:16384
ds_read_b64_tr_b16 v[190:191],v230
v_mfma_f32_16x16x32_bf16 a[32:35],v[200:203],v[156:159],a[32:35]
buffer_load_dwordx4 v236,s[32:35],s62 offen offset:1024 lds
buffer_load_dwordx4 v237,s[32:35],s62 offen offset:2048 lds
v_mfma_f32_16x16x32_bf16 a[32:35],v[204:207],v[180:183],a[32:35]
buffer_load_dwordx4 v238,s[32:35],s62 offen offset:3072 lds
s_xor_b32 s66,s60,3
s_lshl_b32 s66,s66,12
s_add_u32 s66,s66,16384
v_add_u32 v248,s66,v239
v_mfma_f32_16x16x32_bf16 a[64:67],v[200:203],v[160:163],a[64:67]
s_add_u32 s62,s62,20480
s_add_u32 s66,s63,16384
v_add_u32 v243,s66,v239
v_mfma_f32_16x16x32_bf16 a[64:67],v[204:207],a[176:179],a[64:67]
s_add_u32 m0,s63,65536
v_mfma_f32_16x16x32_bf16 a[96:99],v[200:203],v[164:167],a[96:99]
buffer_load_dwordx4 v235,s[32:35],s62 offen lds
v_mfma_f32_16x16x32_bf16 a[96:99],v[204:207],a[180:183],a[96:99]
v_mfma_f32_16x16x32_bf16 a[8:11],v[200:203],v[168:171],a[8:11]
buffer_load_dwordx4 v236,s[32:35],s62 offen offset:1024 lds
v_mfma_f32_16x16x32_bf16 a[8:11],v[204:207],a[184:187],a[8:11]
s_xor_b32 s66,s60,3
s_lshl_b32 s66,s66,12
s_add_u32 s66,s66,98304
v_add_u32 v249,s66,v239
v_mfma_f32_16x16x32_bf16 a[40:43],v[200:203],v[172:175],a[40:43]
s_add_u32 s66,s63,98304
v_add_u32 v244,s66,v239
v_mfma_f32_16x16x32_bf16 a[40:43],v[204:207],a[188:191],a[40:43]
buffer_load_dwordx4 v237,s[32:35],s62 offen offset:2048 lds
v_bfe_u32 v250,v239,4,4
v_mfma_f32_16x16x32_bf16 a[4:7],v[208:211],v[152:155],a[4:7]
ds_read_b64_tr_b16 v[192:193],v234 offset:32768
ds_read_b64_tr_b16 v[194:195],v231 offset:49152
v_mfma_f32_16x16x32_bf16 a[4:7],v[212:215],v[176:179],a[4:7]
ds_read_b64_tr_b16 v[196:197],v232 offset:16384
ds_read_b64_tr_b16 v[198:199],v231
v_mfma_f32_16x16x32_bf16 a[36:39],v[208:211],v[156:159],a[36:39]
buffer_load_dwordx4 v238,s[32:35],s62 offen offset:3072 lds
v_mfma_f32_16x16x32_bf16 a[36:39],v[212:215],v[180:183],a[36:39]
s_add_u32 s62,s62,20480
s_add_u32 m0,s63,114688
v_mul_u32_u24 v250,0xa00,v250
v_mfma_f32_16x16x32_bf16 a[68:71],v[208:211],v[160:163],a[68:71]
v_mfma_f32_16x16x32_bf16 a[68:71],v[212:215],a[176:179],a[68:71]
buffer_load_dwordx4 v235,s[32:35],s62 offen lds
v_mfma_f32_16x16x32_bf16 a[100:103],v[208:211],v[164:167],a[100:103]
v_mfma_f32_16x16x32_bf16 a[100:103],v[212:215],a[180:183],a[100:103]
v_lshrrev_b32 v228,8,v239
v_mfma_f32_16x16x32_bf16 a[12:15],v[208:211],v[168:171],a[12:15]
buffer_load_dwordx4 v236,s[32:35],s62 offen offset:1024 lds
v_mfma_f32_16x16x32_bf16 a[12:15],v[212:215],a[184:187],a[12:15]
v_mfma_f32_16x16x32_bf16 a[44:47],v[208:211],v[172:175],a[44:47]
v_mfma_f32_16x16x32_bf16 a[44:47],v[212:215],a[188:191],a[44:47]
s_waitcnt lgkmcnt(4)
buffer_load_dwordx4 v237,s[32:35],s62 offen offset:2048 lds
v_lshl_or_b32 v250,v228,4,v250
s_waitcnt vmcnt(27)
v_mfma_f32_16x16x32_bf16 v[0:3],v[184:187],a[160:163],v[0:3]
ds_read_b64_tr_b16 v[200:201],v233 offset:33024
ds_read_b64_tr_b16 v[202:203],v230 offset:49408
s_waitcnt vmcnt(20)
v_mfma_f32_16x16x32_bf16 v[0:3],v[188:191],a[200:203],v[0:3]
ds_read_b64_tr_b16 v[204:205],v226 offset:16640
ds_read_b64_tr_b16 v[206:207],v230 offset:256
v_mfma_f32_16x16x32_bf16 v[8:11],v[184:187],a[164:167],v[8:11]
buffer_load_dwordx4 v238,s[32:35],s62 offen offset:3072 lds
s_waitcnt vmcnt(20)
v_mfma_f32_16x16x32_bf16 v[8:11],v[188:191],a[204:207],v[8:11]
s_add_u32 s62,s62,20480
s_lshl_b32 s66,s60,6
v_add_u32 v250,s66,v250
v_mfma_f32_16x16x32_bf16 v[16:19],v[184:187],a[168:171],v[16:19]
s_waitcnt vmcnt(19)
v_mfma_f32_16x16x32_bf16 v[16:19],v[188:191],a[208:211],v[16:19]
v_mfma_f32_16x16x32_bf16 v[24:27],v[184:187],a[172:175],v[24:27]
s_waitcnt vmcnt(18)
v_mfma_f32_16x16x32_bf16 v[24:27],v[188:191],a[212:215],v[24:27]
v_mfma_f32_16x16x32_bf16 v[32:35],v[184:187],a[192:195],v[32:35]
s_waitcnt vmcnt(17)
v_mfma_f32_16x16x32_bf16 v[32:35],v[188:191],a[216:219],v[32:35]
v_cmp_eq_u32 s[72:73],s59,v253
s_nop 0
s_bitcmp1_b32 s72,6
s_cbranch_scc0 .Lspb24
.Lsbb24:
v_mfma_f32_16x16x32_bf16 v[40:43],v[184:187],a[196:199],v[40:43]
buffer_load_dwordx4 a[224:227],v239,s[48:51],s61 offen sc1
buffer_load_dwordx4 a[228:231],v239,s[48:51],s61 offen offset:1024 sc1
s_waitcnt vmcnt(18)
v_mfma_f32_16x16x32_bf16 v[40:43],v[188:191],a[220:223],v[40:43]
s_waitcnt lgkmcnt(4)
buffer_load_dwordx4 a[232:235],v239,s[48:51],s61 offen offset:2048 sc1
v_mfma_f32_16x16x32_bf16 v[4:7],v[192:195],a[160:163],v[4:7]
ds_read_b64_tr_b16 v[208:209],v234 offset:33024
ds_read_b64_tr_b16 v[210:211],v231 offset:49408
v_mfma_f32_16x16x32_bf16 v[4:7],v[196:199],a[200:203],v[4:7]
ds_read_b64_tr_b16 v[212:213],v232 offset:16640
ds_read_b64_tr_b16 v[214:215],v231 offset:256
v_mfma_f32_16x16x32_bf16 v[12:15],v[192:195],a[164:167],v[12:15]
buffer_load_dwordx4 a[236:239],v239,s[48:51],s61 offen offset:3072 sc1
v_mfma_f32_16x16x32_bf16 v[12:15],v[196:199],a[204:207],v[12:15]
v_mfma_f32_16x16x32_bf16 v[20:23],v[192:195],a[168:171],v[20:23]
v_mfma_f32_16x16x32_bf16 v[20:23],v[196:199],a[208:211],v[20:23]
buffer_load_dwordx4 a[240:243],v252,s[48:51],s61 offen sc1
v_mfma_f32_16x16x32_bf16 v[28:31],v[192:195],a[172:175],v[28:31]
v_mfma_f32_16x16x32_bf16 v[28:31],v[196:199],a[212:215],v[28:31]
v_mfma_f32_16x16x32_bf16 v[36:39],v[192:195],a[192:195],v[36:39]
buffer_load_dwordx4 a[244:247],v252,s[48:51],s61 offen offset:1024 sc1
v_mfma_f32_16x16x32_bf16 v[36:39],v[196:199],a[216:219],v[36:39]
s_add_u32 s61,s61,6144
v_mfma_f32_16x16x32_bf16 v[44:47],v[192:195],a[196:199],v[44:47]
v_mfma_f32_16x16x32_bf16 v[44:47],v[196:199],a[220:223],v[44:47]
s_waitcnt lgkmcnt(4)
buffer_load_dwordx4 v[152:155],v239,s[48:51],s61 offen sc1
v_mfma_f32_16x16x32_bf16 v[48:51],v[200:203],a[160:163],v[48:51]
ds_read_b64_tr_b16 v[184:185],v233 offset:33280
ds_read_b64_tr_b16 v[186:187],v230 offset:49664
v_mfma_f32_16x16x32_bf16 v[48:51],v[204:207],a[200:203],v[48:51]
ds_read_b64_tr_b16 v[188:189],v226 offset:16896
ds_read_b64_tr_b16 v[190:191],v230 offset:512
v_mfma_f32_16x16x32_bf16 v[56:59],v[200:203],a[164:167],v[56:59]
buffer_load_dwordx4 v[156:159],v239,s[48:51],s61 offen offset:1024 sc1
v_mfma_f32_16x16x32_bf16 v[56:59],v[204:207],a[204:207],v[56:59]
v_mfma_f32_16x16x32_bf16 v[128:131],v[200:203],a[168:171],v[128:131]
v_mfma_f32_16x16x32_bf16 v[128:131],v[204:207],a[208:211],v[128:131]
buffer_load_dwordx4 v[160:163],v239,s[48:51],s61 offen offset:2048 sc1
v_mfma_f32_16x16x32_bf16 v[136:139],v[200:203],a[172:175],v[136:139]
v_mfma_f32_16x16x32_bf16 v[136:139],v[204:207],a[212:215],v[136:139]
v_mfma_f32_16x16x32_bf16 v[96:99],v[200:203],a[192:195],v[96:99]
buffer_load_dwordx4 v[164:167],v239,s[48:51],s61 offen offset:3072 sc1
v_mfma_f32_16x16x32_bf16 v[96:99],v[204:207],a[216:219],v[96:99]
v_mfma_f32_16x16x32_bf16 v[216:219],v[200:203],a[196:199],v[216:219]
v_mfma_f32_16x16x32_bf16 v[216:219],v[204:207],a[220:223],v[216:219]
s_waitcnt lgkmcnt(4)
buffer_load_dwordx4 v[168:171],v252,s[48:51],s61 offen sc1
v_mfma_f32_16x16x32_bf16 v[52:55],v[208:211],a[160:163],v[52:55]
ds_read_b64_tr_b16 v[192:193],v234 offset:33280
ds_read_b64_tr_b16 v[194:195],v231 offset:49664
v_mfma_f32_16x16x32_bf16 v[52:55],v[212:215],a[200:203],v[52:55]
ds_read_b64_tr_b16 v[196:197],v232 offset:16896
ds_read_b64_tr_b16 v[198:199],v231 offset:512
v_mfma_f32_16x16x32_bf16 v[60:63],v[208:211],a[164:167],v[60:63]
buffer_load_dwordx4 v[172:175],v252,s[48:51],s61 offen offset:1024 sc1
v_mfma_f32_16x16x32_bf16 v[60:63],v[212:215],a[204:207],v[60:63]
s_add_u32 s61,s61,6144
s_add_u32 m0,s63,16384
v_mfma_f32_16x16x32_bf16 v[132:135],v[208:211],a[168:171],v[132:135]
v_mfma_f32_16x16x32_bf16 v[132:135],v[212:215],a[208:211],v[132:135]
buffer_load_dwordx4 v235,s[32:35],s62 offen lds
v_mfma_f32_16x16x32_bf16 v[140:143],v[208:211],a[172:175],v[140:143]
v_mfma_f32_16x16x32_bf16 v[140:143],v[212:215],a[212:215],v[140:143]
v_mfma_f32_16x16x32_bf16 v[144:147],v[208:211],a[192:195],v[144:147]
buffer_load_dwordx4 v236,s[32:35],s62 offen offset:1024 lds
v_mfma_f32_16x16x32_bf16 v[144:147],v[212:215],a[216:219],v[144:147]
v_mfma_f32_16x16x32_bf16 v[220:223],v[208:211],a[196:199],v[220:223]
v_mfma_f32_16x16x32_bf16 v[220:223],v[212:215],a[220:223],v[220:223]
s_waitcnt lgkmcnt(4)
buffer_load_dwordx4 v237,s[32:35],s62 offen offset:2048 lds
v_mfma_f32_16x16x32_bf16 a[128:131],v[184:187],a[160:163],a[128:131]
ds_read_b64_tr_b16 v[200:201],v233 offset:33536
ds_read_b64_tr_b16 v[202:203],v230 offset:49920
v_mfma_f32_16x16x32_bf16 a[128:131],v[188:191],a[200:203],a[128:131]
ds_read_b64_tr_b16 v[204:205],v226 offset:17152
ds_read_b64_tr_b16 v[206:207],v230 offset:768
v_mfma_f32_16x16x32_bf16 a[136:139],v[184:187],a[164:167],a[136:139]
buffer_load_dwordx4 v238,s[32:35],s62 offen offset:3072 lds
v_mfma_f32_16x16x32_bf16 a[136:139],v[188:191],a[204:207],a[136:139]
s_add_u32 s62,s62,20480
v_mfma_f32_16x16x32_bf16 a[144:147],v[184:187],a[168:171],a[144:147]
v_mfma_f32_16x16x32_bf16 a[144:147],v[188:191],a[208:211],a[144:147]
v_mfma_f32_16x16x32_bf16 a[152:155],v[184:187],a[172:175],a[152:155]
v_mfma_f32_16x16x32_bf16 a[152:155],v[188:191],a[212:215],a[152:155]
v_mfma_f32_16x16x32_bf16 a[248:251],v[184:187],a[192:195],a[248:251]
v_mfma_f32_16x16x32_bf16 a[248:251],v[188:191],a[216:219],a[248:251]
v_mfma_f32_16x16x32_bf16 v[72:75],v[184:187],a[196:199],v[72:75]
v_mfma_f32_16x16x32_bf16 v[72:75],v[188:191],a[220:223],v[72:75]
s_waitcnt lgkmcnt(4)
v_mfma_f32_16x16x32_bf16 a[132:135],v[192:195],a[160:163],a[132:135]
ds_read_b64_tr_b16 v[208:209],v234 offset:33536
ds_read_b64_tr_b16 v[210:211],v231 offset:49920
v_mfma_f32_16x16x32_bf16 a[132:135],v[196:199],a[200:203],a[132:135]
ds_read_b64_tr_b16 v[212:213],v232 offset:17152
ds_read_b64_tr_b16 v[214:215],v231 offset:768
v_mfma_f32_16x16x32_bf16 a[140:143],v[192:195],a[164:167],a[140:143]
v_mfma_f32_16x16x32_bf16 a[140:143],v[196:199],a[204:207],a[140:143]
v_mfma_f32_16x16x32_bf16 a[148:151],v[192:195],a[168:171],a[148:151]
v_mfma_f32_16x16x32_bf16 a[148:151],v[196:199],a[208:211],a[148:151]
v_mfma_f32_16x16x32_bf16 a[156:159],v[192:195],a[172:175],a[156:159]
v_mfma_f32_16x16x32_bf16 a[156:159],v[196:199],a[212:215],a[156:159]
v_mfma_f32_16x16x32_bf16 a[252:255],v[192:195],a[192:195],a[252:255]
v_mfma_f32_16x16x32_bf16 a[252:255],v[196:199],a[216:219],a[252:255]
s_waitcnt lgkmcnt(2)
v_mfma_f32_16x16x32_bf16 v[76:79],v[192:195],a[196:199],v[76:79]
s_waitcnt lgkmcnt(0)
s_add_u32 m0,s63,98304
v_mfma_f32_16x16x32_bf16 v[76:79],v[196:199],a[220:223],v[76:79]
s_waitcnt vmcnt(24)
buffer_load_dwordx4 v235,s[32:35],s62 offen lds
v_mfma_f32_16x16x32_bf16 a[0:3],v[200:203],a[160:163],a[0:3]
s_waitcnt vmcnt(17)
ds_read_b64_tr_b16 v[184:185],v233 offset:16384
ds_read_b64_tr_b16 v[186:187],v230 offset:32768
v_mfma_f32_16x16x32_bf16 a[0:3],v[204:207],a[200:203],a[0:3]
ds_read_b64_tr_b16 v[188:189],v233
ds_read_b64_tr_b16 v[190:191],v233 offset:49152
v_mfma_f32_16x16x32_bf16 a[32:35],v[200:203],a[164:167],a[32:35]
buffer_load_dwordx4 v236,s[32:35],s62 offen offset:1024 lds
buffer_load_dwordx4 v237,s[32:35],s62 offen offset:2048 lds
v_mfma_f32_16x16x32_bf16 a[32:35],v[204:207],a[204:207],a[32:35]
buffer_load_dwordx4 v238,s[32:35],s62 offen offset:3072 lds
v_mfma_f32_16x16x32_bf16 a[64:67],v[200:203],a[168:171],a[64:67]
s_add_u32 s62,s62,20480
s_add_u32 m0,s63,49152
v_mfma_f32_16x16x32_bf16 a[64:67],v[204:207],a[208:211],a[64:67]
buffer_load_dwordx4 v235,s[32:35],s62 offen lds
v_mfma_f32_16x16x32_bf16 a[96:99],v[200:203],a[172:175],a[96:99]
v_mfma_f32_16x16x32_bf16 a[96:99],v[204:207],a[212:215],a[96:99]
v_mfma_f32_16x16x32_bf16 a[8:11],v[200:203],a[192:195],a[8:11]
buffer_load_dwordx4 v236,s[32:35],s62 offen offset:1024 lds
)A"
R"A(v_mfma_f32_16x16x32_bf16 a[8:11],v[204:207],a[216:219],a[8:11]
v_mfma_f32_16x16x32_bf16 a[40:43],v[200:203],a[196:199],a[40:43]
v_mfma_f32_16x16x32_bf16 a[40:43],v[204:207],a[220:223],a[40:43]
buffer_load_dwordx4 v237,s[32:35],s62 offen offset:2048 lds
v_mfma_f32_16x16x32_bf16 a[4:7],v[208:211],a[160:163],a[4:7]
ds_read_b64_tr_b16 v[192:193],v234 offset:16384
ds_read_b64_tr_b16 v[194:195],v231 offset:32768
v_mfma_f32_16x16x32_bf16 a[4:7],v[212:215],a[200:203],a[4:7]
ds_read_b64_tr_b16 v[196:197],v234
ds_read_b64_tr_b16 v[198:199],v234 offset:49152
v_mfma_f32_16x16x32_bf16 a[36:39],v[208:211],a[164:167],a[36:39]
buffer_load_dwordx4 v238,s[32:35],s62 offen offset:3072 lds
v_mfma_f32_16x16x32_bf16 a[36:39],v[212:215],a[204:207],a[36:39]
s_add_u32 s62,s62,20480
s_add_u32 m0,s63,147456
v_mfma_f32_16x16x32_bf16 a[68:71],v[208:211],a[168:171],a[68:71]
v_mfma_f32_16x16x32_bf16 a[68:71],v[212:215],a[208:211],a[68:71]
buffer_load_dwordx4 v235,s[32:35],s62 offen lds
v_mfma_f32_16x16x32_bf16 a[100:103],v[208:211],a[172:175],a[100:103]
v_mfma_f32_16x16x32_bf16 a[100:103],v[212:215],a[212:215],a[100:103]
v_mfma_f32_16x16x32_bf16 a[12:15],v[208:211],a[192:195],a[12:15]
buffer_load_dwordx4 v236,s[32:35],s62 offen offset:1024 lds
v_mfma_f32_16x16x32_bf16 a[12:15],v[212:215],a[216:219],a[12:15]
v_mfma_f32_16x16x32_bf16 a[44:47],v[208:211],a[196:199],a[44:47]
v_mfma_f32_16x16x32_bf16 a[44:47],v[212:215],a[220:223],a[44:47]
s_waitcnt lgkmcnt(4)
buffer_load_dwordx4 v237,s[32:35],s62 offen offset:2048 lds
s_waitcnt vmcnt(26)
v_mfma_f32_16x16x32_bf16 v[0:3],v[184:187],a[224:227],v[0:3]
ds_read_b64_tr_b16 v[200:201],v233 offset:16640
ds_read_b64_tr_b16 v[202:203],v230 offset:33024
s_waitcnt vmcnt(20)
v_mfma_f32_16x16x32_bf16 v[0:3],v[188:191],v[152:155],v[0:3]
ds_read_b64_tr_b16 v[204:205],v233 offset:256
ds_read_b64_tr_b16 v[206:207],v233 offset:49408
v_mfma_f32_16x16x32_bf16 v[8:11],v[184:187],a[228:231],v[8:11]
buffer_load_dwordx4 v238,s[32:35],s62 offen offset:3072 lds
s_waitcnt vmcnt(20)
v_mfma_f32_16x16x32_bf16 v[8:11],v[188:191],v[156:159],v[8:11]
s_add_u32 s62,s62,184320
v_mfma_f32_16x16x32_bf16 v[16:19],v[184:187],a[232:235],v[16:19]
s_waitcnt vmcnt(19)
v_mfma_f32_16x16x32_bf16 v[16:19],v[188:191],v[160:163],v[16:19]
buffer_load_dwordx4 v[176:179],v239,s[48:51],s61 offen sc1
v_mfma_f32_16x16x32_bf16 v[24:27],v[184:187],a[236:239],v[24:27]
s_waitcnt vmcnt(19)
v_mfma_f32_16x16x32_bf16 v[24:27],v[188:191],v[164:167],v[24:27]
v_mfma_f32_16x16x32_bf16 v[32:35],v[184:187],a[240:243],v[32:35]
buffer_load_dwordx4 v[180:183],v239,s[48:51],s61 offen offset:1024 sc1
s_waitcnt vmcnt(19)
v_mfma_f32_16x16x32_bf16 v[32:35],v[188:191],v[168:171],v[32:35]
v_mfma_f32_16x16x32_bf16 v[40:43],v[184:187],a[244:247],v[40:43]
s_waitcnt vmcnt(18)
v_mfma_f32_16x16x32_bf16 v[40:43],v[188:191],v[172:175],v[40:43]
s_waitcnt lgkmcnt(4)
buffer_load_dwordx4 a[176:179],v239,s[48:51],s61 offen offset:2048 sc1
v_mfma_f32_16x16x32_bf16 v[4:7],v[192:195],a[224:227],v[4:7]
ds_read_b64_tr_b16 v[208:209],v234 offset:16640
ds_read_b64_tr_b16 v[210:211],v231 offset:33024
v_mfma_f32_16x16x32_bf16 v[4:7],v[196:199],v[152:155],v[4:7]
ds_read_b64_tr_b16 v[212:213],v234 offset:256
ds_read_b64_tr_b16 v[214:215],v234 offset:49408
v_mfma_f32_16x16x32_bf16 v[12:15],v[192:195],a[228:231],v[12:15]
buffer_load_dwordx4 a[180:183],v239,s[48:51],s61 offen offset:3072 sc1
v_mfma_f32_16x16x32_bf16 v[12:15],v[196:199],v[156:159],v[12:15]
v_mfma_f32_16x16x32_bf16 v[20:23],v[192:195],a[232:235],v[20:23]
v_mfma_f32_16x16x32_bf16 v[20:23],v[196:199],v[160:163],v[20:23]
buffer_load_dwordx4 a[184:187],v252,s[48:51],s61 offen sc1
v_mfma_f32_16x16x32_bf16 v[28:31],v[192:195],a[236:239],v[28:31]
v_mfma_f32_16x16x32_bf16 v[28:31],v[196:199],v[164:167],v[28:31]
v_mfma_f32_16x16x32_bf16 v[36:39],v[192:195],a[240:243],v[36:39]
buffer_load_dwordx4 a[188:191],v252,s[48:51],s61 offen offset:1024 sc1
v_mfma_f32_16x16x32_bf16 v[36:39],v[196:199],v[168:171],v[36:39]
s_add_u32 s61,s61,6144
v_and_b32 v255,0x380,v251
v_mfma_f32_16x16x32_bf16 v[44:47],v[192:195],a[244:247],v[44:47]
buffer_load_dword v253,v255,s[40:43],s69 offen sc1
v_mfma_f32_16x16x32_bf16 v[44:47],v[196:199],v[172:175],v[44:47]
s_waitcnt lgkmcnt(4)
buffer_load_dwordx4 a[160:163],v239,s[48:51],s61 offen sc1
v_mfma_f32_16x16x32_bf16 v[48:51],v[200:203],a[224:227],v[48:51]
ds_read_b64_tr_b16 v[184:185],v233 offset:16896
ds_read_b64_tr_b16 v[186:187],v230 offset:33280
v_mfma_f32_16x16x32_bf16 v[48:51],v[204:207],v[152:155],v[48:51]
ds_read_b64_tr_b16 v[188:189],v233 offset:512
ds_read_b64_tr_b16 v[190:191],v233 offset:49664
v_mfma_f32_16x16x32_bf16 v[56:59],v[200:203],a[228:231],v[56:59]
buffer_load_dwordx4 a[164:167],v239,s[48:51],s61 offen offset:1024 sc1
v_mfma_f32_16x16x32_bf16 v[56:59],v[204:207],v[156:159],v[56:59]
v_mfma_f32_16x16x32_bf16 v[128:131],v[200:203],a[232:235],v[128:131]
v_mfma_f32_16x16x32_bf16 v[128:131],v[204:207],v[160:163],v[128:131]
buffer_load_dwordx4 a[168:171],v239,s[48:51],s61 offen offset:2048 sc1
v_mfma_f32_16x16x32_bf16 v[136:139],v[200:203],a[236:239],v[136:139]
v_mfma_f32_16x16x32_bf16 v[136:139],v[204:207],v[164:167],v[136:139]
v_mfma_f32_16x16x32_bf16 v[96:99],v[200:203],a[240:243],v[96:99]
buffer_load_dwordx4 a[172:175],v239,s[48:51],s61 offen offset:3072 sc1
v_mfma_f32_16x16x32_bf16 v[96:99],v[204:207],v[168:171],v[96:99]
v_mfma_f32_16x16x32_bf16 v[216:219],v[200:203],a[244:247],v[216:219]
v_mfma_f32_16x16x32_bf16 v[216:219],v[204:207],v[172:175],v[216:219]
s_waitcnt lgkmcnt(4)
buffer_load_dwordx4 a[192:195],v252,s[48:51],s61 offen sc1
v_mfma_f32_16x16x32_bf16 v[52:55],v[208:211],a[224:227],v[52:55]
ds_read_b64_tr_b16 v[192:193],v234 offset:16896
ds_read_b64_tr_b16 v[194:195],v231 offset:33280
v_mfma_f32_16x16x32_bf16 v[52:55],v[212:215],v[152:155],v[52:55]
ds_read_b64_tr_b16 v[196:197],v234 offset:512
ds_read_b64_tr_b16 v[198:199],v234 offset:49664
v_mfma_f32_16x16x32_bf16 v[60:63],v[208:211],a[228:231],v[60:63]
buffer_load_dwordx4 a[196:199],v252,s[48:51],s61 offen offset:1024 sc1
v_mfma_f32_16x16x32_bf16 v[60:63],v[212:215],v[156:159],v[60:63]
s_add_u32 s61,s61,30720
s_mov_b32 m0,s63
v_mfma_f32_16x16x32_bf16 v[132:135],v[208:211],a[232:235],v[132:135]
v_mfma_f32_16x16x32_bf16 v[132:135],v[212:215],v[160:163],v[132:135]
buffer_load_dwordx4 v235,s[32:35],s62 offen lds
v_mfma_f32_16x16x32_bf16 v[140:143],v[208:211],a[236:239],v[140:143]
v_mfma_f32_16x16x32_bf16 v[140:143],v[212:215],v[164:167],v[140:143]
v_mfma_f32_16x16x32_bf16 v[144:147],v[208:211],a[240:243],v[144:147]
buffer_load_dwordx4 v236,s[32:35],s62 offen offset:1024 lds
v_mfma_f32_16x16x32_bf16 v[144:147],v[212:215],v[168:171],v[144:147]
v_mfma_f32_16x16x32_bf16 v[220:223],v[208:211],a[244:247],v[220:223]
v_mfma_f32_16x16x32_bf16 v[220:223],v[212:215],v[172:175],v[220:223]
s_waitcnt lgkmcnt(4)
buffer_load_dwordx4 v237,s[32:35],s62 offen offset:2048 lds
v_mfma_f32_16x16x32_bf16 a[128:131],v[184:187],a[224:227],a[128:131]
ds_read_b64_tr_b16 v[200:201],v233 offset:17152
ds_read_b64_tr_b16 v[202:203],v230 offset:33536
v_mfma_f32_16x16x32_bf16 a[128:131],v[188:191],v[152:155],a[128:131]
ds_read_b64_tr_b16 v[204:205],v233 offset:768
ds_read_b64_tr_b16 v[206:207],v233 offset:49920
v_mfma_f32_16x16x32_bf16 a[136:139],v[184:187],a[228:231],a[136:139]
buffer_load_dwordx4 v238,s[32:35],s62 offen offset:3072 lds
v_mfma_f32_16x16x32_bf16 a[136:139],v[188:191],v[156:159],a[136:139]
s_add_u32 s62,s62,20480
v_mfma_f32_16x16x32_bf16 a[144:147],v[184:187],a[232:235],a[144:147]
v_mfma_f32_16x16x32_bf16 a[144:147],v[188:191],v[160:163],a[144:147]
v_mfma_f32_16x16x32_bf16 a[152:155],v[184:187],a[236:239],a[152:155]
v_mfma_f32_16x16x32_bf16 a[152:155],v[188:191],v[164:167],a[152:155]
v_mfma_f32_16x16x32_bf16 a[248:251],v[184:187],a[240:243],a[248:251]
v_mfma_f32_16x16x32_bf16 a[248:251],v[188:191],v[168:171],a[248:251]
v_mfma_f32_16x16x32_bf16 v[72:75],v[184:187],a[244:247],v[72:75]
v_mfma_f32_16x16x32_bf16 v[72:75],v[188:191],v[172:175],v[72:75]
s_waitcnt lgkmcnt(4)
v_mfma_f32_16x16x32_bf16 a[132:135],v[192:195],a[224:227],a[132:135]
ds_read_b64_tr_b16 v[208:209],v234 offset:17152
ds_read_b64_tr_b16 v[210:211],v231 offset:33536
v_mfma_f32_16x16x32_bf16 a[132:135],v[196:199],v[152:155],a[132:135]
ds_read_b64_tr_b16 v[212:213],v234 offset:768
ds_read_b64_tr_b16 v[214:215],v234 offset:49920
v_mfma_f32_16x16x32_bf16 a[140:143],v[192:195],a[228:231],a[140:143]
v_mfma_f32_16x16x32_bf16 a[140:143],v[196:199],v[156:159],a[140:143]
v_mfma_f32_16x16x32_bf16 a[148:151],v[192:195],a[232:235],a[148:151]
v_mfma_f32_16x16x32_bf16 a[148:151],v[196:199],v[160:163],a[148:151]
v_mfma_f32_16x16x32_bf16 a[156:159],v[192:195],a[236:239],a[156:159]
v_mfma_f32_16x16x32_bf16 a[156:159],v[196:199],v[164:167],a[156:159]
v_mfma_f32_16x16x32_bf16 a[252:255],v[192:195],a[240:243],a[252:255]
v_mfma_f32_16x16x32_bf16 a[252:255],v[196:199],v[168:171],a[252:255]
s_waitcnt lgkmcnt(2)
v_mfma_f32_16x16x32_bf16 v[76:79],v[192:195],a[244:247],v[76:79]
s_waitcnt lgkmcnt(0)
s_add_u32 m0,s63,81920
v_mfma_f32_16x16x32_bf16 v[76:79],v[196:199],v[172:175],v[76:79]
s_waitcnt vmcnt(25)
buffer_load_dwordx4 v235,s[32:35],s62 offen lds
v_mfma_f32_16x16x32_bf16 a[0:3],v[200:203],a[224:227],a[0:3]
s_waitcnt vmcnt(18)
ds_read_b64_tr_b16 v[184:185],v230 offset:16384
ds_read_b64_tr_b16 v[186:187],v233 offset:32768
v_mfma_f32_16x16x32_bf16 a[0:3],v[204:207],v[152:155],a[0:3]
ds_read_b64_tr_b16 v[188:189],v230 offset:49152
ds_read_b64_tr_b16 v[190:191],v226 offset:16384
v_mfma_f32_16x16x32_bf16 a[32:35],v[200:203],a[228:231],a[32:35]
buffer_load_dwordx4 v236,s[32:35],s62 offen offset:1024 lds
buffer_load_dwordx4 v237,s[32:35],s62 offen offset:2048 lds
v_mfma_f32_16x16x32_bf16 a[32:35],v[204:207],v[156:159],a[32:35]
buffer_load_dwordx4 v238,s[32:35],s62 offen offset:3072 lds
v_mfma_f32_16x16x32_bf16 a[64:67],v[200:203],a[232:235],a[64:67]
s_add_u32 s62,s62,20480
s_add_u32 m0,s63,32768
v_mfma_f32_16x16x32_bf16 a[64:67],v[204:207],v[160:163],a[64:67]
buffer_load_dwordx4 v235,s[32:35],s62 offen lds
v_mfma_f32_16x16x32_bf16 a[96:99],v[200:203],a[236:239],a[96:99]
v_mfma_f32_16x16x32_bf16 a[96:99],v[204:207],v[164:167],a[96:99]
v_mfma_f32_16x16x32_bf16 a[8:11],v[200:203],a[240:243],a[8:11]
buffer_load_dwordx4 v236,s[32:35],s62 offen offset:1024 lds
v_mfma_f32_16x16x32_bf16 a[8:11],v[204:207],v[168:171],a[8:11]
v_mfma_f32_16x16x32_bf16 a[40:43],v[200:203],a[244:247],a[40:43]
v_mfma_f32_16x16x32_bf16 a[40:43],v[204:207],v[172:175],a[40:43]
buffer_load_dwordx4 v237,s[32:35],s62 offen offset:2048 lds
v_mfma_f32_16x16x32_bf16 a[4:7],v[208:211],a[224:227],a[4:7]
ds_read_b64_tr_b16 v[192:193],v231 offset:16384
ds_read_b64_tr_b16 v[194:195],v234 offset:32768
v_mfma_f32_16x16x32_bf16 a[4:7],v[212:215],v[152:155],a[4:7]
ds_read_b64_tr_b16 v[196:197],v231 offset:49152
ds_read_b64_tr_b16 v[198:199],v232 offset:16384
v_mfma_f32_16x16x32_bf16 a[36:39],v[208:211],a[228:231],a[36:39]
buffer_load_dwordx4 v238,s[32:35],s62 offen offset:3072 lds
v_mfma_f32_16x16x32_bf16 a[36:39],v[212:215],v[156:159],a[36:39]
s_add_u32 s62,s62,20480
s_add_u32 m0,s63,65536
v_mfma_f32_16x16x32_bf16 a[68:71],v[208:211],a[232:235],a[68:71]
v_mfma_f32_16x16x32_bf16 a[68:71],v[212:215],v[160:163],a[68:71]
buffer_load_dwordx4 v235,s[32:35],s62 offen lds
v_mfma_f32_16x16x32_bf16 a[100:103],v[208:211],a[236:239],a[100:103]
v_mfma_f32_16x16x32_bf16 a[100:103],v[212:215],v[164:167],a[100:103]
v_mfma_f32_16x16x32_bf16 a[12:15],v[208:211],a[240:243],a[12:15]
buffer_load_dwordx4 v236,s[32:35],s62 offen offset:1024 lds
v_mfma_f32_16x16x32_bf16 a[12:15],v[212:215],v[168:171],a[12:15]
v_mfma_f32_16x16x32_bf16 a[44:47],v[208:211],a[244:247],a[44:47]
v_mfma_f32_16x16x32_bf16 a[44:47],v[212:215],v[172:175],a[44:47]
s_waitcnt lgkmcnt(4)
buffer_load_dwordx4 v237,s[32:35],s62 offen offset:2048 lds
s_waitcnt vmcnt(27)
v_mfma_f32_16x16x32_bf16 v[0:3],v[184:187],v[176:179],v[0:3]
ds_read_b64_tr_b16 v[200:201],v230 offset:16640
ds_read_b64_tr_b16 v[202:203],v233 offset:33024
s_waitcnt vmcnt(20)
v_mfma_f32_16x16x32_bf16 v[0:3],v[188:191],a[160:163],v[0:3]
)A"
R"A(ds_read_b64_tr_b16 v[204:205],v230 offset:49408
ds_read_b64_tr_b16 v[206:207],v226 offset:16640
v_mfma_f32_16x16x32_bf16 v[8:11],v[184:187],v[180:183],v[8:11]
buffer_load_dwordx4 v238,s[32:35],s62 offen offset:3072 lds
s_waitcnt vmcnt(20)
v_mfma_f32_16x16x32_bf16 v[8:11],v[188:191],a[164:167],v[8:11]
s_add_u32 s62,s62,20480
v_mfma_f32_16x16x32_bf16 v[16:19],v[184:187],a[176:179],v[16:19]
s_waitcnt vmcnt(19)
v_mfma_f32_16x16x32_bf16 v[16:19],v[188:191],a[168:171],v[16:19]
v_mfma_f32_16x16x32_bf16 v[24:27],v[184:187],a[180:183],v[24:27]
s_waitcnt vmcnt(18)
v_mfma_f32_16x16x32_bf16 v[24:27],v[188:191],a[172:175],v[24:27]
v_mfma_f32_16x16x32_bf16 v[32:35],v[184:187],a[184:187],v[32:35]
s_waitcnt vmcnt(17)
v_mfma_f32_16x16x32_bf16 v[32:35],v[188:191],a[192:195],v[32:35]
v_cmp_eq_u32 s[72:73],s59,v253
s_nop 0
s_bitcmp1_b32 s72,7
s_cbranch_scc0 .Lspb28
.Lsbb28:
v_mfma_f32_16x16x32_bf16 v[40:43],v[184:187],a[188:191],v[40:43]
buffer_load_dwordx4 a[200:203],v239,s[48:51],s61 offen sc1
buffer_load_dwordx4 a[204:207],v239,s[48:51],s61 offen offset:1024 sc1
s_waitcnt vmcnt(18)
v_mfma_f32_16x16x32_bf16 v[40:43],v[188:191],a[196:199],v[40:43]
s_waitcnt lgkmcnt(4)
buffer_load_dwordx4 a[208:211],v239,s[48:51],s61 offen offset:2048 sc1
v_mfma_f32_16x16x32_bf16 v[4:7],v[192:195],v[176:179],v[4:7]
ds_read_b64_tr_b16 v[208:209],v231 offset:16640
ds_read_b64_tr_b16 v[210:211],v234 offset:33024
v_mfma_f32_16x16x32_bf16 v[4:7],v[196:199],a[160:163],v[4:7]
ds_read_b64_tr_b16 v[212:213],v231 offset:49408
ds_read_b64_tr_b16 v[214:215],v232 offset:16640
v_mfma_f32_16x16x32_bf16 v[12:15],v[192:195],v[180:183],v[12:15]
buffer_load_dwordx4 a[212:215],v239,s[48:51],s61 offen offset:3072 sc1
v_mfma_f32_16x16x32_bf16 v[12:15],v[196:199],a[164:167],v[12:15]
v_mfma_f32_16x16x32_bf16 v[20:23],v[192:195],a[176:179],v[20:23]
v_mfma_f32_16x16x32_bf16 v[20:23],v[196:199],a[168:171],v[20:23]
buffer_load_dwordx4 a[216:219],v252,s[48:51],s61 offen sc1
v_mfma_f32_16x16x32_bf16 v[28:31],v[192:195],a[180:183],v[28:31]
v_mfma_f32_16x16x32_bf16 v[28:31],v[196:199],a[172:175],v[28:31]
v_mfma_f32_16x16x32_bf16 v[36:39],v[192:195],a[184:187],v[36:39]
buffer_load_dwordx4 a[220:223],v252,s[48:51],s61 offen offset:1024 sc1
v_mfma_f32_16x16x32_bf16 v[36:39],v[196:199],a[192:195],v[36:39]
s_add_u32 s61,s61,6144
v_mfma_f32_16x16x32_bf16 v[44:47],v[192:195],a[188:191],v[44:47]
v_mfma_f32_16x16x32_bf16 v[44:47],v[196:199],a[196:199],v[44:47]
s_waitcnt lgkmcnt(4)
buffer_load_dwordx4 a[224:227],v239,s[48:51],s61 offen sc1
v_mfma_f32_16x16x32_bf16 v[48:51],v[200:203],v[176:179],v[48:51]
ds_read_b64_tr_b16 v[184:185],v230 offset:16896
ds_read_b64_tr_b16 v[186:187],v233 offset:33280
v_mfma_f32_16x16x32_bf16 v[48:51],v[204:207],a[160:163],v[48:51]
ds_read_b64_tr_b16 v[188:189],v230 offset:49664
ds_read_b64_tr_b16 v[190:191],v226 offset:16896
v_mfma_f32_16x16x32_bf16 v[56:59],v[200:203],v[180:183],v[56:59]
buffer_load_dwordx4 a[228:231],v239,s[48:51],s61 offen offset:1024 sc1
v_mfma_f32_16x16x32_bf16 v[56:59],v[204:207],a[164:167],v[56:59]
v_mfma_f32_16x16x32_bf16 v[128:131],v[200:203],a[176:179],v[128:131]
v_mfma_f32_16x16x32_bf16 v[128:131],v[204:207],a[168:171],v[128:131]
buffer_load_dwordx4 a[232:235],v239,s[48:51],s61 offen offset:2048 sc1
v_mfma_f32_16x16x32_bf16 v[136:139],v[200:203],a[180:183],v[136:139]
v_mfma_f32_16x16x32_bf16 v[136:139],v[204:207],a[172:175],v[136:139]
v_mfma_f32_16x16x32_bf16 v[96:99],v[200:203],a[184:187],v[96:99]
buffer_load_dwordx4 a[236:239],v239,s[48:51],s61 offen offset:3072 sc1
v_mfma_f32_16x16x32_bf16 v[96:99],v[204:207],a[192:195],v[96:99]
v_mfma_f32_16x16x32_bf16 v[216:219],v[200:203],a[188:191],v[216:219]
v_mfma_f32_16x16x32_bf16 v[216:219],v[204:207],a[196:199],v[216:219]
s_waitcnt lgkmcnt(4)
buffer_load_dwordx4 a[240:243],v252,s[48:51],s61 offen sc1
v_mfma_f32_16x16x32_bf16 v[52:55],v[208:211],v[176:179],v[52:55]
ds_read_b64_tr_b16 v[192:193],v231 offset:16896
ds_read_b64_tr_b16 v[194:195],v234 offset:33280
v_mfma_f32_16x16x32_bf16 v[52:55],v[212:215],a[160:163],v[52:55]
ds_read_b64_tr_b16 v[196:197],v231 offset:49664
ds_read_b64_tr_b16 v[198:199],v232 offset:16896
v_mfma_f32_16x16x32_bf16 v[60:63],v[208:211],v[180:183],v[60:63]
buffer_load_dwordx4 a[244:247],v252,s[48:51],s61 offen offset:1024 sc1
v_mfma_f32_16x16x32_bf16 v[60:63],v[212:215],a[164:167],v[60:63]
s_add_u32 s61,s61,6144
s_add_u32 m0,s63,114688
v_mfma_f32_16x16x32_bf16 v[132:135],v[208:211],a[176:179],v[132:135]
v_mfma_f32_16x16x32_bf16 v[132:135],v[212:215],a[168:171],v[132:135]
buffer_load_dwordx4 v235,s[32:35],s62 offen lds
v_mfma_f32_16x16x32_bf16 v[140:143],v[208:211],a[180:183],v[140:143]
v_mfma_f32_16x16x32_bf16 v[140:143],v[212:215],a[172:175],v[140:143]
v_mfma_f32_16x16x32_bf16 v[144:147],v[208:211],a[184:187],v[144:147]
buffer_load_dwordx4 v236,s[32:35],s62 offen offset:1024 lds
v_mfma_f32_16x16x32_bf16 v[144:147],v[212:215],a[192:195],v[144:147]
v_mfma_f32_16x16x32_bf16 v[220:223],v[208:211],a[188:191],v[220:223]
v_mfma_f32_16x16x32_bf16 v[220:223],v[212:215],a[196:199],v[220:223]
s_waitcnt lgkmcnt(4)
buffer_load_dwordx4 v237,s[32:35],s62 offen offset:2048 lds
v_mfma_f32_16x16x32_bf16 a[128:131],v[184:187],v[176:179],a[128:131]
ds_read_b64_tr_b16 v[200:201],v230 offset:17152
ds_read_b64_tr_b16 v[202:203],v233 offset:33536
v_mfma_f32_16x16x32_bf16 a[128:131],v[188:191],a[160:163],a[128:131]
ds_read_b64_tr_b16 v[204:205],v230 offset:49920
ds_read_b64_tr_b16 v[206:207],v226 offset:17152
v_mfma_f32_16x16x32_bf16 a[136:139],v[184:187],v[180:183],a[136:139]
buffer_load_dwordx4 v238,s[32:35],s62 offen offset:3072 lds
v_mfma_f32_16x16x32_bf16 a[136:139],v[188:191],a[164:167],a[136:139]
s_add_u32 s62,s62,20480
v_mfma_f32_16x16x32_bf16 a[144:147],v[184:187],a[176:179],a[144:147]
v_mfma_f32_16x16x32_bf16 a[144:147],v[188:191],a[168:171],a[144:147]
v_mfma_f32_16x16x32_bf16 a[152:155],v[184:187],a[180:183],a[152:155]
v_mfma_f32_16x16x32_bf16 a[152:155],v[188:191],a[172:175],a[152:155]
v_mfma_f32_16x16x32_bf16 a[248:251],v[184:187],a[184:187],a[248:251]
v_mfma_f32_16x16x32_bf16 a[248:251],v[188:191],a[192:195],a[248:251]
v_mfma_f32_16x16x32_bf16 v[72:75],v[184:187],a[188:191],v[72:75]
v_mfma_f32_16x16x32_bf16 v[72:75],v[188:191],a[196:199],v[72:75]
s_waitcnt lgkmcnt(4)
v_mfma_f32_16x16x32_bf16 a[132:135],v[192:195],v[176:179],a[132:135]
ds_read_b64_tr_b16 v[208:209],v231 offset:17152
ds_read_b64_tr_b16 v[210:211],v234 offset:33536
v_mfma_f32_16x16x32_bf16 a[132:135],v[196:199],a[160:163],a[132:135]
ds_read_b64_tr_b16 v[212:213],v231 offset:49920
ds_read_b64_tr_b16 v[214:215],v232 offset:17152
v_mfma_f32_16x16x32_bf16 a[140:143],v[192:195],v[180:183],a[140:143]
v_mfma_f32_16x16x32_bf16 a[140:143],v[196:199],a[164:167],a[140:143]
v_mfma_f32_16x16x32_bf16 a[148:151],v[192:195],a[176:179],a[148:151]
v_mfma_f32_16x16x32_bf16 a[148:151],v[196:199],a[168:171],a[148:151]
v_mfma_f32_16x16x32_bf16 a[156:159],v[192:195],a[180:183],a[156:159]
v_mfma_f32_16x16x32_bf16 a[156:159],v[196:199],a[172:175],a[156:159]
v_mfma_f32_16x16x32_bf16 a[252:255],v[192:195],a[184:187],a[252:255]
v_mfma_f32_16x16x32_bf16 a[252:255],v[196:199],a[192:195],a[252:255]
s_waitcnt lgkmcnt(2)
v_mfma_f32_16x16x32_bf16 v[76:79],v[192:195],a[188:191],v[76:79]
s_waitcnt lgkmcnt(0)
s_add_u32 m0,s63,16384
v_mfma_f32_16x16x32_bf16 v[76:79],v[196:199],a[196:199],v[76:79]
s_waitcnt vmcnt(24)
buffer_load_dwordx4 v235,s[32:35],s62 offen lds
v_mfma_f32_16x16x32_bf16 a[0:3],v[200:203],v[176:179],a[0:3]
s_waitcnt vmcnt(17)
ds_read_b64_tr_b16 v[184:185],v230
ds_read_b64_tr_b16 v[186:187],v233 offset:16384
v_mfma_f32_16x16x32_bf16 a[0:3],v[204:207],a[160:163],a[0:3]
ds_read_b64_tr_b16 v[188:189],v230 offset:32768
ds_read_b64_tr_b16 v[190:191],v233
v_mfma_f32_16x16x32_bf16 a[32:35],v[200:203],v[180:183],a[32:35]
buffer_load_dwordx4 v236,s[32:35],s62 offen offset:1024 lds
buffer_load_dwordx4 v237,s[32:35],s62 offen offset:2048 lds
v_mfma_f32_16x16x32_bf16 a[32:35],v[204:207],a[164:167],a[32:35]
buffer_load_dwordx4 v238,s[32:35],s62 offen offset:3072 lds
v_mfma_f32_16x16x32_bf16 a[64:67],v[200:203],a[176:179],a[64:67]
s_add_u32 s62,s62,20480
s_add_u32 m0,s63,98304
v_mfma_f32_16x16x32_bf16 a[64:67],v[204:207],a[168:171],a[64:67]
buffer_load_dwordx4 v235,s[32:35],s62 offen lds
v_mfma_f32_16x16x32_bf16 a[96:99],v[200:203],a[180:183],a[96:99]
v_mfma_f32_16x16x32_bf16 a[96:99],v[204:207],a[172:175],a[96:99]
v_mfma_f32_16x16x32_bf16 a[8:11],v[200:203],a[184:187],a[8:11]
buffer_load_dwordx4 v236,s[32:35],s62 offen offset:1024 lds
v_mfma_f32_16x16x32_bf16 a[8:11],v[204:207],a[192:195],a[8:11]
v_mfma_f32_16x16x32_bf16 a[40:43],v[200:203],a[188:191],a[40:43]
v_mfma_f32_16x16x32_bf16 a[40:43],v[204:207],a[196:199],a[40:43]
buffer_load_dwordx4 v237,s[32:35],s62 offen offset:2048 lds
v_mfma_f32_16x16x32_bf16 a[4:7],v[208:211],v[176:179],a[4:7]
ds_read_b64_tr_b16 v[192:193],v231
ds_read_b64_tr_b16 v[194:195],v234 offset:16384
v_mfma_f32_16x16x32_bf16 a[4:7],v[212:215],a[160:163],a[4:7]
ds_read_b64_tr_b16 v[196:197],v231 offset:32768
ds_read_b64_tr_b16 v[198:199],v234
v_mfma_f32_16x16x32_bf16 a[36:39],v[208:211],v[180:183],a[36:39]
buffer_load_dwordx4 v238,s[32:35],s62 offen offset:3072 lds
v_mfma_f32_16x16x32_bf16 a[36:39],v[212:215],a[164:167],a[36:39]
s_add_u32 s62,s62,20480
s_add_u32 m0,s63,49152
v_mfma_f32_16x16x32_bf16 a[68:71],v[208:211],a[176:179],a[68:71]
v_mfma_f32_16x16x32_bf16 a[68:71],v[212:215],a[168:171],a[68:71]
buffer_load_dwordx4 v235,s[32:35],s62 offen lds
v_mfma_f32_16x16x32_bf16 a[100:103],v[208:211],a[180:183],a[100:103]
v_mfma_f32_16x16x32_bf16 a[100:103],v[212:215],a[172:175],a[100:103]
v_mfma_f32_16x16x32_bf16 a[12:15],v[208:211],a[184:187],a[12:15]
buffer_load_dwordx4 v236,s[32:35],s62 offen offset:1024 lds
v_mfma_f32_16x16x32_bf16 a[12:15],v[212:215],a[192:195],a[12:15]
v_mfma_f32_16x16x32_bf16 a[44:47],v[208:211],a[188:191],a[44:47]
v_mfma_f32_16x16x32_bf16 a[44:47],v[212:215],a[196:199],a[44:47]
s_waitcnt lgkmcnt(4)
buffer_load_dwordx4 v237,s[32:35],s62 offen offset:2048 lds
s_waitcnt vmcnt(26)
v_mfma_f32_16x16x32_bf16 v[0:3],v[184:187],a[200:203],v[0:3]
ds_read_b64_tr_b16 v[200:201],v230 offset:256
ds_read_b64_tr_b16 v[202:203],v233 offset:16640
s_waitcnt vmcnt(20)
v_mfma_f32_16x16x32_bf16 v[0:3],v[188:191],a[224:227],v[0:3]
ds_read_b64_tr_b16 v[204:205],v230 offset:33024
ds_read_b64_tr_b16 v[206:207],v233 offset:256
v_mfma_f32_16x16x32_bf16 v[8:11],v[184:187],a[204:207],v[8:11]
buffer_load_dwordx4 v238,s[32:35],s62 offen offset:3072 lds
s_waitcnt vmcnt(20)
v_mfma_f32_16x16x32_bf16 v[8:11],v[188:191],a[228:231],v[8:11]
s_add_u32 s62,s62,184320
v_mfma_f32_16x16x32_bf16 v[16:19],v[184:187],a[208:211],v[16:19]
s_waitcnt vmcnt(19)
v_mfma_f32_16x16x32_bf16 v[16:19],v[188:191],a[232:235],v[16:19]
buffer_load_dwordx4 v[152:155],v239,s[48:51],s61 offen sc1
v_mfma_f32_16x16x32_bf16 v[24:27],v[184:187],a[212:215],v[24:27]
s_waitcnt vmcnt(19)
v_mfma_f32_16x16x32_bf16 v[24:27],v[188:191],a[236:239],v[24:27]
v_mfma_f32_16x16x32_bf16 v[32:35],v[184:187],a[216:219],v[32:35]
buffer_load_dwordx4 v[156:159],v239,s[48:51],s61 offen offset:1024 sc1
s_waitcnt vmcnt(19)
v_mfma_f32_16x16x32_bf16 v[32:35],v[188:191],a[240:243],v[32:35]
v_mfma_f32_16x16x32_bf16 v[40:43],v[184:187],a[220:223],v[40:43]
s_waitcnt vmcnt(18)
v_mfma_f32_16x16x32_bf16 v[40:43],v[188:191],a[244:247],v[40:43]
s_waitcnt lgkmcnt(4)
buffer_load_dwordx4 v[160:163],v239,s[48:51],s61 offen offset:2048 sc1
v_mfma_f32_16x16x32_bf16 v[4:7],v[192:195],a[200:203],v[4:7]
ds_read_b64_tr_b16 v[208:209],v231 offset:256
ds_read_b64_tr_b16 v[210:211],v234 offset:16640
v_mfma_f32_16x16x32_bf16 v[4:7],v[196:199],a[224:227],v[4:7]
ds_read_b64_tr_b16 v[212:213],v231 offset:33024
ds_read_b64_tr_b16 v[214:215],v234 offset:256
v_mfma_f32_16x16x32_bf16 v[12:15],v[192:195],a[204:207],v[12:15]
buffer_load_dwordx4 v[164:167],v239,s[48:51],s61 offen offset:3072 sc1
v_mfma_f32_16x16x32_bf16 v[12:15],v[196:199],a[228:231],v[12:15]
v_mfma_f32_16x16x32_bf16 v[20:23],v[192:195],a[208:211],v[20:23]
v_mfma_f32_16x16x32_bf16 v[20:23],v[196:199],a[232:235],v[20:23]
)A"
R"A(buffer_load_dwordx4 v[168:171],v252,s[48:51],s61 offen sc1
v_mfma_f32_16x16x32_bf16 v[28:31],v[192:195],a[212:215],v[28:31]
v_mfma_f32_16x16x32_bf16 v[28:31],v[196:199],a[236:239],v[28:31]
v_mfma_f32_16x16x32_bf16 v[36:39],v[192:195],a[216:219],v[36:39]
buffer_load_dwordx4 v[172:175],v252,s[48:51],s61 offen offset:1024 sc1
v_mfma_f32_16x16x32_bf16 v[36:39],v[196:199],a[240:243],v[36:39]
s_add_u32 s61,s61,6144
v_mfma_f32_16x16x32_bf16 v[44:47],v[192:195],a[220:223],v[44:47]
v_mfma_f32_16x16x32_bf16 v[44:47],v[196:199],a[244:247],v[44:47]
s_waitcnt lgkmcnt(4)
buffer_load_dwordx4 v[176:179],v239,s[48:51],s61 offen sc1
v_mfma_f32_16x16x32_bf16 v[48:51],v[200:203],a[200:203],v[48:51]
ds_read_b64_tr_b16 v[184:185],v230 offset:512
ds_read_b64_tr_b16 v[186:187],v233 offset:16896
v_mfma_f32_16x16x32_bf16 v[48:51],v[204:207],a[224:227],v[48:51]
ds_read_b64_tr_b16 v[188:189],v230 offset:33280
ds_read_b64_tr_b16 v[190:191],v233 offset:512
v_mfma_f32_16x16x32_bf16 v[56:59],v[200:203],a[204:207],v[56:59]
buffer_load_dwordx4 v[180:183],v239,s[48:51],s61 offen offset:1024 sc1
v_mfma_f32_16x16x32_bf16 v[56:59],v[204:207],a[228:231],v[56:59]
v_mfma_f32_16x16x32_bf16 v[128:131],v[200:203],a[208:211],v[128:131]
v_mfma_f32_16x16x32_bf16 v[128:131],v[204:207],a[232:235],v[128:131]
buffer_load_dwordx4 a[176:179],v239,s[48:51],s61 offen offset:2048 sc1
v_mfma_f32_16x16x32_bf16 v[136:139],v[200:203],a[212:215],v[136:139]
v_mfma_f32_16x16x32_bf16 v[136:139],v[204:207],a[236:239],v[136:139]
v_mfma_f32_16x16x32_bf16 v[96:99],v[200:203],a[216:219],v[96:99]
buffer_load_dwordx4 a[180:183],v239,s[48:51],s61 offen offset:3072 sc1
v_mfma_f32_16x16x32_bf16 v[96:99],v[204:207],a[240:243],v[96:99]
v_mfma_f32_16x16x32_bf16 v[216:219],v[200:203],a[220:223],v[216:219]
v_mfma_f32_16x16x32_bf16 v[216:219],v[204:207],a[244:247],v[216:219]
s_waitcnt lgkmcnt(4)
buffer_load_dwordx4 a[184:187],v252,s[48:51],s61 offen sc1
v_mfma_f32_16x16x32_bf16 v[52:55],v[208:211],a[200:203],v[52:55]
ds_read_b64_tr_b16 v[192:193],v231 offset:512
ds_read_b64_tr_b16 v[194:195],v234 offset:16896
v_mfma_f32_16x16x32_bf16 v[52:55],v[212:215],a[224:227],v[52:55]
ds_read_b64_tr_b16 v[196:197],v231 offset:33280
ds_read_b64_tr_b16 v[198:199],v234 offset:512
v_mfma_f32_16x16x32_bf16 v[60:63],v[208:211],a[204:207],v[60:63]
buffer_load_dwordx4 a[188:191],v252,s[48:51],s61 offen offset:1024 sc1
v_mfma_f32_16x16x32_bf16 v[60:63],v[212:215],a[228:231],v[60:63]
s_add_u32 s61,s61,30720
v_mfma_f32_16x16x32_bf16 v[132:135],v[208:211],a[208:211],v[132:135]
v_mfma_f32_16x16x32_bf16 v[132:135],v[212:215],a[232:235],v[132:135]
v_mfma_f32_16x16x32_bf16 v[140:143],v[208:211],a[212:215],v[140:143]
v_mfma_f32_16x16x32_bf16 v[140:143],v[212:215],a[236:239],v[140:143]
v_mfma_f32_16x16x32_bf16 v[144:147],v[208:211],a[216:219],v[144:147]
v_mfma_f32_16x16x32_bf16 v[144:147],v[212:215],a[240:243],v[144:147]
v_mfma_f32_16x16x32_bf16 v[220:223],v[208:211],a[220:223],v[220:223]
v_mfma_f32_16x16x32_bf16 v[220:223],v[212:215],a[244:247],v[220:223]
s_waitcnt lgkmcnt(4)
v_mfma_f32_16x16x32_bf16 a[128:131],v[184:187],a[200:203],a[128:131]
ds_read_b64_tr_b16 v[200:201],v230 offset:768
ds_read_b64_tr_b16 v[202:203],v233 offset:17152
v_mfma_f32_16x16x32_bf16 a[128:131],v[188:191],a[224:227],a[128:131]
ds_read_b64_tr_b16 v[204:205],v230 offset:33536
ds_read_b64_tr_b16 v[206:207],v233 offset:768
v_mfma_f32_16x16x32_bf16 a[136:139],v[184:187],a[204:207],a[136:139]
v_mfma_f32_16x16x32_bf16 a[136:139],v[188:191],a[228:231],a[136:139]
v_mfma_f32_16x16x32_bf16 a[144:147],v[184:187],a[208:211],a[144:147]
v_mfma_f32_16x16x32_bf16 a[144:147],v[188:191],a[232:235],a[144:147]
v_mfma_f32_16x16x32_bf16 a[152:155],v[184:187],a[212:215],a[152:155]
v_mfma_f32_16x16x32_bf16 a[152:155],v[188:191],a[236:239],a[152:155]
v_mfma_f32_16x16x32_bf16 a[248:251],v[184:187],a[216:219],a[248:251]
v_mfma_f32_16x16x32_bf16 a[248:251],v[188:191],a[240:243],a[248:251]
v_mfma_f32_16x16x32_bf16 v[72:75],v[184:187],a[220:223],v[72:75]
v_mfma_f32_16x16x32_bf16 v[72:75],v[188:191],a[244:247],v[72:75]
s_waitcnt lgkmcnt(4)
v_mfma_f32_16x16x32_bf16 a[132:135],v[192:195],a[200:203],a[132:135]
ds_read_b64_tr_b16 v[208:209],v231 offset:768
ds_read_b64_tr_b16 v[210:211],v234 offset:17152
v_mfma_f32_16x16x32_bf16 a[132:135],v[196:199],a[224:227],a[132:135]
ds_read_b64_tr_b16 v[212:213],v231 offset:33536
ds_read_b64_tr_b16 v[214:215],v234 offset:768
v_mfma_f32_16x16x32_bf16 a[140:143],v[192:195],a[204:207],a[140:143]
v_mfma_f32_16x16x32_bf16 a[140:143],v[196:199],a[228:231],a[140:143]
v_mfma_f32_16x16x32_bf16 a[148:151],v[192:195],a[208:211],a[148:151]
v_mfma_f32_16x16x32_bf16 a[148:151],v[196:199],a[232:235],a[148:151]
v_mfma_f32_16x16x32_bf16 a[156:159],v[192:195],a[212:215],a[156:159]
v_mfma_f32_16x16x32_bf16 a[156:159],v[196:199],a[236:239],a[156:159]
v_mfma_f32_16x16x32_bf16 a[252:255],v[192:195],a[216:219],a[252:255]
v_mfma_f32_16x16x32_bf16 a[252:255],v[196:199],a[240:243],a[252:255]
v_mfma_f32_16x16x32_bf16 v[76:79],v[192:195],a[220:223],v[76:79]
v_mfma_f32_16x16x32_bf16 v[76:79],v[196:199],a[244:247],v[76:79]
s_waitcnt vmcnt(20) lgkmcnt(4)
v_mfma_f32_16x16x32_bf16 a[0:3],v[200:203],a[200:203],a[0:3]
s_waitcnt vmcnt(12)
ds_read_b64_tr_b16 v[184:185],v233 offset:49152
ds_read_b64_tr_b16 v[186:187],v230 offset:16384
v_mfma_f32_16x16x32_bf16 a[0:3],v[204:207],a[224:227],a[0:3]
ds_read_b64_tr_b16 v[188:189],v233 offset:32768
ds_read_b64_tr_b16 v[190:191],v230 offset:49152
v_mfma_f32_16x16x32_bf16 a[32:35],v[200:203],a[204:207],a[32:35]
v_mfma_f32_16x16x32_bf16 a[32:35],v[204:207],a[228:231],a[32:35]
v_mfma_f32_16x16x32_bf16 a[64:67],v[200:203],a[208:211],a[64:67]
v_mfma_f32_16x16x32_bf16 a[64:67],v[204:207],a[232:235],a[64:67]
v_mfma_f32_16x16x32_bf16 a[96:99],v[200:203],a[212:215],a[96:99]
v_mfma_f32_16x16x32_bf16 a[96:99],v[204:207],a[236:239],a[96:99]
v_mfma_f32_16x16x32_bf16 a[8:11],v[200:203],a[216:219],a[8:11]
v_mfma_f32_16x16x32_bf16 a[8:11],v[204:207],a[240:243],a[8:11]
v_mfma_f32_16x16x32_bf16 a[40:43],v[200:203],a[220:223],a[40:43]
v_mfma_f32_16x16x32_bf16 a[40:43],v[204:207],a[244:247],a[40:43]
s_waitcnt lgkmcnt(4)
v_mfma_f32_16x16x32_bf16 a[4:7],v[208:211],a[200:203],a[4:7]
ds_read_b64_tr_b16 v[192:193],v234 offset:49152
ds_read_b64_tr_b16 v[194:195],v231 offset:16384
v_mfma_f32_16x16x32_bf16 a[4:7],v[212:215],a[224:227],a[4:7]
ds_read_b64_tr_b16 v[196:197],v234 offset:32768
ds_read_b64_tr_b16 v[198:199],v231 offset:49152
v_mfma_f32_16x16x32_bf16 a[36:39],v[208:211],a[204:207],a[36:39]
v_mfma_f32_16x16x32_bf16 a[36:39],v[212:215],a[228:231],a[36:39]
v_mfma_f32_16x16x32_bf16 a[68:71],v[208:211],a[208:211],a[68:71]
v_mfma_f32_16x16x32_bf16 a[68:71],v[212:215],a[232:235],a[68:71]
v_mfma_f32_16x16x32_bf16 a[100:103],v[208:211],a[212:215],a[100:103]
v_mfma_f32_16x16x32_bf16 a[100:103],v[212:215],a[236:239],a[100:103]
v_mfma_f32_16x16x32_bf16 a[12:15],v[208:211],a[216:219],a[12:15]
v_mfma_f32_16x16x32_bf16 a[12:15],v[212:215],a[240:243],a[12:15]
v_mfma_f32_16x16x32_bf16 a[44:47],v[208:211],a[220:223],a[44:47]
v_mfma_f32_16x16x32_bf16 a[44:47],v[212:215],a[244:247],a[44:47]
s_waitcnt vmcnt(11) lgkmcnt(4)
v_mfma_f32_16x16x32_bf16 v[0:3],v[184:187],v[152:155],v[0:3]
ds_read_b64_tr_b16 v[200:201],v233 offset:49408
ds_read_b64_tr_b16 v[202:203],v230 offset:16640
s_waitcnt vmcnt(5)
v_mfma_f32_16x16x32_bf16 v[0:3],v[188:191],v[176:179],v[0:3]
ds_read_b64_tr_b16 v[204:205],v233 offset:33024
ds_read_b64_tr_b16 v[206:207],v230 offset:49408
v_mfma_f32_16x16x32_bf16 v[8:11],v[184:187],v[156:159],v[8:11]
s_waitcnt vmcnt(4)
v_mfma_f32_16x16x32_bf16 v[8:11],v[188:191],v[180:183],v[8:11]
v_mfma_f32_16x16x32_bf16 v[16:19],v[184:187],v[160:163],v[16:19]
s_waitcnt vmcnt(3)
v_mfma_f32_16x16x32_bf16 v[16:19],v[188:191],a[176:179],v[16:19]
v_mfma_f32_16x16x32_bf16 v[24:27],v[184:187],v[164:167],v[24:27]
s_waitcnt vmcnt(2)
v_mfma_f32_16x16x32_bf16 v[24:27],v[188:191],a[180:183],v[24:27]
v_mfma_f32_16x16x32_bf16 v[32:35],v[184:187],v[168:171],v[32:35]
s_waitcnt vmcnt(1)
v_mfma_f32_16x16x32_bf16 v[32:35],v[188:191],a[184:187],v[32:35]
v_mfma_f32_16x16x32_bf16 v[40:43],v[184:187],v[172:175],v[40:43]
s_waitcnt vmcnt(0)
v_mfma_f32_16x16x32_bf16 v[40:43],v[188:191],a[188:191],v[40:43]
s_waitcnt lgkmcnt(4)
v_mfma_f32_16x16x32_bf16 v[4:7],v[192:195],v[152:155],v[4:7]
ds_read_b64_tr_b16 v[208:209],v234 offset:49408
ds_read_b64_tr_b16 v[210:211],v231 offset:16640
v_mfma_f32_16x16x32_bf16 v[4:7],v[196:199],v[176:179],v[4:7]
ds_read_b64_tr_b16 v[212:213],v234 offset:33024
ds_read_b64_tr_b16 v[214:215],v231 offset:49408
v_mfma_f32_16x16x32_bf16 v[12:15],v[192:195],v[156:159],v[12:15]
v_mfma_f32_16x16x32_bf16 v[12:15],v[196:199],v[180:183],v[12:15]
v_mfma_f32_16x16x32_bf16 v[20:23],v[192:195],v[160:163],v[20:23]
v_mfma_f32_16x16x32_bf16 v[20:23],v[196:199],a[176:179],v[20:23]
v_mfma_f32_16x16x32_bf16 v[28:31],v[192:195],v[164:167],v[28:31]
v_mfma_f32_16x16x32_bf16 v[28:31],v[196:199],a[180:183],v[28:31]
v_mfma_f32_16x16x32_bf16 v[36:39],v[192:195],v[168:171],v[36:39]
v_mfma_f32_16x16x32_bf16 v[36:39],v[196:199],a[184:187],v[36:39]
v_mfma_f32_16x16x32_bf16 v[44:47],v[192:195],v[172:175],v[44:47]
v_mfma_f32_16x16x32_bf16 v[44:47],v[196:199],a[188:191],v[44:47]
s_waitcnt lgkmcnt(4)
v_mfma_f32_16x16x32_bf16 v[48:51],v[200:203],v[152:155],v[48:51]
ds_read_b64_tr_b16 v[184:185],v233 offset:49664
ds_read_b64_tr_b16 v[186:187],v230 offset:16896
v_mfma_f32_16x16x32_bf16 v[48:51],v[204:207],v[176:179],v[48:51]
ds_read_b64_tr_b16 v[188:189],v233 offset:33280
ds_read_b64_tr_b16 v[190:191],v230 offset:49664
v_mfma_f32_16x16x32_bf16 v[56:59],v[200:203],v[156:159],v[56:59]
v_mfma_f32_16x16x32_bf16 v[56:59],v[204:207],v[180:183],v[56:59]
v_mfma_f32_16x16x32_bf16 v[128:131],v[200:203],v[160:163],v[128:131]
v_mfma_f32_16x16x32_bf16 v[128:131],v[204:207],a[176:179],v[128:131]
v_mfma_f32_16x16x32_bf16 v[136:139],v[200:203],v[164:167],v[136:139]
v_mfma_f32_16x16x32_bf16 v[136:139],v[204:207],a[180:183],v[136:139]
v_mfma_f32_16x16x32_bf16 v[96:99],v[200:203],v[168:171],v[96:99]
v_mfma_f32_16x16x32_bf16 v[96:99],v[204:207],a[184:187],v[96:99]
v_mfma_f32_16x16x32_bf16 v[216:219],v[200:203],v[172:175],v[216:219]
v_mfma_f32_16x16x32_bf16 v[216:219],v[204:207],a[188:191],v[216:219]
s_waitcnt lgkmcnt(4)
v_mfma_f32_16x16x32_bf16 v[52:55],v[208:211],v[152:155],v[52:55]
ds_read_b64_tr_b16 v[192:193],v234 offset:49664
ds_read_b64_tr_b16 v[194:195],v231 offset:16896
v_mfma_f32_16x16x32_bf16 v[52:55],v[212:215],v[176:179],v[52:55]
ds_read_b64_tr_b16 v[196:197],v234 offset:33280
ds_read_b64_tr_b16 v[198:199],v231 offset:49664
v_mfma_f32_16x16x32_bf16 v[60:63],v[208:211],v[156:159],v[60:63]
v_mfma_f32_16x16x32_bf16 v[60:63],v[212:215],v[180:183],v[60:63]
v_mfma_f32_16x16x32_bf16 v[132:135],v[208:211],v[160:163],v[132:135]
v_mfma_f32_16x16x32_bf16 v[132:135],v[212:215],a[176:179],v[132:135]
v_mfma_f32_16x16x32_bf16 v[140:143],v[208:211],v[164:167],v[140:143]
v_mfma_f32_16x16x32_bf16 v[140:143],v[212:215],a[180:183],v[140:143]
v_mfma_f32_16x16x32_bf16 v[144:147],v[208:211],v[168:171],v[144:147]
v_mfma_f32_16x16x32_bf16 v[144:147],v[212:215],a[184:187],v[144:147]
v_mfma_f32_16x16x32_bf16 v[220:223],v[208:211],v[172:175],v[220:223]
v_mfma_f32_16x16x32_bf16 v[220:223],v[212:215],a[188:191],v[220:223]
s_waitcnt lgkmcnt(4)
v_mfma_f32_16x16x32_bf16 a[128:131],v[184:187],v[152:155],a[128:131]
ds_read_b64_tr_b16 v[200:201],v233 offset:49920
ds_read_b64_tr_b16 v[202:203],v230 offset:17152
v_mfma_f32_16x16x32_bf16 a[128:131],v[188:191],v[176:179],a[128:131]
ds_read_b64_tr_b16 v[204:205],v233 offset:33536
ds_read_b64_tr_b16 v[206:207],v230 offset:49920
v_mfma_f32_16x16x32_bf16 a[136:139],v[184:187],v[156:159],a[136:139]
v_mfma_f32_16x16x32_bf16 a[136:139],v[188:191],v[180:183],a[136:139]
v_mfma_f32_16x16x32_bf16 a[144:147],v[184:187],v[160:163],a[144:147]
v_mfma_f32_16x16x32_bf16 a[144:147],v[188:191],a[176:179],a[144:147]
v_mfma_f32_16x16x32_bf16 a[152:155],v[184:187],v[164:167],a[152:155]
v_mfma_f32_16x16x32_bf16 a[152:155],v[188:191],a[180:183],a[152:155]
v_mfma_f32_16x16x32_bf16 a[248:251],v[184:187],v[168:171],a[248:251]
)A"
R"A(v_mfma_f32_16x16x32_bf16 a[248:251],v[188:191],a[184:187],a[248:251]
v_mfma_f32_16x16x32_bf16 v[72:75],v[184:187],v[172:175],v[72:75]
v_mfma_f32_16x16x32_bf16 v[72:75],v[188:191],a[188:191],v[72:75]
s_waitcnt lgkmcnt(4)
v_mfma_f32_16x16x32_bf16 a[132:135],v[192:195],v[152:155],a[132:135]
ds_read_b64_tr_b16 v[208:209],v234 offset:49920
ds_read_b64_tr_b16 v[210:211],v231 offset:17152
v_mfma_f32_16x16x32_bf16 a[132:135],v[196:199],v[176:179],a[132:135]
ds_read_b64_tr_b16 v[212:213],v234 offset:33536
ds_read_b64_tr_b16 v[214:215],v231 offset:49920
v_mfma_f32_16x16x32_bf16 a[140:143],v[192:195],v[156:159],a[140:143]
v_mfma_f32_16x16x32_bf16 a[140:143],v[196:199],v[180:183],a[140:143]
v_mfma_f32_16x16x32_bf16 a[148:151],v[192:195],v[160:163],a[148:151]
v_mfma_f32_16x16x32_bf16 a[148:151],v[196:199],a[176:179],a[148:151]
v_mfma_f32_16x16x32_bf16 a[156:159],v[192:195],v[164:167],a[156:159]
v_mfma_f32_16x16x32_bf16 a[156:159],v[196:199],a[180:183],a[156:159]
v_mfma_f32_16x16x32_bf16 a[252:255],v[192:195],v[168:171],a[252:255]
v_mfma_f32_16x16x32_bf16 a[252:255],v[196:199],a[184:187],a[252:255]
v_mfma_f32_16x16x32_bf16 v[76:79],v[192:195],v[172:175],v[76:79]
v_mfma_f32_16x16x32_bf16 v[76:79],v[196:199],a[188:191],v[76:79]
s_waitcnt lgkmcnt(4)
v_mfma_f32_16x16x32_bf16 a[0:3],v[200:203],v[152:155],a[0:3]
v_mfma_f32_16x16x32_bf16 a[0:3],v[204:207],v[176:179],a[0:3]
v_mfma_f32_16x16x32_bf16 a[32:35],v[200:203],v[156:159],a[32:35]
v_mfma_f32_16x16x32_bf16 a[32:35],v[204:207],v[180:183],a[32:35]
v_mfma_f32_16x16x32_bf16 a[64:67],v[200:203],v[160:163],a[64:67]
v_mfma_f32_16x16x32_bf16 a[64:67],v[204:207],a[176:179],a[64:67]
v_mfma_f32_16x16x32_bf16 a[96:99],v[200:203],v[164:167],a[96:99]
v_mfma_f32_16x16x32_bf16 a[96:99],v[204:207],a[180:183],a[96:99]
v_mfma_f32_16x16x32_bf16 a[8:11],v[200:203],v[168:171],a[8:11]
v_mfma_f32_16x16x32_bf16 a[8:11],v[204:207],a[184:187],a[8:11]
v_mfma_f32_16x16x32_bf16 a[40:43],v[200:203],v[172:175],a[40:43]
v_mfma_f32_16x16x32_bf16 a[40:43],v[204:207],a[188:191],a[40:43]
s_waitcnt lgkmcnt(0)
v_mfma_f32_16x16x32_bf16 a[4:7],v[208:211],v[152:155],a[4:7]
v_mfma_f32_16x16x32_bf16 a[4:7],v[212:215],v[176:179],a[4:7]
v_mfma_f32_16x16x32_bf16 a[36:39],v[208:211],v[156:159],a[36:39]
v_mfma_f32_16x16x32_bf16 a[36:39],v[212:215],v[180:183],a[36:39]
v_mfma_f32_16x16x32_bf16 a[68:71],v[208:211],v[160:163],a[68:71]
v_mfma_f32_16x16x32_bf16 a[68:71],v[212:215],a[176:179],a[68:71]
v_mfma_f32_16x16x32_bf16 a[100:103],v[208:211],v[164:167],a[100:103]
v_mfma_f32_16x16x32_bf16 a[100:103],v[212:215],a[180:183],a[100:103]
v_mfma_f32_16x16x32_bf16 a[12:15],v[208:211],v[168:171],a[12:15]
v_mfma_f32_16x16x32_bf16 a[12:15],v[212:215],a[184:187],a[12:15]
v_mfma_f32_16x16x32_bf16 a[44:47],v[208:211],v[172:175],a[44:47]
v_mfma_f32_16x16x32_bf16 a[44:47],v[212:215],a[188:191],a[44:47]
ds_write_b128 v240,v[48:51]
ds_write_b128 v240,v[56:59] offset:2048
ds_write_b128 v240,v[52:55] offset:1024
ds_write_b128 v240,v[60:63] offset:3072
ds_write_b128 v241,a[128:131]
ds_write_b128 v241,a[136:139] offset:2048
ds_write_b128 v241,a[132:135] offset:1024
ds_write_b128 v241,a[140:143] offset:3072
ds_write_b128 v244,a[0:3]
ds_write_b128 v244,a[32:35] offset:2048
ds_write_b128 v244,a[4:7] offset:1024
ds_write_b128 v244,a[36:39] offset:3072
ds_write_b128 v240,v[128:131] offset:32768
ds_write_b128 v240,v[136:139] offset:34816
ds_write_b128 v240,v[132:135] offset:33792
ds_write_b128 v240,v[140:143] offset:35840
ds_write_b128 v242,a[144:147]
ds_write_b128 v242,a[152:155] offset:2048
ds_write_b128 v242,a[148:151] offset:1024
ds_write_b128 v242,a[156:159] offset:3072
ds_write_b128 v244,a[64:67] offset:16384
ds_write_b128 v244,a[96:99] offset:18432
ds_write_b128 v244,a[68:71] offset:17408
ds_write_b128 v244,a[100:103] offset:19456
s_waitcnt lgkmcnt(12)
s_barrier
ds_read_b128 v[152:155],v245
ds_read_b128 v[156:159],v245 offset:1024
ds_read_b128 v[160:163],v245 offset:2048
ds_read_b128 v[164:167],v245 offset:3072
ds_read_b128 v[168:171],v246
ds_read_b128 v[172:175],v246 offset:1024
ds_read_b128 v[176:179],v246 offset:2048
ds_read_b128 v[180:183],v246 offset:3072
ds_read_b128 v[184:187],v249
ds_read_b128 v[188:191],v249 offset:1024
ds_read_b128 v[192:195],v249 offset:2048
ds_read_b128 v[196:199],v249 offset:3072
s_waitcnt lgkmcnt(12)
s_barrier
ds_write_b128 v240,v[96:99] offset:49152
ds_write_b128 v240,v[216:219] offset:51200
ds_write_b128 v240,v[144:147] offset:50176
ds_write_b128 v240,v[220:223] offset:52224
ds_write_b128 v241,a[248:251] offset:49152
ds_write_b128 v241,v[72:75] offset:51200
s_waitcnt lgkmcnt(15)
v_pk_add_f32 v[0:1],v[0:1],v[152:153]
ds_write_b128 v241,a[252:255] offset:50176
v_pk_add_f32 v[2:3],v[2:3],v[154:155]
v_pk_add_f32 v[4:5],v[4:5],v[156:157]
v_pk_add_f32 v[6:7],v[6:7],v[158:159]
ds_write_b128 v241,v[76:79] offset:52224
v_pk_add_f32 v[8:9],v[8:9],v[160:161]
v_pk_add_f32 v[10:11],v[10:11],v[162:163]
ds_write_b128 v243,a[8:11]
s_waitcnt lgkmcnt(15)
v_pk_add_f32 v[12:13],v[12:13],v[164:165]
v_pk_add_f32 v[14:15],v[14:15],v[166:167]
ds_write_b128 v243,a[40:43] offset:2048
s_waitcnt lgkmcnt(13)
v_pk_add_f32 v[168:169],v[168:169],v[184:185]
v_pk_add_f32 v[170:171],v[170:171],v[186:187]
ds_write_b128 v243,a[12:15] offset:1024
s_waitcnt lgkmcnt(13)
v_pk_add_f32 v[172:173],v[172:173],v[188:189]
v_pk_add_f32 v[174:175],v[174:175],v[190:191]
ds_write_b128 v243,a[44:47] offset:3072
s_waitcnt lgkmcnt(13)
v_pk_add_f32 v[176:177],v[176:177],v[192:193]
v_pk_add_f32 v[178:179],v[178:179],v[194:195]
ds_read_b128 v[48:51],v245 offset:32768
s_waitcnt lgkmcnt(13)
v_pk_add_f32 v[180:181],v[180:181],v[196:197]
v_pk_add_f32 v[182:183],v[182:183],v[198:199]
ds_read_b128 v[52:55],v245 offset:33792
v_pk_add_f32 v[0:1],v[0:1],v[168:169]
v_pk_add_f32 v[2:3],v[2:3],v[170:171]
ds_read_b128 v[56:59],v245 offset:34816
v_pk_add_f32 v[4:5],v[4:5],v[172:173]
v_pk_add_f32 v[6:7],v[6:7],v[174:175]
ds_read_b128 v[60:63],v245 offset:35840
v_pk_add_f32 v[8:9],v[8:9],v[176:177]
v_pk_add_f32 v[10:11],v[10:11],v[178:179]
v_pk_add_f32 v[12:13],v[12:13],v[180:181]
ds_read_b128 v[64:67],v247
v_pk_add_f32 v[14:15],v[14:15],v[182:183]
v_cvt_pk_bf16_f32 v0,v0,v1
ds_read_b128 v[68:71],v247 offset:1024
v_cvt_pk_bf16_f32 v1,v2,v3
v_cvt_pk_bf16_f32 v2,v4,v5
ds_read_b128 v[72:75],v247 offset:2048
v_cvt_pk_bf16_f32 v3,v6,v7
v_cvt_pk_bf16_f32 v4,v8,v9
ds_read_b128 v[76:79],v247 offset:3072
v_cvt_pk_bf16_f32 v5,v10,v11
v_cvt_pk_bf16_f32 v6,v12,v13
ds_read_b128 v[80:83],v249 offset:16384
v_cvt_pk_bf16_f32 v7,v14,v15
v_permlane32_swap_b32 v0,v2
ds_read_b128 v[84:87],v249 offset:17408
v_permlane32_swap_b32 v1,v3
v_permlane32_swap_b32 v4,v6
ds_read_b128 v[88:91],v249 offset:18432
v_permlane32_swap_b32 v5,v7
v_permlane16_swap_b32 v0,v2
ds_read_b128 v[92:95],v249 offset:19456
v_permlane16_swap_b32 v1,v3
v_permlane16_swap_b32 v4,v6
v_permlane16_swap_b32 v5,v7
s_mov_b32 s66,0
buffer_store_dwordx4 v[0:3],v250,s[44:47],s66 offen sc0 sc1
s_mov_b32 s66,40960
buffer_store_dwordx4 v[4:7],v250,s[44:47],s66 offen sc0 sc1
s_waitcnt lgkmcnt(12)
s_barrier
ds_read_b128 v[96:99],v245 offset:49152
ds_read_b128 v[100:103],v245 offset:50176
ds_read_b128 v[104:107],v245 offset:51200
ds_read_b128 v[108:111],v245 offset:52224
s_waitcnt lgkmcnt(15)
v_pk_add_f32 v[16:17],v[16:17],v[48:49]
ds_read_b128 v[112:115],v246 offset:49152
v_pk_add_f32 v[18:19],v[18:19],v[50:51]
s_waitcnt lgkmcnt(15)
v_pk_add_f32 v[20:21],v[20:21],v[52:53]
v_pk_add_f32 v[22:23],v[22:23],v[54:55]
s_waitcnt lgkmcnt(14)
v_pk_add_f32 v[24:25],v[24:25],v[56:57]
v_pk_add_f32 v[26:27],v[26:27],v[58:59]
ds_read_b128 v[116:119],v246 offset:50176
s_waitcnt lgkmcnt(14)
v_pk_add_f32 v[28:29],v[28:29],v[60:61]
v_pk_add_f32 v[30:31],v[30:31],v[62:63]
s_waitcnt lgkmcnt(9)
v_pk_add_f32 v[64:65],v[64:65],v[80:81]
v_pk_add_f32 v[66:67],v[66:67],v[82:83]
s_waitcnt lgkmcnt(8)
v_pk_add_f32 v[68:69],v[68:69],v[84:85]
ds_read_b128 v[120:123],v246 offset:51200
v_pk_add_f32 v[70:71],v[70:71],v[86:87]
s_waitcnt lgkmcnt(8)
v_pk_add_f32 v[72:73],v[72:73],v[88:89]
v_pk_add_f32 v[74:75],v[74:75],v[90:91]
s_waitcnt lgkmcnt(7)
v_pk_add_f32 v[76:77],v[76:77],v[92:93]
v_pk_add_f32 v[78:79],v[78:79],v[94:95]
ds_read_b128 v[124:127],v246 offset:52224
v_pk_add_f32 v[16:17],v[16:17],v[64:65]
v_pk_add_f32 v[18:19],v[18:19],v[66:67]
v_pk_add_f32 v[20:21],v[20:21],v[68:69]
v_pk_add_f32 v[22:23],v[22:23],v[70:71]
ds_read_b128 v[128:131],v248
v_pk_add_f32 v[24:25],v[24:25],v[72:73]
v_pk_add_f32 v[26:27],v[26:27],v[74:75]
v_pk_add_f32 v[28:29],v[28:29],v[76:77]
v_pk_add_f32 v[30:31],v[30:31],v[78:79]
v_cvt_pk_bf16_f32 v16,v16,v17
ds_read_b128 v[132:135],v248 offset:1024
v_cvt_pk_bf16_f32 v17,v18,v19
v_cvt_pk_bf16_f32 v18,v20,v21
v_cvt_pk_bf16_f32 v19,v22,v23
v_cvt_pk_bf16_f32 v20,v24,v25
v_cvt_pk_bf16_f32 v21,v26,v27
ds_read_b128 v[136:139],v248 offset:2048
v_cvt_pk_bf16_f32 v22,v28,v29
v_cvt_pk_bf16_f32 v23,v30,v31
v_permlane32_swap_b32 v16,v18
v_permlane32_swap_b32 v17,v19
v_permlane32_swap_b32 v20,v22
ds_read_b128 v[140:143],v248 offset:3072
v_permlane32_swap_b32 v21,v23
v_permlane16_swap_b32 v16,v18
v_permlane16_swap_b32 v17,v19
v_permlane16_swap_b32 v20,v22
v_permlane16_swap_b32 v21,v23
s_mov_b32 s66,81920
buffer_store_dwordx4 v[16:19],v250,s[44:47],s66 offen sc0 sc1
s_mov_b32 s66,122880
buffer_store_dwordx4 v[20:23],v250,s[44:47],s66 offen sc0 sc1
s_waitcnt lgkmcnt(11)
v_pk_add_f32 v[32:33],v[32:33],v[96:97]
v_pk_add_f32 v[34:35],v[34:35],v[98:99]
s_waitcnt lgkmcnt(10)
v_pk_add_f32 v[36:37],v[36:37],v[100:101]
v_pk_add_f32 v[38:39],v[38:39],v[102:103]
s_waitcnt lgkmcnt(9)
v_pk_add_f32 v[40:41],v[40:41],v[104:105]
v_pk_add_f32 v[42:43],v[42:43],v[106:107]
s_waitcnt lgkmcnt(8)
v_pk_add_f32 v[44:45],v[44:45],v[108:109]
v_pk_add_f32 v[46:47],v[46:47],v[110:111]
s_waitcnt lgkmcnt(3)
v_pk_add_f32 v[112:113],v[112:113],v[128:129]
v_pk_add_f32 v[114:115],v[114:115],v[130:131]
s_waitcnt lgkmcnt(2)
v_pk_add_f32 v[116:117],v[116:117],v[132:133]
v_pk_add_f32 v[118:119],v[118:119],v[134:135]
s_waitcnt lgkmcnt(1)
v_pk_add_f32 v[120:121],v[120:121],v[136:137]
v_pk_add_f32 v[122:123],v[122:123],v[138:139]
s_waitcnt lgkmcnt(0)
v_pk_add_f32 v[124:125],v[124:125],v[140:141]
v_pk_add_f32 v[126:127],v[126:127],v[142:143]
v_pk_add_f32 v[32:33],v[32:33],v[112:113]
v_pk_add_f32 v[34:35],v[34:35],v[114:115]
v_pk_add_f32 v[36:37],v[36:37],v[116:117]
v_pk_add_f32 v[38:39],v[38:39],v[118:119]
v_pk_add_f32 v[40:41],v[40:41],v[120:121]
v_pk_add_f32 v[42:43],v[42:43],v[122:123]
v_pk_add_f32 v[44:45],v[44:45],v[124:125]
v_pk_add_f32 v[46:47],v[46:47],v[126:127]
v_cvt_pk_bf16_f32 v32,v32,v33
v_cvt_pk_bf16_f32 v33,v34,v35
v_cvt_pk_bf16_f32 v34,v36,v37
v_cvt_pk_bf16_f32 v35,v38,v39
v_cvt_pk_bf16_f32 v36,v40,v41
v_cvt_pk_bf16_f32 v37,v42,v43
v_cvt_pk_bf16_f32 v38,v44,v45
v_cvt_pk_bf16_f32 v39,v46,v47
v_permlane32_swap_b32 v32,v34
v_permlane32_swap_b32 v33,v35
v_permlane32_swap_b32 v36,v38
v_permlane32_swap_b32 v37,v39
v_permlane16_swap_b32 v32,v34
v_permlane16_swap_b32 v33,v35
v_permlane16_swap_b32 v36,v38
v_permlane16_swap_b32 v37,v39
s_mov_b32 s66,163840
buffer_store_dwordx4 v[32:35],v250,s[44:47],s66 offen sc0 sc1
s_mov_b32 s66,204800
buffer_store_dwordx4 v[36:39],v250,s[44:47],s66 offen sc0 sc1
s_branch .Lend
.Lspb16:
s_mov_b32 s70,65536
.Lsqb16:
v_and_b32 v255,0x380,v251
buffer_load_dword v253,v255,s[40:43],s69 offen sc1
s_waitcnt vmcnt(0)
s_waitcnt vmcnt(0)
v_cmp_eq_u32 s[72:73],s59,v253
s_nop 0
s_bitcmp1_b32 s72,4
s_cbranch_scc1 .Lsbb16
s_sleep 4
s_sub_u32 s70,s70,1
s_cmp_lg_u32 s70,0
s_cbranch_scc1 .Lsqb16
s_branch .Lsbb16
.Lspb20:
s_mov_b32 s70,65536
.Lsqb20:
v_and_b32 v255,0x380,v251
buffer_load_dword v253,v255,s[40:43],s69 offen sc1
s_waitcnt vmcnt(0)
s_waitcnt vmcnt(0)
v_cmp_eq_u32 s[72:73],s59,v253
s_nop 0
s_bitcmp1_b32 s72,5
s_cbranch_scc1 .Lsbb20
s_sleep 4
s_sub_u32 s70,s70,1
s_cmp_lg_u32 s70,0
s_cbranch_scc1 .Lsqb20
s_branch .Lsbb20
.Lspb24:
s_mov_b32 s70,65536
.Lsqb24:
v_and_b32 v255,0x380,v251
buffer_load_dword v253,v255,s[40:43],s69 offen sc1
s_waitcnt vmcnt(0)
s_waitcnt vmcnt(0)
v_cmp_eq_u32 s[72:73],s59,v253
s_nop 0
s_bitcmp1_b32 s72,6
s_cbranch_scc1 .Lsbb24
)A"
R"A(s_sleep 4
s_sub_u32 s70,s70,1
s_cmp_lg_u32 s70,0
s_cbranch_scc1 .Lsqb24
s_branch .Lsbb24
.Lspb28:
s_mov_b32 s70,65536
.Lsqb28:
v_and_b32 v255,0x380,v251
buffer_load_dword v253,v255,s[40:43],s69 offen sc1
s_waitcnt vmcnt(0)
s_waitcnt vmcnt(0)
v_cmp_eq_u32 s[72:73],s59,v253
s_nop 0
s_bitcmp1_b32 s72,7
s_cbranch_scc1 .Lsbb28
s_sleep 4
s_sub_u32 s70,s70,1
s_cmp_lg_u32 s70,0
s_cbranch_scc1 .Lsqb28
s_branch .Lsbb28
.Lep0:
v_lshrrev_b32 v104,8,v239
v_lshlrev_b32 v104,12,v104
v_bfe_u32 v105,v239,6,2
v_lshl_or_b32 v104,v105,10,v104
v_bfe_u32 v105,v239,4,2
v_lshl_or_b32 v104,v105,7,v104
s_mov_b32 s70,65536
.Lep:
s_mov_b32 s72,0
buffer_load_dword v108,v104,s[40:43],s72 offen sc1
s_mov_b32 s72,16384
buffer_load_dword v109,v104,s[40:43],s72 offen sc1
s_mov_b32 s72,32768
buffer_load_dword v110,v104,s[40:43],s72 offen sc1
s_mov_b32 s72,49152
buffer_load_dword v111,v104,s[40:43],s72 offen sc1
s_waitcnt vmcnt(3)
v_xor_b32 v108,s59,v108
s_waitcnt vmcnt(2)
v_xor_b32 v109,s59,v109
s_waitcnt vmcnt(1)
v_xor_b32 v110,s59,v110
s_waitcnt vmcnt(0)
v_xor_b32 v111,s59,v111
v_or_b32 v108,v108,v109
v_or_b32 v110,v110,v111
v_or_b32 v108,v108,v110
v_cmp_ne_u32 vcc,0,v108
s_nop 0
s_cbranch_vccz .Lepok
s_sleep 8
s_sub_u32 s70,s70,1
s_cmp_lg_u32 s70,0
s_cbranch_scc1 .Lep
.Lepok:
s_mov_b32 s68,65536
v_lshlrev_b32 v104,13,v239
v_mov_b32 v105,s59
buffer_store_dword v105,v104,s[40:43],s68 offen sc1
s_branch .Lepb
.Lidle:
s_waitcnt vmcnt(0) lgkmcnt(0)
s_branch .Lend
.Lst1:
s_call_b64 s[70:71],.Lrare1
s_branch .Lbk1
.Lst2:
s_call_b64 s[70:71],.Lrare0
s_branch .Lbk2
.Lst3:
s_call_b64 s[70:71],.Lrare1
s_branch .Lbk3
.Lst4:
s_call_b64 s[70:71],.Lrare0
s_branch .Lbk4
.Lst5:
s_call_b64 s[70:71],.Lrare1
s_branch .Lbk5
.Lst6:
s_call_b64 s[70:71],.Lrare0
s_branch .Lbk6
.Lst7:
s_call_b64 s[70:71],.Lrare1
s_branch .Lbk7
.Lst8:
s_call_b64 s[70:71],.Lrare0
s_branch .Lbk8
.Lst9:
s_call_b64 s[70:71],.Lrare1
s_branch .Lbk9
.Lst10:
s_call_b64 s[70:71],.Lrare0
s_branch .Lbk10
.Lst11:
s_call_b64 s[70:71],.Lrare1
s_branch .Lbk11
.Lst12:
s_call_b64 s[70:71],.Lrare0
s_branch .Lbk12
.Lst13:
s_call_b64 s[70:71],.Lrare1
s_branch .Lbk13
.Lst14:
s_call_b64 s[70:71],.Lrare0
s_branch .Lbk14
.Lst15:
s_call_b64 s[70:71],.Lrare2
s_branch .Lbk15
.Lst16:
s_call_b64 s[70:71],.Lrare1
s_branch .Lbk16
.Lst17:
s_call_b64 s[70:71],.Lrare0
s_branch .Lbk17
.Lst18:
s_call_b64 s[70:71],.Lrare2
s_branch .Lbk18
.Lspa:
s_mov_b32 s70,65536
.Lsqa:
v_and_b32 v255,0x380,v251
buffer_load_dword v253,v255,s[40:43],s69 offen sc1
s_waitcnt vmcnt(0)
s_waitcnt vmcnt(0)
v_cmp_eq_u32 s[72:73],s59,v253
s_nop 0
s_and_b32 s72,s72,15
s_cmp_eq_u32 s72,15
s_cbranch_scc1 .Lsba
s_sleep 4
s_sub_u32 s70,s70,1
s_cmp_lg_u32 s70,0
s_cbranch_scc1 .Lsqa
s_branch .Lsba
.Lrare0:
s_nop 15
v_max3_f32 v228,v0,v1,v2
v_max3_f32 v228,v228,v3,v16
v_max3_f32 v228,v228,v17,v18
v_max_f32 v228,v228,v19
v_mov_b32 v218,v228
s_nop 1
v_permlane32_swap_b32 v218,v228
v_max_f32 v228,v228,v218
v_mov_b32 v218,v228
s_nop 1
v_permlane16_swap_b32 v218,v228
v_max_f32 v228,v228,v218
v_max_f32 v228,0,v228
v_exp_f32 v217,-v228
v_sub_f32 v80,v80,v228
v_sub_f32 v32,v32,v228
v_sub_f32 v48,v48,v228
v_sub_f32 v0,v0,v228
v_sub_f32 v16,v16,v228
v_sub_f32 v81,v81,v228
v_sub_f32 v33,v33,v228
v_sub_f32 v49,v49,v228
v_sub_f32 v1,v1,v228
v_sub_f32 v17,v17,v228
v_sub_f32 v82,v82,v228
v_sub_f32 v34,v34,v228
v_sub_f32 v50,v50,v228
v_sub_f32 v2,v2,v228
v_sub_f32 v18,v18,v228
v_sub_f32 v83,v83,v228
v_sub_f32 v35,v35,v228
v_sub_f32 v51,v51,v228
v_sub_f32 v3,v3,v228
v_sub_f32 v19,v19,v228
v_mul_f32 v104,v217,v104
v_accvgpr_read_b32 v218,a0
v_accvgpr_read_b32 v219,a1
v_mul_f32 v218,v217,v218
v_mul_f32 v219,v217,v219
v_accvgpr_write_b32 a0,v218
v_accvgpr_write_b32 a1,v219
v_accvgpr_read_b32 v218,a2
v_accvgpr_read_b32 v219,a3
v_mul_f32 v218,v217,v218
v_mul_f32 v219,v217,v219
v_accvgpr_write_b32 a2,v218
v_accvgpr_write_b32 a3,v219
v_accvgpr_read_b32 v218,a4
v_accvgpr_read_b32 v219,a5
v_mul_f32 v218,v217,v218
v_mul_f32 v219,v217,v219
v_accvgpr_write_b32 a4,v218
v_accvgpr_write_b32 a5,v219
v_accvgpr_read_b32 v218,a6
v_accvgpr_read_b32 v219,a7
v_mul_f32 v218,v217,v218
v_mul_f32 v219,v217,v219
v_accvgpr_write_b32 a6,v218
v_accvgpr_write_b32 a7,v219
v_accvgpr_read_b32 v218,a8
v_accvgpr_read_b32 v219,a9
v_mul_f32 v218,v217,v218
v_mul_f32 v219,v217,v219
v_accvgpr_write_b32 a8,v218
v_accvgpr_write_b32 a9,v219
v_accvgpr_read_b32 v218,a10
v_accvgpr_read_b32 v219,a11
v_mul_f32 v218,v217,v218
v_mul_f32 v219,v217,v219
v_accvgpr_write_b32 a10,v218
v_accvgpr_write_b32 a11,v219
v_accvgpr_read_b32 v218,a12
v_accvgpr_read_b32 v219,a13
v_mul_f32 v218,v217,v218
v_mul_f32 v219,v217,v219
v_accvgpr_write_b32 a12,v218
v_accvgpr_write_b32 a13,v219
v_accvgpr_read_b32 v218,a14
v_accvgpr_read_b32 v219,a15
v_mul_f32 v218,v217,v218
v_mul_f32 v219,v217,v219
v_accvgpr_write_b32 a14,v218
v_accvgpr_write_b32 a15,v219
v_accvgpr_read_b32 v218,a16
v_accvgpr_read_b32 v219,a17
v_mul_f32 v218,v217,v218
v_mul_f32 v219,v217,v219
v_accvgpr_write_b32 a16,v218
v_accvgpr_write_b32 a17,v219
v_accvgpr_read_b32 v218,a18
v_accvgpr_read_b32 v219,a19
v_mul_f32 v218,v217,v218
v_mul_f32 v219,v217,v219
v_accvgpr_write_b32 a18,v218
v_accvgpr_write_b32 a19,v219
v_accvgpr_read_b32 v218,a20
v_accvgpr_read_b32 v219,a21
v_mul_f32 v218,v217,v218
v_mul_f32 v219,v217,v219
v_accvgpr_write_b32 a20,v218
v_accvgpr_write_b32 a21,v219
v_accvgpr_read_b32 v218,a22
v_accvgpr_read_b32 v219,a23
v_mul_f32 v218,v217,v218
v_mul_f32 v219,v217,v219
v_accvgpr_write_b32 a22,v218
v_accvgpr_write_b32 a23,v219
v_accvgpr_read_b32 v218,a24
v_accvgpr_read_b32 v219,a25
v_mul_f32 v218,v217,v218
v_mul_f32 v219,v217,v219
v_accvgpr_write_b32 a24,v218
v_accvgpr_write_b32 a25,v219
v_accvgpr_read_b32 v218,a26
v_accvgpr_read_b32 v219,a27
v_mul_f32 v218,v217,v218
v_mul_f32 v219,v217,v219
v_accvgpr_write_b32 a26,v218
v_accvgpr_write_b32 a27,v219
v_accvgpr_read_b32 v218,a28
v_accvgpr_read_b32 v219,a29
v_mul_f32 v218,v217,v218
v_mul_f32 v219,v217,v219
v_accvgpr_write_b32 a28,v218
v_accvgpr_write_b32 a29,v219
v_accvgpr_read_b32 v218,a30
v_accvgpr_read_b32 v219,a31
v_mul_f32 v218,v217,v218
v_mul_f32 v219,v217,v219
v_accvgpr_write_b32 a30,v218
v_accvgpr_write_b32 a31,v219
v_max3_f32 v228,v4,v5,v6
v_max3_f32 v228,v228,v7,v20
v_max3_f32 v228,v228,v21,v22
v_max_f32 v228,v228,v23
v_mov_b32 v218,v228
s_nop 1
v_permlane32_swap_b32 v218,v228
v_max_f32 v228,v228,v218
v_mov_b32 v218,v228
s_nop 1
v_permlane16_swap_b32 v218,v228
v_max_f32 v228,v228,v218
v_max_f32 v228,0,v228
v_exp_f32 v217,-v228
v_sub_f32 v84,v84,v228
v_sub_f32 v36,v36,v228
v_sub_f32 v52,v52,v228
v_sub_f32 v4,v4,v228
v_sub_f32 v20,v20,v228
v_sub_f32 v85,v85,v228
v_sub_f32 v37,v37,v228
v_sub_f32 v53,v53,v228
v_sub_f32 v5,v5,v228
v_sub_f32 v21,v21,v228
v_sub_f32 v86,v86,v228
v_sub_f32 v38,v38,v228
v_sub_f32 v54,v54,v228
v_sub_f32 v6,v6,v228
v_sub_f32 v22,v22,v228
v_sub_f32 v87,v87,v228
v_sub_f32 v39,v39,v228
v_sub_f32 v55,v55,v228
v_sub_f32 v7,v7,v228
v_sub_f32 v23,v23,v228
v_mul_f32 v105,v217,v105
v_accvgpr_read_b32 v218,a32
v_accvgpr_read_b32 v219,a33
v_mul_f32 v218,v217,v218
v_mul_f32 v219,v217,v219
v_accvgpr_write_b32 a32,v218
v_accvgpr_write_b32 a33,v219
v_accvgpr_read_b32 v218,a34
v_accvgpr_read_b32 v219,a35
v_mul_f32 v218,v217,v218
v_mul_f32 v219,v217,v219
v_accvgpr_write_b32 a34,v218
v_accvgpr_write_b32 a35,v219
v_accvgpr_read_b32 v218,a36
v_accvgpr_read_b32 v219,a37
v_mul_f32 v218,v217,v218
v_mul_f32 v219,v217,v219
v_accvgpr_write_b32 a36,v218
v_accvgpr_write_b32 a37,v219
v_accvgpr_read_b32 v218,a38
v_accvgpr_read_b32 v219,a39
v_mul_f32 v218,v217,v218
v_mul_f32 v219,v217,v219
v_accvgpr_write_b32 a38,v218
v_accvgpr_write_b32 a39,v219
v_accvgpr_read_b32 v218,a40
v_accvgpr_read_b32 v219,a41
v_mul_f32 v218,v217,v218
v_mul_f32 v219,v217,v219
v_accvgpr_write_b32 a40,v218
v_accvgpr_write_b32 a41,v219
v_accvgpr_read_b32 v218,a42
v_accvgpr_read_b32 v219,a43
v_mul_f32 v218,v217,v218
v_mul_f32 v219,v217,v219
v_accvgpr_write_b32 a42,v218
v_accvgpr_write_b32 a43,v219
v_accvgpr_read_b32 v218,a44
v_accvgpr_read_b32 v219,a45
v_mul_f32 v218,v217,v218
v_mul_f32 v219,v217,v219
v_accvgpr_write_b32 a44,v218
v_accvgpr_write_b32 a45,v219
v_accvgpr_read_b32 v218,a46
v_accvgpr_read_b32 v219,a47
v_mul_f32 v218,v217,v218
v_mul_f32 v219,v217,v219
v_accvgpr_write_b32 a46,v218
v_accvgpr_write_b32 a47,v219
v_accvgpr_read_b32 v218,a48
v_accvgpr_read_b32 v219,a49
v_mul_f32 v218,v217,v218
v_mul_f32 v219,v217,v219
v_accvgpr_write_b32 a48,v218
v_accvgpr_write_b32 a49,v219
v_accvgpr_read_b32 v218,a50
v_accvgpr_read_b32 v219,a51
v_mul_f32 v218,v217,v218
v_mul_f32 v219,v217,v219
v_accvgpr_write_b32 a50,v218
v_accvgpr_write_b32 a51,v219
v_accvgpr_read_b32 v218,a52
v_accvgpr_read_b32 v219,a53
v_mul_f32 v218,v217,v218
v_mul_f32 v219,v217,v219
v_accvgpr_write_b32 a52,v218
v_accvgpr_write_b32 a53,v219
v_accvgpr_read_b32 v218,a54
v_accvgpr_read_b32 v219,a55
v_mul_f32 v218,v217,v218
v_mul_f32 v219,v217,v219
v_accvgpr_write_b32 a54,v218
v_accvgpr_write_b32 a55,v219
v_accvgpr_read_b32 v218,a56
v_accvgpr_read_b32 v219,a57
v_mul_f32 v218,v217,v218
v_mul_f32 v219,v217,v219
v_accvgpr_write_b32 a56,v218
v_accvgpr_write_b32 a57,v219
v_accvgpr_read_b32 v218,a58
v_accvgpr_read_b32 v219,a59
v_mul_f32 v218,v217,v218
v_mul_f32 v219,v217,v219
v_accvgpr_write_b32 a58,v218
v_accvgpr_write_b32 a59,v219
v_accvgpr_read_b32 v218,a60
v_accvgpr_read_b32 v219,a61
v_mul_f32 v218,v217,v218
v_mul_f32 v219,v217,v219
v_accvgpr_write_b32 a60,v218
v_accvgpr_write_b32 a61,v219
v_accvgpr_read_b32 v218,a62
v_accvgpr_read_b32 v219,a63
v_mul_f32 v218,v217,v218
v_mul_f32 v219,v217,v219
v_accvgpr_write_b32 a62,v218
v_accvgpr_write_b32 a63,v219
v_max3_f32 v228,v8,v9,v10
v_max3_f32 v228,v228,v11,v24
v_max3_f32 v228,v228,v25,v26
v_max_f32 v228,v228,v27
v_mov_b32 v218,v228
s_nop 1
v_permlane32_swap_b32 v218,v228
v_max_f32 v228,v228,v218
v_mov_b32 v218,v228
s_nop 1
v_permlane16_swap_b32 v218,v228
v_max_f32 v228,v228,v218
v_max_f32 v228,0,v228
v_exp_f32 v217,-v228
v_sub_f32 v88,v88,v228
v_sub_f32 v40,v40,v228
v_sub_f32 v56,v56,v228
v_sub_f32 v8,v8,v228
v_sub_f32 v24,v24,v228
v_sub_f32 v89,v89,v228
v_sub_f32 v41,v41,v228
v_sub_f32 v57,v57,v228
v_sub_f32 v9,v9,v228
v_sub_f32 v25,v25,v228
v_sub_f32 v90,v90,v228
v_sub_f32 v42,v42,v228
v_sub_f32 v58,v58,v228
v_sub_f32 v10,v10,v228
v_sub_f32 v26,v26,v228
v_sub_f32 v91,v91,v228
v_sub_f32 v43,v43,v228
v_sub_f32 v59,v59,v228
v_sub_f32 v11,v11,v228
v_sub_f32 v27,v27,v228
v_mul_f32 v106,v217,v106
v_accvgpr_read_b32 v218,a64
v_accvgpr_read_b32 v219,a65
v_mul_f32 v218,v217,v218
v_mul_f32 v219,v217,v219
v_accvgpr_write_b32 a64,v218
v_accvgpr_write_b32 a65,v219
v_accvgpr_read_b32 v218,a66
v_accvgpr_read_b32 v219,a67
v_mul_f32 v218,v217,v218
v_mul_f32 v219,v217,v219
v_accvgpr_write_b32 a66,v218
v_accvgpr_write_b32 a67,v219
v_accvgpr_read_b32 v218,a68
v_accvgpr_read_b32 v219,a69
v_mul_f32 v218,v217,v218
v_mul_f32 v219,v217,v219
v_accvgpr_write_b32 a68,v218
v_accvgpr_write_b32 a69,v219
v_accvgpr_read_b32 v218,a70
v_accvgpr_read_b32 v219,a71
v_mul_f32 v218,v217,v218
v_mul_f32 v219,v217,v219
v_accvgpr_write_b32 a70,v218
v_accvgpr_write_b32 a71,v219
v_accvgpr_read_b32 v218,a72
v_accvgpr_read_b32 v219,a73
v_mul_f32 v218,v217,v218
v_mul_f32 v219,v217,v219
v_accvgpr_write_b32 a72,v218
v_accvgpr_write_b32 a73,v219
v_accvgpr_read_b32 v218,a74
v_accvgpr_read_b32 v219,a75
v_mul_f32 v218,v217,v218
v_mul_f32 v219,v217,v219
v_accvgpr_write_b32 a74,v218
v_accvgpr_write_b32 a75,v219
v_accvgpr_read_b32 v218,a76
v_accvgpr_read_b32 v219,a77
v_mul_f32 v218,v217,v218
v_mul_f32 v219,v217,v219
v_accvgpr_write_b32 a76,v218
v_accvgpr_write_b32 a77,v219
v_accvgpr_read_b32 v218,a78
v_accvgpr_read_b32 v219,a79
v_mul_f32 v218,v217,v218
v_mul_f32 v219,v217,v219
v_accvgpr_write_b32 a78,v218
v_accvgpr_write_b32 a79,v219
v_accvgpr_read_b32 v218,a80
v_accvgpr_read_b32 v219,a81
v_mul_f32 v218,v217,v218
v_mul_f32 v219,v217,v219
v_accvgpr_write_b32 a80,v218
v_accvgpr_write_b32 a81,v219
v_accvgpr_read_b32 v218,a82
v_accvgpr_read_b32 v219,a83
v_mul_f32 v218,v217,v218
v_mul_f32 v219,v217,v219
v_accvgpr_write_b32 a82,v218
v_accvgpr_write_b32 a83,v219
v_accvgpr_read_b32 v218,a84
)A"
R"A(v_accvgpr_read_b32 v219,a85
v_mul_f32 v218,v217,v218
v_mul_f32 v219,v217,v219
v_accvgpr_write_b32 a84,v218
v_accvgpr_write_b32 a85,v219
v_accvgpr_read_b32 v218,a86
v_accvgpr_read_b32 v219,a87
v_mul_f32 v218,v217,v218
v_mul_f32 v219,v217,v219
v_accvgpr_write_b32 a86,v218
v_accvgpr_write_b32 a87,v219
v_accvgpr_read_b32 v218,a88
v_accvgpr_read_b32 v219,a89
v_mul_f32 v218,v217,v218
v_mul_f32 v219,v217,v219
v_accvgpr_write_b32 a88,v218
v_accvgpr_write_b32 a89,v219
v_accvgpr_read_b32 v218,a90
v_accvgpr_read_b32 v219,a91
v_mul_f32 v218,v217,v218
v_mul_f32 v219,v217,v219
v_accvgpr_write_b32 a90,v218
v_accvgpr_write_b32 a91,v219
v_accvgpr_read_b32 v218,a92
v_accvgpr_read_b32 v219,a93
v_mul_f32 v218,v217,v218
v_mul_f32 v219,v217,v219
v_accvgpr_write_b32 a92,v218
v_accvgpr_write_b32 a93,v219
v_accvgpr_read_b32 v218,a94
v_accvgpr_read_b32 v219,a95
v_mul_f32 v218,v217,v218
v_mul_f32 v219,v217,v219
v_accvgpr_write_b32 a94,v218
v_accvgpr_write_b32 a95,v219
v_max3_f32 v228,v12,v13,v14
v_max3_f32 v228,v228,v15,v28
v_max3_f32 v228,v228,v29,v30
v_max_f32 v228,v228,v31
v_mov_b32 v218,v228
s_nop 1
v_permlane32_swap_b32 v218,v228
v_max_f32 v228,v228,v218
v_mov_b32 v218,v228
s_nop 1
v_permlane16_swap_b32 v218,v228
v_max_f32 v228,v228,v218
v_max_f32 v228,0,v228
v_exp_f32 v217,-v228
v_sub_f32 v92,v92,v228
v_sub_f32 v44,v44,v228
v_sub_f32 v60,v60,v228
v_sub_f32 v12,v12,v228
v_sub_f32 v28,v28,v228
v_sub_f32 v93,v93,v228
v_sub_f32 v45,v45,v228
v_sub_f32 v61,v61,v228
v_sub_f32 v13,v13,v228
v_sub_f32 v29,v29,v228
v_sub_f32 v94,v94,v228
v_sub_f32 v46,v46,v228
v_sub_f32 v62,v62,v228
v_sub_f32 v14,v14,v228
v_sub_f32 v30,v30,v228
v_sub_f32 v95,v95,v228
v_sub_f32 v47,v47,v228
v_sub_f32 v63,v63,v228
v_sub_f32 v15,v15,v228
v_sub_f32 v31,v31,v228
v_mul_f32 v107,v217,v107
v_accvgpr_read_b32 v218,a96
v_accvgpr_read_b32 v219,a97
v_mul_f32 v218,v217,v218
v_mul_f32 v219,v217,v219
v_accvgpr_write_b32 a96,v218
v_accvgpr_write_b32 a97,v219
v_accvgpr_read_b32 v218,a98
v_accvgpr_read_b32 v219,a99
v_mul_f32 v218,v217,v218
v_mul_f32 v219,v217,v219
v_accvgpr_write_b32 a98,v218
v_accvgpr_write_b32 a99,v219
v_accvgpr_read_b32 v218,a100
v_accvgpr_read_b32 v219,a101
v_mul_f32 v218,v217,v218
v_mul_f32 v219,v217,v219
v_accvgpr_write_b32 a100,v218
v_accvgpr_write_b32 a101,v219
v_accvgpr_read_b32 v218,a102
v_accvgpr_read_b32 v219,a103
v_mul_f32 v218,v217,v218
v_mul_f32 v219,v217,v219
v_accvgpr_write_b32 a102,v218
v_accvgpr_write_b32 a103,v219
v_accvgpr_read_b32 v218,a104
v_accvgpr_read_b32 v219,a105
v_mul_f32 v218,v217,v218
v_mul_f32 v219,v217,v219
v_accvgpr_write_b32 a104,v218
v_accvgpr_write_b32 a105,v219
v_accvgpr_read_b32 v218,a106
v_accvgpr_read_b32 v219,a107
v_mul_f32 v218,v217,v218
v_mul_f32 v219,v217,v219
v_accvgpr_write_b32 a106,v218
v_accvgpr_write_b32 a107,v219
v_accvgpr_read_b32 v218,a108
v_accvgpr_read_b32 v219,a109
v_mul_f32 v218,v217,v218
v_mul_f32 v219,v217,v219
v_accvgpr_write_b32 a108,v218
v_accvgpr_write_b32 a109,v219
v_accvgpr_read_b32 v218,a110
v_accvgpr_read_b32 v219,a111
v_mul_f32 v218,v217,v218
v_mul_f32 v219,v217,v219
v_accvgpr_write_b32 a110,v218
v_accvgpr_write_b32 a111,v219
v_accvgpr_read_b32 v218,a112
v_accvgpr_read_b32 v219,a113
v_mul_f32 v218,v217,v218
v_mul_f32 v219,v217,v219
v_accvgpr_write_b32 a112,v218
v_accvgpr_write_b32 a113,v219
v_accvgpr_read_b32 v218,a114
v_accvgpr_read_b32 v219,a115
v_mul_f32 v218,v217,v218
v_mul_f32 v219,v217,v219
v_accvgpr_write_b32 a114,v218
v_accvgpr_write_b32 a115,v219
v_accvgpr_read_b32 v218,a116
v_accvgpr_read_b32 v219,a117
v_mul_f32 v218,v217,v218
v_mul_f32 v219,v217,v219
v_accvgpr_write_b32 a116,v218
v_accvgpr_write_b32 a117,v219
v_accvgpr_read_b32 v218,a118
v_accvgpr_read_b32 v219,a119
v_mul_f32 v218,v217,v218
v_mul_f32 v219,v217,v219
v_accvgpr_write_b32 a118,v218
v_accvgpr_write_b32 a119,v219
v_accvgpr_read_b32 v218,a120
v_accvgpr_read_b32 v219,a121
v_mul_f32 v218,v217,v218
v_mul_f32 v219,v217,v219
v_accvgpr_write_b32 a120,v218
v_accvgpr_write_b32 a121,v219
v_accvgpr_read_b32 v218,a122
v_accvgpr_read_b32 v219,a123
v_mul_f32 v218,v217,v218
v_mul_f32 v219,v217,v219
v_accvgpr_write_b32 a122,v218
v_accvgpr_write_b32 a123,v219
v_accvgpr_read_b32 v218,a124
v_accvgpr_read_b32 v219,a125
v_mul_f32 v218,v217,v218
v_mul_f32 v219,v217,v219
v_accvgpr_write_b32 a124,v218
v_accvgpr_write_b32 a125,v219
v_accvgpr_read_b32 v218,a126
v_accvgpr_read_b32 v219,a127
v_mul_f32 v218,v217,v218
v_mul_f32 v219,v217,v219
v_accvgpr_write_b32 a126,v218
v_accvgpr_write_b32 a127,v219
v_max3_f32 v228,v64,v65,v66
v_max3_f32 v228,v228,v67,v68
v_max3_f32 v228,v228,v69,v70
v_max_f32 v228,v228,v71
v_mov_b32 v218,v228
s_nop 1
v_permlane32_swap_b32 v218,v228
v_max_f32 v228,v228,v218
v_mov_b32 v218,v228
s_nop 1
v_permlane16_swap_b32 v218,v228
v_max_f32 v228,v228,v218
v_max_f32 v228,0,v228
v_exp_f32 v217,-v228
v_sub_f32 v96,v96,v228
v_sub_f32 v64,v64,v228
v_sub_f32 v68,v68,v228
v_sub_f32 v97,v97,v228
v_sub_f32 v65,v65,v228
v_sub_f32 v69,v69,v228
v_sub_f32 v98,v98,v228
v_sub_f32 v66,v66,v228
v_sub_f32 v70,v70,v228
v_sub_f32 v99,v99,v228
v_sub_f32 v67,v67,v228
v_sub_f32 v71,v71,v228
v_mul_f32 v108,v217,v108
v_accvgpr_read_b32 v218,a128
v_accvgpr_read_b32 v219,a129
v_mul_f32 v218,v217,v218
v_mul_f32 v219,v217,v219
v_accvgpr_write_b32 a128,v218
v_accvgpr_write_b32 a129,v219
v_accvgpr_read_b32 v218,a130
v_accvgpr_read_b32 v219,a131
v_mul_f32 v218,v217,v218
v_mul_f32 v219,v217,v219
v_accvgpr_write_b32 a130,v218
v_accvgpr_write_b32 a131,v219
v_accvgpr_read_b32 v218,a132
v_accvgpr_read_b32 v219,a133
v_mul_f32 v218,v217,v218
v_mul_f32 v219,v217,v219
v_accvgpr_write_b32 a132,v218
v_accvgpr_write_b32 a133,v219
v_accvgpr_read_b32 v218,a134
v_accvgpr_read_b32 v219,a135
v_mul_f32 v218,v217,v218
v_mul_f32 v219,v217,v219
v_accvgpr_write_b32 a134,v218
v_accvgpr_write_b32 a135,v219
v_accvgpr_read_b32 v218,a136
v_accvgpr_read_b32 v219,a137
v_mul_f32 v218,v217,v218
v_mul_f32 v219,v217,v219
v_accvgpr_write_b32 a136,v218
v_accvgpr_write_b32 a137,v219
v_accvgpr_read_b32 v218,a138
v_accvgpr_read_b32 v219,a139
v_mul_f32 v218,v217,v218
v_mul_f32 v219,v217,v219
v_accvgpr_write_b32 a138,v218
v_accvgpr_write_b32 a139,v219
v_accvgpr_read_b32 v218,a140
v_accvgpr_read_b32 v219,a141
v_mul_f32 v218,v217,v218
v_mul_f32 v219,v217,v219
v_accvgpr_write_b32 a140,v218
v_accvgpr_write_b32 a141,v219
v_accvgpr_read_b32 v218,a142
v_accvgpr_read_b32 v219,a143
v_mul_f32 v218,v217,v218
v_mul_f32 v219,v217,v219
v_accvgpr_write_b32 a142,v218
v_accvgpr_write_b32 a143,v219
v_accvgpr_read_b32 v218,a144
v_accvgpr_read_b32 v219,a145
v_mul_f32 v218,v217,v218
v_mul_f32 v219,v217,v219
v_accvgpr_write_b32 a144,v218
v_accvgpr_write_b32 a145,v219
v_accvgpr_read_b32 v218,a146
v_accvgpr_read_b32 v219,a147
v_mul_f32 v218,v217,v218
v_mul_f32 v219,v217,v219
v_accvgpr_write_b32 a146,v218
v_accvgpr_write_b32 a147,v219
v_accvgpr_read_b32 v218,a148
v_accvgpr_read_b32 v219,a149
v_mul_f32 v218,v217,v218
v_mul_f32 v219,v217,v219
v_accvgpr_write_b32 a148,v218
v_accvgpr_write_b32 a149,v219
v_accvgpr_read_b32 v218,a150
v_accvgpr_read_b32 v219,a151
v_mul_f32 v218,v217,v218
v_mul_f32 v219,v217,v219
v_accvgpr_write_b32 a150,v218
v_accvgpr_write_b32 a151,v219
v_accvgpr_read_b32 v218,a152
v_accvgpr_read_b32 v219,a153
v_mul_f32 v218,v217,v218
v_mul_f32 v219,v217,v219
v_accvgpr_write_b32 a152,v218
v_accvgpr_write_b32 a153,v219
v_accvgpr_read_b32 v218,a154
v_accvgpr_read_b32 v219,a155
v_mul_f32 v218,v217,v218
v_mul_f32 v219,v217,v219
v_accvgpr_write_b32 a154,v218
v_accvgpr_write_b32 a155,v219
v_accvgpr_read_b32 v218,a156
v_accvgpr_read_b32 v219,a157
v_mul_f32 v218,v217,v218
v_mul_f32 v219,v217,v219
v_accvgpr_write_b32 a156,v218
v_accvgpr_write_b32 a157,v219
v_accvgpr_read_b32 v218,a158
v_accvgpr_read_b32 v219,a159
v_mul_f32 v218,v217,v218
v_mul_f32 v219,v217,v219
v_accvgpr_write_b32 a158,v218
v_accvgpr_write_b32 a159,v219
v_exp_f32 v216,v0
v_exp_f32 v217,v4
v_exp_f32 v218,v1
v_exp_f32 v219,v5
v_exp_f32 v220,v2
v_exp_f32 v221,v6
v_add_f32 v72,v216,v218
v_cvt_pk_bf16_f32 v112,v216,v218
v_exp_f32 v222,v3
v_add_f32 v73,v217,v219
v_cvt_pk_bf16_f32 v116,v217,v219
v_exp_f32 v223,v7
v_add_f32 v72,v220,v72
v_exp_f32 v216,v8
v_add_f32 v73,v221,v73
v_exp_f32 v217,v12
v_add_f32 v72,v222,v72
v_cvt_pk_bf16_f32 v113,v220,v222
v_exp_f32 v218,v9
v_add_f32 v73,v223,v73
v_cvt_pk_bf16_f32 v117,v221,v223
v_exp_f32 v219,v13
v_exp_f32 v220,v10
v_exp_f32 v221,v14
v_add_f32 v74,v216,v218
v_cvt_pk_bf16_f32 v120,v216,v218
v_exp_f32 v222,v11
v_add_f32 v75,v217,v219
v_cvt_pk_bf16_f32 v124,v217,v219
v_exp_f32 v223,v15
v_add_f32 v74,v220,v74
v_exp_f32 v216,v64
v_add_f32 v75,v221,v75
v_exp_f32 v217,v65
v_add_f32 v74,v222,v74
v_cvt_pk_bf16_f32 v121,v220,v222
v_exp_f32 v218,v66
v_add_f32 v75,v223,v75
v_cvt_pk_bf16_f32 v125,v221,v223
v_exp_f32 v219,v67
v_exp_f32 v220,v16
v_add_f32 v76,v216,v217
v_cvt_pk_bf16_f32 v144,v216,v217
v_exp_f32 v221,v20
v_add_f32 v76,v218,v76
v_exp_f32 v222,v17
v_add_f32 v76,v219,v76
v_cvt_pk_bf16_f32 v145,v218,v219
v_exp_f32 v223,v21
v_add_f32 v72,v220,v72
v_exp_f32 v216,v18
v_add_f32 v73,v221,v73
v_exp_f32 v217,v22
v_add_f32 v72,v222,v72
v_cvt_pk_bf16_f32 v114,v220,v222
v_exp_f32 v218,v19
v_add_f32 v73,v223,v73
v_cvt_pk_bf16_f32 v118,v221,v223
v_exp_f32 v219,v23
v_add_f32 v72,v216,v72
v_exp_f32 v220,v24
v_add_f32 v73,v217,v73
v_exp_f32 v221,v28
v_add_f32 v72,v218,v72
v_cvt_pk_bf16_f32 v115,v216,v218
v_exp_f32 v222,v25
v_add_f32 v73,v219,v73
v_cvt_pk_bf16_f32 v119,v217,v219
v_exp_f32 v223,v29
v_add_f32 v74,v220,v74
v_exp_f32 v216,v26
v_add_f32 v75,v221,v75
v_exp_f32 v217,v30
v_add_f32 v74,v222,v74
v_cvt_pk_bf16_f32 v122,v220,v222
v_exp_f32 v218,v27
v_add_f32 v75,v223,v75
v_cvt_pk_bf16_f32 v126,v221,v223
v_exp_f32 v219,v31
v_add_f32 v74,v216,v74
v_exp_f32 v220,v68
v_add_f32 v75,v217,v75
v_exp_f32 v221,v69
v_add_f32 v74,v218,v74
v_cvt_pk_bf16_f32 v123,v216,v218
v_exp_f32 v222,v70
v_add_f32 v75,v219,v75
v_cvt_pk_bf16_f32 v127,v217,v219
v_exp_f32 v223,v71
v_add_f32 v76,v220,v76
v_add_f32 v76,v221,v76
v_cvt_pk_bf16_f32 v146,v220,v221
v_add_f32 v76,v222,v76
v_add_f32 v76,v223,v76
v_cvt_pk_bf16_f32 v147,v222,v223
s_nop 3
s_setpc_b64 s[70:71]
.Lrare1:
s_nop 15
v_max3_f32 v228,v32,v33,v34
v_max3_f32 v228,v228,v35,v48
v_max3_f32 v228,v228,v49,v50
v_max_f32 v228,v228,v51
v_mov_b32 v218,v228
s_nop 1
v_permlane32_swap_b32 v218,v228
v_max_f32 v228,v228,v218
v_mov_b32 v218,v228
s_nop 1
v_permlane16_swap_b32 v218,v228
v_max_f32 v228,v228,v218
v_max_f32 v228,0,v228
v_exp_f32 v217,-v228
v_sub_f32 v80,v80,v228
v_sub_f32 v0,v0,v228
v_sub_f32 v16,v16,v228
v_sub_f32 v32,v32,v228
v_sub_f32 v48,v48,v228
v_sub_f32 v81,v81,v228
v_sub_f32 v1,v1,v228
v_sub_f32 v17,v17,v228
v_sub_f32 v33,v33,v228
v_sub_f32 v49,v49,v228
v_sub_f32 v82,v82,v228
v_sub_f32 v2,v2,v228
v_sub_f32 v18,v18,v228
v_sub_f32 v34,v34,v228
v_sub_f32 v50,v50,v228
v_sub_f32 v83,v83,v228
v_sub_f32 v3,v3,v228
v_sub_f32 v19,v19,v228
v_sub_f32 v35,v35,v228
v_sub_f32 v51,v51,v228
v_mul_f32 v104,v217,v104
v_accvgpr_read_b32 v218,a0
v_accvgpr_read_b32 v219,a1
v_mul_f32 v218,v217,v218
v_mul_f32 v219,v217,v219
v_accvgpr_write_b32 a0,v218
v_accvgpr_write_b32 a1,v219
v_accvgpr_read_b32 v218,a2
v_accvgpr_read_b32 v219,a3
v_mul_f32 v218,v217,v218
v_mul_f32 v219,v217,v219
v_accvgpr_write_b32 a2,v218
v_accvgpr_write_b32 a3,v219
v_accvgpr_read_b32 v218,a4
v_accvgpr_read_b32 v219,a5
v_mul_f32 v218,v217,v218
v_mul_f32 v219,v217,v219
v_accvgpr_write_b32 a4,v218
v_accvgpr_write_b32 a5,v219
v_accvgpr_read_b32 v218,a6
v_accvgpr_read_b32 v219,a7
v_mul_f32 v218,v217,v218
v_mul_f32 v219,v217,v219
v_accvgpr_write_b32 a6,v218
v_accvgpr_write_b32 a7,v219
v_accvgpr_read_b32 v218,a8
v_accvgpr_read_b32 v219,a9
v_mul_f32 v218,v217,v218
v_mul_f32 v219,v217,v219
v_accvgpr_write_b32 a8,v218
v_accvgpr_write_b32 a9,v219
v_accvgpr_read_b32 v218,a10
v_accvgpr_read_b32 v219,a11
v_mul_f32 v218,v217,v218
v_mul_f32 v219,v217,v219
v_accvgpr_write_b32 a10,v218
v_accvgpr_write_b32 a11,v219
v_accvgpr_read_b32 v218,a12
v_accvgpr_read_b32 v219,a13
v_mul_f32 v218,v217,v218
v_mul_f32 v219,v217,v219
v_accvgpr_write_b32 a12,v218
v_accvgpr_write_b32 a13,v219
v_accvgpr_read_b32 v218,a14
v_accvgpr_read_b32 v219,a15
)A"
R"A(v_mul_f32 v218,v217,v218
v_mul_f32 v219,v217,v219
v_accvgpr_write_b32 a14,v218
v_accvgpr_write_b32 a15,v219
v_accvgpr_read_b32 v218,a16
v_accvgpr_read_b32 v219,a17
v_mul_f32 v218,v217,v218
v_mul_f32 v219,v217,v219
v_accvgpr_write_b32 a16,v218
v_accvgpr_write_b32 a17,v219
v_accvgpr_read_b32 v218,a18
v_accvgpr_read_b32 v219,a19
v_mul_f32 v218,v217,v218
v_mul_f32 v219,v217,v219
v_accvgpr_write_b32 a18,v218
v_accvgpr_write_b32 a19,v219
v_accvgpr_read_b32 v218,a20
v_accvgpr_read_b32 v219,a21
v_mul_f32 v218,v217,v218
v_mul_f32 v219,v217,v219
v_accvgpr_write_b32 a20,v218
v_accvgpr_write_b32 a21,v219
v_accvgpr_read_b32 v218,a22
v_accvgpr_read_b32 v219,a23
v_mul_f32 v218,v217,v218
v_mul_f32 v219,v217,v219
v_accvgpr_write_b32 a22,v218
v_accvgpr_write_b32 a23,v219
v_accvgpr_read_b32 v218,a24
v_accvgpr_read_b32 v219,a25
v_mul_f32 v218,v217,v218
v_mul_f32 v219,v217,v219
v_accvgpr_write_b32 a24,v218
v_accvgpr_write_b32 a25,v219
v_accvgpr_read_b32 v218,a26
v_accvgpr_read_b32 v219,a27
v_mul_f32 v218,v217,v218
v_mul_f32 v219,v217,v219
v_accvgpr_write_b32 a26,v218
v_accvgpr_write_b32 a27,v219
v_accvgpr_read_b32 v218,a28
v_accvgpr_read_b32 v219,a29
v_mul_f32 v218,v217,v218
v_mul_f32 v219,v217,v219
v_accvgpr_write_b32 a28,v218
v_accvgpr_write_b32 a29,v219
v_accvgpr_read_b32 v218,a30
v_accvgpr_read_b32 v219,a31
v_mul_f32 v218,v217,v218
v_mul_f32 v219,v217,v219
v_accvgpr_write_b32 a30,v218
v_accvgpr_write_b32 a31,v219
v_max3_f32 v228,v36,v37,v38
v_max3_f32 v228,v228,v39,v52
v_max3_f32 v228,v228,v53,v54
v_max_f32 v228,v228,v55
v_mov_b32 v218,v228
s_nop 1
v_permlane32_swap_b32 v218,v228
v_max_f32 v228,v228,v218
v_mov_b32 v218,v228
s_nop 1
v_permlane16_swap_b32 v218,v228
v_max_f32 v228,v228,v218
v_max_f32 v228,0,v228
v_exp_f32 v217,-v228
v_sub_f32 v84,v84,v228
v_sub_f32 v4,v4,v228
v_sub_f32 v20,v20,v228
v_sub_f32 v36,v36,v228
v_sub_f32 v52,v52,v228
v_sub_f32 v85,v85,v228
v_sub_f32 v5,v5,v228
v_sub_f32 v21,v21,v228
v_sub_f32 v37,v37,v228
v_sub_f32 v53,v53,v228
v_sub_f32 v86,v86,v228
v_sub_f32 v6,v6,v228
v_sub_f32 v22,v22,v228
v_sub_f32 v38,v38,v228
v_sub_f32 v54,v54,v228
v_sub_f32 v87,v87,v228
v_sub_f32 v7,v7,v228
v_sub_f32 v23,v23,v228
v_sub_f32 v39,v39,v228
v_sub_f32 v55,v55,v228
v_mul_f32 v105,v217,v105
v_accvgpr_read_b32 v218,a32
v_accvgpr_read_b32 v219,a33
v_mul_f32 v218,v217,v218
v_mul_f32 v219,v217,v219
v_accvgpr_write_b32 a32,v218
v_accvgpr_write_b32 a33,v219
v_accvgpr_read_b32 v218,a34
v_accvgpr_read_b32 v219,a35
v_mul_f32 v218,v217,v218
v_mul_f32 v219,v217,v219
v_accvgpr_write_b32 a34,v218
v_accvgpr_write_b32 a35,v219
v_accvgpr_read_b32 v218,a36
v_accvgpr_read_b32 v219,a37
v_mul_f32 v218,v217,v218
v_mul_f32 v219,v217,v219
v_accvgpr_write_b32 a36,v218
v_accvgpr_write_b32 a37,v219
v_accvgpr_read_b32 v218,a38
v_accvgpr_read_b32 v219,a39
v_mul_f32 v218,v217,v218
v_mul_f32 v219,v217,v219
v_accvgpr_write_b32 a38,v218
v_accvgpr_write_b32 a39,v219
v_accvgpr_read_b32 v218,a40
v_accvgpr_read_b32 v219,a41
v_mul_f32 v218,v217,v218
v_mul_f32 v219,v217,v219
v_accvgpr_write_b32 a40,v218
v_accvgpr_write_b32 a41,v219
v_accvgpr_read_b32 v218,a42
v_accvgpr_read_b32 v219,a43
v_mul_f32 v218,v217,v218
v_mul_f32 v219,v217,v219
v_accvgpr_write_b32 a42,v218
v_accvgpr_write_b32 a43,v219
v_accvgpr_read_b32 v218,a44
v_accvgpr_read_b32 v219,a45
v_mul_f32 v218,v217,v218
v_mul_f32 v219,v217,v219
v_accvgpr_write_b32 a44,v218
v_accvgpr_write_b32 a45,v219
v_accvgpr_read_b32 v218,a46
v_accvgpr_read_b32 v219,a47
v_mul_f32 v218,v217,v218
v_mul_f32 v219,v217,v219
v_accvgpr_write_b32 a46,v218
v_accvgpr_write_b32 a47,v219
v_accvgpr_read_b32 v218,a48
v_accvgpr_read_b32 v219,a49
v_mul_f32 v218,v217,v218
v_mul_f32 v219,v217,v219
v_accvgpr_write_b32 a48,v218
v_accvgpr_write_b32 a49,v219
v_accvgpr_read_b32 v218,a50
v_accvgpr_read_b32 v219,a51
v_mul_f32 v218,v217,v218
v_mul_f32 v219,v217,v219
v_accvgpr_write_b32 a50,v218
v_accvgpr_write_b32 a51,v219
v_accvgpr_read_b32 v218,a52
v_accvgpr_read_b32 v219,a53
v_mul_f32 v218,v217,v218
v_mul_f32 v219,v217,v219
v_accvgpr_write_b32 a52,v218
v_accvgpr_write_b32 a53,v219
v_accvgpr_read_b32 v218,a54
v_accvgpr_read_b32 v219,a55
v_mul_f32 v218,v217,v218
v_mul_f32 v219,v217,v219
v_accvgpr_write_b32 a54,v218
v_accvgpr_write_b32 a55,v219
v_accvgpr_read_b32 v218,a56
v_accvgpr_read_b32 v219,a57
v_mul_f32 v218,v217,v218
v_mul_f32 v219,v217,v219
v_accvgpr_write_b32 a56,v218
v_accvgpr_write_b32 a57,v219
v_accvgpr_read_b32 v218,a58
v_accvgpr_read_b32 v219,a59
v_mul_f32 v218,v217,v218
v_mul_f32 v219,v217,v219
v_accvgpr_write_b32 a58,v218
v_accvgpr_write_b32 a59,v219
v_accvgpr_read_b32 v218,a60
v_accvgpr_read_b32 v219,a61
v_mul_f32 v218,v217,v218
v_mul_f32 v219,v217,v219
v_accvgpr_write_b32 a60,v218
v_accvgpr_write_b32 a61,v219
v_accvgpr_read_b32 v218,a62
v_accvgpr_read_b32 v219,a63
v_mul_f32 v218,v217,v218
v_mul_f32 v219,v217,v219
v_accvgpr_write_b32 a62,v218
v_accvgpr_write_b32 a63,v219
v_max3_f32 v228,v40,v41,v42
v_max3_f32 v228,v228,v43,v56
v_max3_f32 v228,v228,v57,v58
v_max_f32 v228,v228,v59
v_mov_b32 v218,v228
s_nop 1
v_permlane32_swap_b32 v218,v228
v_max_f32 v228,v228,v218
v_mov_b32 v218,v228
s_nop 1
v_permlane16_swap_b32 v218,v228
v_max_f32 v228,v228,v218
v_max_f32 v228,0,v228
v_exp_f32 v217,-v228
v_sub_f32 v88,v88,v228
v_sub_f32 v8,v8,v228
v_sub_f32 v24,v24,v228
v_sub_f32 v40,v40,v228
v_sub_f32 v56,v56,v228
v_sub_f32 v89,v89,v228
v_sub_f32 v9,v9,v228
v_sub_f32 v25,v25,v228
v_sub_f32 v41,v41,v228
v_sub_f32 v57,v57,v228
v_sub_f32 v90,v90,v228
v_sub_f32 v10,v10,v228
v_sub_f32 v26,v26,v228
v_sub_f32 v42,v42,v228
v_sub_f32 v58,v58,v228
v_sub_f32 v91,v91,v228
v_sub_f32 v11,v11,v228
v_sub_f32 v27,v27,v228
v_sub_f32 v43,v43,v228
v_sub_f32 v59,v59,v228
v_mul_f32 v106,v217,v106
v_accvgpr_read_b32 v218,a64
v_accvgpr_read_b32 v219,a65
v_mul_f32 v218,v217,v218
v_mul_f32 v219,v217,v219
v_accvgpr_write_b32 a64,v218
v_accvgpr_write_b32 a65,v219
v_accvgpr_read_b32 v218,a66
v_accvgpr_read_b32 v219,a67
v_mul_f32 v218,v217,v218
v_mul_f32 v219,v217,v219
v_accvgpr_write_b32 a66,v218
v_accvgpr_write_b32 a67,v219
v_accvgpr_read_b32 v218,a68
v_accvgpr_read_b32 v219,a69
v_mul_f32 v218,v217,v218
v_mul_f32 v219,v217,v219
v_accvgpr_write_b32 a68,v218
v_accvgpr_write_b32 a69,v219
v_accvgpr_read_b32 v218,a70
v_accvgpr_read_b32 v219,a71
v_mul_f32 v218,v217,v218
v_mul_f32 v219,v217,v219
v_accvgpr_write_b32 a70,v218
v_accvgpr_write_b32 a71,v219
v_accvgpr_read_b32 v218,a72
v_accvgpr_read_b32 v219,a73
v_mul_f32 v218,v217,v218
v_mul_f32 v219,v217,v219
v_accvgpr_write_b32 a72,v218
v_accvgpr_write_b32 a73,v219
v_accvgpr_read_b32 v218,a74
v_accvgpr_read_b32 v219,a75
v_mul_f32 v218,v217,v218
v_mul_f32 v219,v217,v219
v_accvgpr_write_b32 a74,v218
v_accvgpr_write_b32 a75,v219
v_accvgpr_read_b32 v218,a76
v_accvgpr_read_b32 v219,a77
v_mul_f32 v218,v217,v218
v_mul_f32 v219,v217,v219
v_accvgpr_write_b32 a76,v218
v_accvgpr_write_b32 a77,v219
v_accvgpr_read_b32 v218,a78
v_accvgpr_read_b32 v219,a79
v_mul_f32 v218,v217,v218
v_mul_f32 v219,v217,v219
v_accvgpr_write_b32 a78,v218
v_accvgpr_write_b32 a79,v219
v_accvgpr_read_b32 v218,a80
v_accvgpr_read_b32 v219,a81
v_mul_f32 v218,v217,v218
v_mul_f32 v219,v217,v219
v_accvgpr_write_b32 a80,v218
v_accvgpr_write_b32 a81,v219
v_accvgpr_read_b32 v218,a82
v_accvgpr_read_b32 v219,a83
v_mul_f32 v218,v217,v218
v_mul_f32 v219,v217,v219
v_accvgpr_write_b32 a82,v218
v_accvgpr_write_b32 a83,v219
v_accvgpr_read_b32 v218,a84
v_accvgpr_read_b32 v219,a85
v_mul_f32 v218,v217,v218
v_mul_f32 v219,v217,v219
v_accvgpr_write_b32 a84,v218
v_accvgpr_write_b32 a85,v219
v_accvgpr_read_b32 v218,a86
v_accvgpr_read_b32 v219,a87
v_mul_f32 v218,v217,v218
v_mul_f32 v219,v217,v219
v_accvgpr_write_b32 a86,v218
v_accvgpr_write_b32 a87,v219
v_accvgpr_read_b32 v218,a88
v_accvgpr_read_b32 v219,a89
v_mul_f32 v218,v217,v218
v_mul_f32 v219,v217,v219
v_accvgpr_write_b32 a88,v218
v_accvgpr_write_b32 a89,v219
v_accvgpr_read_b32 v218,a90
v_accvgpr_read_b32 v219,a91
v_mul_f32 v218,v217,v218
v_mul_f32 v219,v217,v219
v_accvgpr_write_b32 a90,v218
v_accvgpr_write_b32 a91,v219
v_accvgpr_read_b32 v218,a92
v_accvgpr_read_b32 v219,a93
v_mul_f32 v218,v217,v218
v_mul_f32 v219,v217,v219
v_accvgpr_write_b32 a92,v218
v_accvgpr_write_b32 a93,v219
v_accvgpr_read_b32 v218,a94
v_accvgpr_read_b32 v219,a95
v_mul_f32 v218,v217,v218
v_mul_f32 v219,v217,v219
v_accvgpr_write_b32 a94,v218
v_accvgpr_write_b32 a95,v219
v_max3_f32 v228,v44,v45,v46
v_max3_f32 v228,v228,v47,v60
v_max3_f32 v228,v228,v61,v62
v_max_f32 v228,v228,v63
v_mov_b32 v218,v228
s_nop 1
v_permlane32_swap_b32 v218,v228
v_max_f32 v228,v228,v218
v_mov_b32 v218,v228
s_nop 1
v_permlane16_swap_b32 v218,v228
v_max_f32 v228,v228,v218
v_max_f32 v228,0,v228
v_exp_f32 v217,-v228
v_sub_f32 v92,v92,v228
v_sub_f32 v12,v12,v228
v_sub_f32 v28,v28,v228
v_sub_f32 v44,v44,v228
v_sub_f32 v60,v60,v228
v_sub_f32 v93,v93,v228
v_sub_f32 v13,v13,v228
v_sub_f32 v29,v29,v228
v_sub_f32 v45,v45,v228
v_sub_f32 v61,v61,v228
v_sub_f32 v94,v94,v228
v_sub_f32 v14,v14,v228
v_sub_f32 v30,v30,v228
v_sub_f32 v46,v46,v228
v_sub_f32 v62,v62,v228
v_sub_f32 v95,v95,v228
v_sub_f32 v15,v15,v228
v_sub_f32 v31,v31,v228
v_sub_f32 v47,v47,v228
v_sub_f32 v63,v63,v228
v_mul_f32 v107,v217,v107
v_accvgpr_read_b32 v218,a96
v_accvgpr_read_b32 v219,a97
v_mul_f32 v218,v217,v218
v_mul_f32 v219,v217,v219
v_accvgpr_write_b32 a96,v218
v_accvgpr_write_b32 a97,v219
v_accvgpr_read_b32 v218,a98
v_accvgpr_read_b32 v219,a99
v_mul_f32 v218,v217,v218
v_mul_f32 v219,v217,v219
v_accvgpr_write_b32 a98,v218
v_accvgpr_write_b32 a99,v219
v_accvgpr_read_b32 v218,a100
v_accvgpr_read_b32 v219,a101
v_mul_f32 v218,v217,v218
v_mul_f32 v219,v217,v219
v_accvgpr_write_b32 a100,v218
v_accvgpr_write_b32 a101,v219
v_accvgpr_read_b32 v218,a102
v_accvgpr_read_b32 v219,a103
v_mul_f32 v218,v217,v218
v_mul_f32 v219,v217,v219
v_accvgpr_write_b32 a102,v218
v_accvgpr_write_b32 a103,v219
v_accvgpr_read_b32 v218,a104
v_accvgpr_read_b32 v219,a105
v_mul_f32 v218,v217,v218
v_mul_f32 v219,v217,v219
v_accvgpr_write_b32 a104,v218
v_accvgpr_write_b32 a105,v219
v_accvgpr_read_b32 v218,a106
v_accvgpr_read_b32 v219,a107
v_mul_f32 v218,v217,v218
v_mul_f32 v219,v217,v219
v_accvgpr_write_b32 a106,v218
v_accvgpr_write_b32 a107,v219
v_accvgpr_read_b32 v218,a108
v_accvgpr_read_b32 v219,a109
v_mul_f32 v218,v217,v218
v_mul_f32 v219,v217,v219
v_accvgpr_write_b32 a108,v218
v_accvgpr_write_b32 a109,v219
v_accvgpr_read_b32 v218,a110
v_accvgpr_read_b32 v219,a111
v_mul_f32 v218,v217,v218
v_mul_f32 v219,v217,v219
v_accvgpr_write_b32 a110,v218
v_accvgpr_write_b32 a111,v219
v_accvgpr_read_b32 v218,a112
v_accvgpr_read_b32 v219,a113
v_mul_f32 v218,v217,v218
v_mul_f32 v219,v217,v219
v_accvgpr_write_b32 a112,v218
v_accvgpr_write_b32 a113,v219
v_accvgpr_read_b32 v218,a114
v_accvgpr_read_b32 v219,a115
v_mul_f32 v218,v217,v218
v_mul_f32 v219,v217,v219
v_accvgpr_write_b32 a114,v218
v_accvgpr_write_b32 a115,v219
v_accvgpr_read_b32 v218,a116
v_accvgpr_read_b32 v219,a117
v_mul_f32 v218,v217,v218
v_mul_f32 v219,v217,v219
v_accvgpr_write_b32 a116,v218
v_accvgpr_write_b32 a117,v219
v_accvgpr_read_b32 v218,a118
v_accvgpr_read_b32 v219,a119
v_mul_f32 v218,v217,v218
v_mul_f32 v219,v217,v219
v_accvgpr_write_b32 a118,v218
v_accvgpr_write_b32 a119,v219
v_accvgpr_read_b32 v218,a120
v_accvgpr_read_b32 v219,a121
v_mul_f32 v218,v217,v218
v_mul_f32 v219,v217,v219
v_accvgpr_write_b32 a120,v218
v_accvgpr_write_b32 a121,v219
v_accvgpr_read_b32 v218,a122
v_accvgpr_read_b32 v219,a123
v_mul_f32 v218,v217,v218
v_mul_f32 v219,v217,v219
v_accvgpr_write_b32 a122,v218
v_accvgpr_write_b32 a123,v219
v_accvgpr_read_b32 v218,a124
v_accvgpr_read_b32 v219,a125
v_mul_f32 v218,v217,v218
v_mul_f32 v219,v217,v219
v_accvgpr_write_b32 a124,v218
v_accvgpr_write_b32 a125,v219
v_accvgpr_read_b32 v218,a126
v_accvgpr_read_b32 v219,a127
v_mul_f32 v218,v217,v218
v_mul_f32 v219,v217,v219
v_accvgpr_write_b32 a126,v218
v_accvgpr_write_b32 a127,v219
v_exp_f32 v216,v32
v_exp_f32 v217,v36
v_exp_f32 v218,v33
v_exp_f32 v219,v37
v_exp_f32 v220,v34
v_exp_f32 v221,v38
v_add_f32 v72,v216,v218
v_cvt_pk_bf16_f32 v128,v216,v218
v_exp_f32 v222,v35
v_add_f32 v73,v217,v219
v_cvt_pk_bf16_f32 v132,v217,v219
)A"
R"A(v_exp_f32 v223,v39
v_add_f32 v72,v220,v72
v_exp_f32 v216,v40
v_add_f32 v73,v221,v73
v_exp_f32 v217,v44
v_add_f32 v72,v222,v72
v_cvt_pk_bf16_f32 v129,v220,v222
v_exp_f32 v218,v41
v_add_f32 v73,v223,v73
v_cvt_pk_bf16_f32 v133,v221,v223
v_exp_f32 v219,v45
v_exp_f32 v220,v42
v_exp_f32 v221,v46
v_add_f32 v74,v216,v218
v_cvt_pk_bf16_f32 v136,v216,v218
v_exp_f32 v222,v43
v_add_f32 v75,v217,v219
v_cvt_pk_bf16_f32 v140,v217,v219
v_exp_f32 v223,v47
v_add_f32 v74,v220,v74
v_exp_f32 v216,v48
v_add_f32 v75,v221,v75
v_exp_f32 v217,v52
v_add_f32 v74,v222,v74
v_cvt_pk_bf16_f32 v137,v220,v222
v_exp_f32 v218,v49
v_add_f32 v75,v223,v75
v_cvt_pk_bf16_f32 v141,v221,v223
v_exp_f32 v219,v53
v_add_f32 v72,v216,v72
v_exp_f32 v220,v50
v_add_f32 v73,v217,v73
v_exp_f32 v221,v54
v_add_f32 v72,v218,v72
v_cvt_pk_bf16_f32 v130,v216,v218
v_exp_f32 v222,v51
v_add_f32 v73,v219,v73
v_cvt_pk_bf16_f32 v134,v217,v219
v_exp_f32 v223,v55
v_add_f32 v72,v220,v72
v_exp_f32 v216,v56
v_add_f32 v73,v221,v73
v_exp_f32 v217,v60
v_add_f32 v72,v222,v72
v_cvt_pk_bf16_f32 v131,v220,v222
v_exp_f32 v218,v57
v_add_f32 v73,v223,v73
v_cvt_pk_bf16_f32 v135,v221,v223
v_exp_f32 v219,v61
v_add_f32 v74,v216,v74
v_exp_f32 v220,v58
v_add_f32 v75,v217,v75
v_exp_f32 v221,v62
v_add_f32 v74,v218,v74
v_cvt_pk_bf16_f32 v138,v216,v218
v_exp_f32 v222,v59
v_add_f32 v75,v219,v75
v_cvt_pk_bf16_f32 v142,v217,v219
v_exp_f32 v223,v63
v_add_f32 v74,v220,v74
v_add_f32 v75,v221,v75
v_add_f32 v74,v222,v74
v_cvt_pk_bf16_f32 v139,v220,v222
v_add_f32 v75,v223,v75
v_cvt_pk_bf16_f32 v143,v221,v223
s_nop 3
s_setpc_b64 s[70:71]
.Lrare2:
s_nop 15
v_max3_f32 v228,v32,v33,v34
v_max3_f32 v228,v228,v35,v48
v_max3_f32 v228,v228,v49,v50
v_max_f32 v228,v228,v51
v_mov_b32 v218,v228
s_nop 1
v_permlane32_swap_b32 v218,v228
v_max_f32 v228,v228,v218
v_mov_b32 v218,v228
s_nop 1
v_permlane16_swap_b32 v218,v228
v_max_f32 v228,v228,v218
v_max_f32 v228,0,v228
v_exp_f32 v217,-v228
v_sub_f32 v32,v32,v228
v_sub_f32 v48,v48,v228
v_sub_f32 v33,v33,v228
v_sub_f32 v49,v49,v228
v_sub_f32 v34,v34,v228
v_sub_f32 v50,v50,v228
v_sub_f32 v35,v35,v228
v_sub_f32 v51,v51,v228
v_mul_f32 v104,v217,v104
v_accvgpr_read_b32 v218,a0
v_accvgpr_read_b32 v219,a1
v_mul_f32 v218,v217,v218
v_mul_f32 v219,v217,v219
v_accvgpr_write_b32 a0,v218
v_accvgpr_write_b32 a1,v219
v_accvgpr_read_b32 v218,a2
v_accvgpr_read_b32 v219,a3
v_mul_f32 v218,v217,v218
v_mul_f32 v219,v217,v219
v_accvgpr_write_b32 a2,v218
v_accvgpr_write_b32 a3,v219
v_accvgpr_read_b32 v218,a4
v_accvgpr_read_b32 v219,a5
v_mul_f32 v218,v217,v218
v_mul_f32 v219,v217,v219
v_accvgpr_write_b32 a4,v218
v_accvgpr_write_b32 a5,v219
v_accvgpr_read_b32 v218,a6
v_accvgpr_read_b32 v219,a7
v_mul_f32 v218,v217,v218
v_mul_f32 v219,v217,v219
v_accvgpr_write_b32 a6,v218
v_accvgpr_write_b32 a7,v219
v_accvgpr_read_b32 v218,a8
v_accvgpr_read_b32 v219,a9
v_mul_f32 v218,v217,v218
v_mul_f32 v219,v217,v219
v_accvgpr_write_b32 a8,v218
v_accvgpr_write_b32 a9,v219
v_accvgpr_read_b32 v218,a10
v_accvgpr_read_b32 v219,a11
v_mul_f32 v218,v217,v218
v_mul_f32 v219,v217,v219
v_accvgpr_write_b32 a10,v218
v_accvgpr_write_b32 a11,v219
v_accvgpr_read_b32 v218,a12
v_accvgpr_read_b32 v219,a13
v_mul_f32 v218,v217,v218
v_mul_f32 v219,v217,v219
v_accvgpr_write_b32 a12,v218
v_accvgpr_write_b32 a13,v219
v_accvgpr_read_b32 v218,a14
v_accvgpr_read_b32 v219,a15
v_mul_f32 v218,v217,v218
v_mul_f32 v219,v217,v219
v_accvgpr_write_b32 a14,v218
v_accvgpr_write_b32 a15,v219
v_accvgpr_read_b32 v218,a16
v_accvgpr_read_b32 v219,a17
v_mul_f32 v218,v217,v218
v_mul_f32 v219,v217,v219
v_accvgpr_write_b32 a16,v218
v_accvgpr_write_b32 a17,v219
v_accvgpr_read_b32 v218,a18
v_accvgpr_read_b32 v219,a19
v_mul_f32 v218,v217,v218
v_mul_f32 v219,v217,v219
v_accvgpr_write_b32 a18,v218
v_accvgpr_write_b32 a19,v219
v_accvgpr_read_b32 v218,a20
v_accvgpr_read_b32 v219,a21
v_mul_f32 v218,v217,v218
v_mul_f32 v219,v217,v219
v_accvgpr_write_b32 a20,v218
v_accvgpr_write_b32 a21,v219
v_accvgpr_read_b32 v218,a22
v_accvgpr_read_b32 v219,a23
v_mul_f32 v218,v217,v218
v_mul_f32 v219,v217,v219
v_accvgpr_write_b32 a22,v218
v_accvgpr_write_b32 a23,v219
v_accvgpr_read_b32 v218,a24
v_accvgpr_read_b32 v219,a25
v_mul_f32 v218,v217,v218
v_mul_f32 v219,v217,v219
v_accvgpr_write_b32 a24,v218
v_accvgpr_write_b32 a25,v219
v_accvgpr_read_b32 v218,a26
v_accvgpr_read_b32 v219,a27
v_mul_f32 v218,v217,v218
v_mul_f32 v219,v217,v219
v_accvgpr_write_b32 a26,v218
v_accvgpr_write_b32 a27,v219
v_accvgpr_read_b32 v218,a28
v_accvgpr_read_b32 v219,a29
v_mul_f32 v218,v217,v218
v_mul_f32 v219,v217,v219
v_accvgpr_write_b32 a28,v218
v_accvgpr_write_b32 a29,v219
v_accvgpr_read_b32 v218,a30
v_accvgpr_read_b32 v219,a31
v_mul_f32 v218,v217,v218
v_mul_f32 v219,v217,v219
v_accvgpr_write_b32 a30,v218
v_accvgpr_write_b32 a31,v219
v_max3_f32 v228,v36,v37,v38
v_max3_f32 v228,v228,v39,v52
v_max3_f32 v228,v228,v53,v54
v_max_f32 v228,v228,v55
v_mov_b32 v218,v228
s_nop 1
v_permlane32_swap_b32 v218,v228
v_max_f32 v228,v228,v218
v_mov_b32 v218,v228
s_nop 1
v_permlane16_swap_b32 v218,v228
v_max_f32 v228,v228,v218
v_max_f32 v228,0,v228
v_exp_f32 v217,-v228
v_sub_f32 v36,v36,v228
v_sub_f32 v52,v52,v228
v_sub_f32 v37,v37,v228
v_sub_f32 v53,v53,v228
v_sub_f32 v38,v38,v228
v_sub_f32 v54,v54,v228
v_sub_f32 v39,v39,v228
v_sub_f32 v55,v55,v228
v_mul_f32 v105,v217,v105
v_accvgpr_read_b32 v218,a32
v_accvgpr_read_b32 v219,a33
v_mul_f32 v218,v217,v218
v_mul_f32 v219,v217,v219
v_accvgpr_write_b32 a32,v218
v_accvgpr_write_b32 a33,v219
v_accvgpr_read_b32 v218,a34
v_accvgpr_read_b32 v219,a35
v_mul_f32 v218,v217,v218
v_mul_f32 v219,v217,v219
v_accvgpr_write_b32 a34,v218
v_accvgpr_write_b32 a35,v219
v_accvgpr_read_b32 v218,a36
v_accvgpr_read_b32 v219,a37
v_mul_f32 v218,v217,v218
v_mul_f32 v219,v217,v219
v_accvgpr_write_b32 a36,v218
v_accvgpr_write_b32 a37,v219
v_accvgpr_read_b32 v218,a38
v_accvgpr_read_b32 v219,a39
v_mul_f32 v218,v217,v218
v_mul_f32 v219,v217,v219
v_accvgpr_write_b32 a38,v218
v_accvgpr_write_b32 a39,v219
v_accvgpr_read_b32 v218,a40
v_accvgpr_read_b32 v219,a41
v_mul_f32 v218,v217,v218
v_mul_f32 v219,v217,v219
v_accvgpr_write_b32 a40,v218
v_accvgpr_write_b32 a41,v219
v_accvgpr_read_b32 v218,a42
v_accvgpr_read_b32 v219,a43
v_mul_f32 v218,v217,v218
v_mul_f32 v219,v217,v219
v_accvgpr_write_b32 a42,v218
v_accvgpr_write_b32 a43,v219
v_accvgpr_read_b32 v218,a44
v_accvgpr_read_b32 v219,a45
v_mul_f32 v218,v217,v218
v_mul_f32 v219,v217,v219
v_accvgpr_write_b32 a44,v218
v_accvgpr_write_b32 a45,v219
v_accvgpr_read_b32 v218,a46
v_accvgpr_read_b32 v219,a47
v_mul_f32 v218,v217,v218
v_mul_f32 v219,v217,v219
v_accvgpr_write_b32 a46,v218
v_accvgpr_write_b32 a47,v219
v_accvgpr_read_b32 v218,a48
v_accvgpr_read_b32 v219,a49
v_mul_f32 v218,v217,v218
v_mul_f32 v219,v217,v219
v_accvgpr_write_b32 a48,v218
v_accvgpr_write_b32 a49,v219
v_accvgpr_read_b32 v218,a50
v_accvgpr_read_b32 v219,a51
v_mul_f32 v218,v217,v218
v_mul_f32 v219,v217,v219
v_accvgpr_write_b32 a50,v218
v_accvgpr_write_b32 a51,v219
v_accvgpr_read_b32 v218,a52
v_accvgpr_read_b32 v219,a53
v_mul_f32 v218,v217,v218
v_mul_f32 v219,v217,v219
v_accvgpr_write_b32 a52,v218
v_accvgpr_write_b32 a53,v219
v_accvgpr_read_b32 v218,a54
v_accvgpr_read_b32 v219,a55
v_mul_f32 v218,v217,v218
v_mul_f32 v219,v217,v219
v_accvgpr_write_b32 a54,v218
v_accvgpr_write_b32 a55,v219
v_accvgpr_read_b32 v218,a56
v_accvgpr_read_b32 v219,a57
v_mul_f32 v218,v217,v218
v_mul_f32 v219,v217,v219
v_accvgpr_write_b32 a56,v218
v_accvgpr_write_b32 a57,v219
v_accvgpr_read_b32 v218,a58
v_accvgpr_read_b32 v219,a59
v_mul_f32 v218,v217,v218
v_mul_f32 v219,v217,v219
v_accvgpr_write_b32 a58,v218
v_accvgpr_write_b32 a59,v219
v_accvgpr_read_b32 v218,a60
v_accvgpr_read_b32 v219,a61
v_mul_f32 v218,v217,v218
v_mul_f32 v219,v217,v219
v_accvgpr_write_b32 a60,v218
v_accvgpr_write_b32 a61,v219
v_accvgpr_read_b32 v218,a62
v_accvgpr_read_b32 v219,a63
v_mul_f32 v218,v217,v218
v_mul_f32 v219,v217,v219
v_accvgpr_write_b32 a62,v218
v_accvgpr_write_b32 a63,v219
v_max3_f32 v228,v40,v41,v42
v_max3_f32 v228,v228,v43,v56
v_max3_f32 v228,v228,v57,v58
v_max_f32 v228,v228,v59
v_mov_b32 v218,v228
s_nop 1
v_permlane32_swap_b32 v218,v228
v_max_f32 v228,v228,v218
v_mov_b32 v218,v228
s_nop 1
v_permlane16_swap_b32 v218,v228
v_max_f32 v228,v228,v218
v_max_f32 v228,0,v228
v_exp_f32 v217,-v228
v_sub_f32 v40,v40,v228
v_sub_f32 v56,v56,v228
v_sub_f32 v41,v41,v228
v_sub_f32 v57,v57,v228
v_sub_f32 v42,v42,v228
v_sub_f32 v58,v58,v228
v_sub_f32 v43,v43,v228
v_sub_f32 v59,v59,v228
v_mul_f32 v106,v217,v106
v_accvgpr_read_b32 v218,a64
v_accvgpr_read_b32 v219,a65
v_mul_f32 v218,v217,v218
v_mul_f32 v219,v217,v219
v_accvgpr_write_b32 a64,v218
v_accvgpr_write_b32 a65,v219
v_accvgpr_read_b32 v218,a66
v_accvgpr_read_b32 v219,a67
v_mul_f32 v218,v217,v218
v_mul_f32 v219,v217,v219
v_accvgpr_write_b32 a66,v218
v_accvgpr_write_b32 a67,v219
v_accvgpr_read_b32 v218,a68
v_accvgpr_read_b32 v219,a69
v_mul_f32 v218,v217,v218
v_mul_f32 v219,v217,v219
v_accvgpr_write_b32 a68,v218
v_accvgpr_write_b32 a69,v219
v_accvgpr_read_b32 v218,a70
v_accvgpr_read_b32 v219,a71
v_mul_f32 v218,v217,v218
v_mul_f32 v219,v217,v219
v_accvgpr_write_b32 a70,v218
v_accvgpr_write_b32 a71,v219
v_accvgpr_read_b32 v218,a72
v_accvgpr_read_b32 v219,a73
v_mul_f32 v218,v217,v218
v_mul_f32 v219,v217,v219
v_accvgpr_write_b32 a72,v218
v_accvgpr_write_b32 a73,v219
v_accvgpr_read_b32 v218,a74
v_accvgpr_read_b32 v219,a75
v_mul_f32 v218,v217,v218
v_mul_f32 v219,v217,v219
v_accvgpr_write_b32 a74,v218
v_accvgpr_write_b32 a75,v219
v_accvgpr_read_b32 v218,a76
v_accvgpr_read_b32 v219,a77
v_mul_f32 v218,v217,v218
v_mul_f32 v219,v217,v219
v_accvgpr_write_b32 a76,v218
v_accvgpr_write_b32 a77,v219
v_accvgpr_read_b32 v218,a78
v_accvgpr_read_b32 v219,a79
v_mul_f32 v218,v217,v218
v_mul_f32 v219,v217,v219
v_accvgpr_write_b32 a78,v218
v_accvgpr_write_b32 a79,v219
v_accvgpr_read_b32 v218,a80
v_accvgpr_read_b32 v219,a81
v_mul_f32 v218,v217,v218
v_mul_f32 v219,v217,v219
v_accvgpr_write_b32 a80,v218
v_accvgpr_write_b32 a81,v219
v_accvgpr_read_b32 v218,a82
v_accvgpr_read_b32 v219,a83
v_mul_f32 v218,v217,v218
v_mul_f32 v219,v217,v219
v_accvgpr_write_b32 a82,v218
v_accvgpr_write_b32 a83,v219
v_accvgpr_read_b32 v218,a84
v_accvgpr_read_b32 v219,a85
v_mul_f32 v218,v217,v218
v_mul_f32 v219,v217,v219
v_accvgpr_write_b32 a84,v218
v_accvgpr_write_b32 a85,v219
v_accvgpr_read_b32 v218,a86
v_accvgpr_read_b32 v219,a87
v_mul_f32 v218,v217,v218
v_mul_f32 v219,v217,v219
v_accvgpr_write_b32 a86,v218
v_accvgpr_write_b32 a87,v219
v_accvgpr_read_b32 v218,a88
v_accvgpr_read_b32 v219,a89
v_mul_f32 v218,v217,v218
v_mul_f32 v219,v217,v219
v_accvgpr_write_b32 a88,v218
v_accvgpr_write_b32 a89,v219
v_accvgpr_read_b32 v218,a90
v_accvgpr_read_b32 v219,a91
v_mul_f32 v218,v217,v218
v_mul_f32 v219,v217,v219
v_accvgpr_write_b32 a90,v218
v_accvgpr_write_b32 a91,v219
v_accvgpr_read_b32 v218,a92
v_accvgpr_read_b32 v219,a93
v_mul_f32 v218,v217,v218
v_mul_f32 v219,v217,v219
v_accvgpr_write_b32 a92,v218
v_accvgpr_write_b32 a93,v219
v_accvgpr_read_b32 v218,a94
v_accvgpr_read_b32 v219,a95
v_mul_f32 v218,v217,v218
v_mul_f32 v219,v217,v219
v_accvgpr_write_b32 a94,v218
v_accvgpr_write_b32 a95,v219
v_max3_f32 v228,v44,v45,v46
v_max3_f32 v228,v228,v47,v60
v_max3_f32 v228,v228,v61,v62
v_max_f32 v228,v228,v63
v_mov_b32 v218,v228
s_nop 1
v_permlane32_swap_b32 v218,v228
v_max_f32 v228,v228,v218
v_mov_b32 v218,v228
s_nop 1
v_permlane16_swap_b32 v218,v228
v_max_f32 v228,v228,v218
v_max_f32 v228,0,v228
v_exp_f32 v217,-v228
v_sub_f32 v44,v44,v228
v_sub_f32 v60,v60,v228
v_sub_f32 v45,v45,v228
v_sub_f32 v61,v61,v228
v_sub_f32 v46,v46,v228
v_sub_f32 v62,v62,v228
v_sub_f32 v47,v47,v228
v_sub_f32 v63,v63,v228
v_mul_f32 v107,v217,v107
v_accvgpr_read_b32 v218,a96
v_accvgpr_read_b32 v219,a97
v_mul_f32 v218,v217,v218
v_mul_f32 v219,v217,v219
v_accvgpr_write_b32 a96,v218
v_accvgpr_write_b32 a97,v219
v_accvgpr_read_b32 v218,a98
v_accvgpr_read_b32 v219,a99
v_mul_f32 v218,v217,v218
v_mul_f32 v219,v217,v219
v_accvgpr_write_b32 a98,v218
v_accvgpr_write_b32 a99,v219
v_accvgpr_read_b32 v218,a100
v_accvgpr_read_b32 v219,a101
v_mul_f32 v218,v217,v218
v_mul_f32 v219,v217,v219
v_accvgpr_write_b32 a100,v218
)A"
R"A(v_accvgpr_write_b32 a101,v219
v_accvgpr_read_b32 v218,a102
v_accvgpr_read_b32 v219,a103
v_mul_f32 v218,v217,v218
v_mul_f32 v219,v217,v219
v_accvgpr_write_b32 a102,v218
v_accvgpr_write_b32 a103,v219
v_accvgpr_read_b32 v218,a104
v_accvgpr_read_b32 v219,a105
v_mul_f32 v218,v217,v218
v_mul_f32 v219,v217,v219
v_accvgpr_write_b32 a104,v218
v_accvgpr_write_b32 a105,v219
v_accvgpr_read_b32 v218,a106
v_accvgpr_read_b32 v219,a107
v_mul_f32 v218,v217,v218
v_mul_f32 v219,v217,v219
v_accvgpr_write_b32 a106,v218
v_accvgpr_write_b32 a107,v219
v_accvgpr_read_b32 v218,a108
v_accvgpr_read_b32 v219,a109
v_mul_f32 v218,v217,v218
v_mul_f32 v219,v217,v219
v_accvgpr_write_b32 a108,v218
v_accvgpr_write_b32 a109,v219
v_accvgpr_read_b32 v218,a110
v_accvgpr_read_b32 v219,a111
v_mul_f32 v218,v217,v218
v_mul_f32 v219,v217,v219
v_accvgpr_write_b32 a110,v218
v_accvgpr_write_b32 a111,v219
v_accvgpr_read_b32 v218,a112
v_accvgpr_read_b32 v219,a113
v_mul_f32 v218,v217,v218
v_mul_f32 v219,v217,v219
v_accvgpr_write_b32 a112,v218
v_accvgpr_write_b32 a113,v219
v_accvgpr_read_b32 v218,a114
v_accvgpr_read_b32 v219,a115
v_mul_f32 v218,v217,v218
v_mul_f32 v219,v217,v219
v_accvgpr_write_b32 a114,v218
v_accvgpr_write_b32 a115,v219
v_accvgpr_read_b32 v218,a116
v_accvgpr_read_b32 v219,a117
v_mul_f32 v218,v217,v218
v_mul_f32 v219,v217,v219
v_accvgpr_write_b32 a116,v218
v_accvgpr_write_b32 a117,v219
v_accvgpr_read_b32 v218,a118
v_accvgpr_read_b32 v219,a119
v_mul_f32 v218,v217,v218
v_mul_f32 v219,v217,v219
v_accvgpr_write_b32 a118,v218
v_accvgpr_write_b32 a119,v219
v_accvgpr_read_b32 v218,a120
v_accvgpr_read_b32 v219,a121
v_mul_f32 v218,v217,v218
v_mul_f32 v219,v217,v219
v_accvgpr_write_b32 a120,v218
v_accvgpr_write_b32 a121,v219
v_accvgpr_read_b32 v218,a122
v_accvgpr_read_b32 v219,a123
v_mul_f32 v218,v217,v218
v_mul_f32 v219,v217,v219
v_accvgpr_write_b32 a122,v218
v_accvgpr_write_b32 a123,v219
v_accvgpr_read_b32 v218,a124
v_accvgpr_read_b32 v219,a125
v_mul_f32 v218,v217,v218
v_mul_f32 v219,v217,v219
v_accvgpr_write_b32 a124,v218
v_accvgpr_write_b32 a125,v219
v_accvgpr_read_b32 v218,a126
v_accvgpr_read_b32 v219,a127
v_mul_f32 v218,v217,v218
v_mul_f32 v219,v217,v219
v_accvgpr_write_b32 a126,v218
v_accvgpr_write_b32 a127,v219
v_exp_f32 v216,v32
v_exp_f32 v217,v36
v_exp_f32 v218,v33
v_exp_f32 v219,v37
v_exp_f32 v220,v34
v_exp_f32 v221,v38
v_add_f32 v72,v216,v218
v_cvt_pk_bf16_f32 v128,v216,v218
v_exp_f32 v222,v35
v_add_f32 v73,v217,v219
v_cvt_pk_bf16_f32 v132,v217,v219
v_exp_f32 v223,v39
v_add_f32 v72,v220,v72
v_exp_f32 v216,v40
v_add_f32 v73,v221,v73
v_exp_f32 v217,v44
v_add_f32 v72,v222,v72
v_cvt_pk_bf16_f32 v129,v220,v222
v_exp_f32 v218,v41
v_add_f32 v73,v223,v73
v_cvt_pk_bf16_f32 v133,v221,v223
v_exp_f32 v219,v45
v_exp_f32 v220,v42
v_exp_f32 v221,v46
v_add_f32 v74,v216,v218
v_cvt_pk_bf16_f32 v136,v216,v218
v_exp_f32 v222,v43
v_add_f32 v75,v217,v219
v_cvt_pk_bf16_f32 v140,v217,v219
v_exp_f32 v223,v47
v_add_f32 v74,v220,v74
v_exp_f32 v216,v48
v_add_f32 v75,v221,v75
v_exp_f32 v217,v52
v_add_f32 v74,v222,v74
v_cvt_pk_bf16_f32 v137,v220,v222
v_exp_f32 v218,v49
v_add_f32 v75,v223,v75
v_cvt_pk_bf16_f32 v141,v221,v223
v_exp_f32 v219,v53
v_add_f32 v72,v216,v72
v_exp_f32 v220,v50
v_add_f32 v73,v217,v73
v_exp_f32 v221,v54
v_add_f32 v72,v218,v72
v_cvt_pk_bf16_f32 v130,v216,v218
v_exp_f32 v222,v51
v_add_f32 v73,v219,v73
v_cvt_pk_bf16_f32 v134,v217,v219
v_exp_f32 v223,v55
v_add_f32 v72,v220,v72
v_exp_f32 v216,v56
v_add_f32 v73,v221,v73
v_exp_f32 v217,v60
v_add_f32 v72,v222,v72
v_cvt_pk_bf16_f32 v131,v220,v222
v_exp_f32 v218,v57
v_add_f32 v73,v223,v73
v_cvt_pk_bf16_f32 v135,v221,v223
v_exp_f32 v219,v61
v_add_f32 v74,v216,v74
v_exp_f32 v220,v58
v_add_f32 v75,v217,v75
v_exp_f32 v221,v62
v_add_f32 v74,v218,v74
v_cvt_pk_bf16_f32 v138,v216,v218
v_exp_f32 v222,v59
v_add_f32 v75,v219,v75
v_cvt_pk_bf16_f32 v142,v217,v219
v_exp_f32 v223,v63
v_add_f32 v74,v220,v74
v_add_f32 v75,v221,v75
v_add_f32 v74,v222,v74
v_cvt_pk_bf16_f32 v139,v220,v222
v_add_f32 v75,v223,v75
v_cvt_pk_bf16_f32 v143,v221,v223
s_nop 3
s_setpc_b64 s[70:71]
.Lend:
)A"
  :: "s"(ka), "s"(g), "s"(w) : "s4","s5","s6","s7","s8","s9","s10","s11","s12","s13","s14","s15","s16","s17","s18","s19","s20","s21","s22","s23","s24","s25","s26","v0","v1","v2","v3","v4","v5","v6","v7","v8","v9","v10","v11","v12","v13","v14","v15","v16","v17","v18","v19","v20","v21","v22","v23","v24","v25","v26","v27","v28","v29","v30","v31","v32","v33","v34","v35","v36","v37","v38","v39","v40","v41","v42","v43","v44","v45","v46","v47","v48","v49","v50","v51","v52","v53","v54","v55","v56","v57","v58","v59","v60","v61","v62","v63","v64","v65","v66","v67","v68","v69","v70","v71","v72","v73","v74","v75","v76","v77","v78","v79","v80","v81","v82","v83","v84","v85","v86","v87","v88","v89","v90","v91","v92","v93","v94","v95","v96","v97","v98","v99","v100","v101","v102","v103","v104","v105","v106","v107","v108","v109","v110","v111","v112","v113","v114","v115","v116","v117","v118","v119","v120","v121","v122","v123","v124","v125","v126","v127","v128","v129","v130","v131","v132","v133","v134","v135","v136","v137","v138","v139","v140","v141","v142","v143","v144","v145","v146","v147","v148","v149","v150","v151","v152","v153","v154","v155","v156","v157","v158","v159","v160","v161","v162","v163","v164","v165","v166","v167","v168","v169","v170","v171","v172","v173","v174","v175","v176","v177","v178","v179","v180","v181","v182","v183","v184","v185","v186","v187","v188","v189","v190","v191","v192","v193","v194","v195","v196","v197","v198","v199","v200","v201","v202","v203","v204","v205","v206","v207","v208","v209","v210","v211","v212","v213","v214","v215","v216","v217","v218","v219","v220","v221","v222","v223","v224","v225","v226","v227","v228","v229","v230","v231","v232","v233","v234","v235","v236","v237","v238","v239","v240","v241","v242","v243","v244","v245","v246","v247","v248","v249","v250","v251","v252","v253","v254","v255","a0","a1","a2","a3","a4","a5","a6","a7","a8","a9","a10","a11","a12","a13","a14","a15","a16","a17","a18","a19","a20","a21","a22","a23","a24","a25","a26","a27","a28","a29","a30","a31","a32","a33","a34","a35","a36","a37","a38","a39","a40","a41","a42","a43","a44","a45","a46","a47","a48","a49","a50","a51","a52","a53","a54","a55","a56","a57","a58","a59","a60","a61","a62","a63","a64","a65","a66","a67","a68","a69","a70","a71","a72","a73","a74","a75","a76","a77","a78","a79","a80","a81","a82","a83","a84","a85","a86","a87","a88","a89","a90","a91","a92","a93","a94","a95","a96","a97","a98","a99","a100","a101","a102","a103","a104","a105","a106","a107","a108","a109","a110","a111","a112","a113","a114","a115","a116","a117","a118","a119","a120","a121","a122","a123","a124","a125","a126","a127","a128","a129","a130","a131","a132","a133","a134","a135","a136","a137","a138","a139","a140","a141","a142","a143","a144","a145","a146","a147","a148","a149","a150","a151","a152","a153","a154","a155","a156","a157","a158","a159","a160","a161","a162","a163","a164","a165","a166","a167","a168","a169","a170","a171","a172","a173","a174","a175","a176","a177","a178","a179","a180","a181","a182","a183","a184","a185","a186","a187","a188","a189","a190","a191","a192","a193","a194","a195","a196","a197","a198","a199","a200","a201","a202","a203","a204","a205","a206","a207","a208","a209","a210","a211","a212","a213","a214","a215","a216","a217","a218","a219","a220","a221","a222","a223","a224","a225","a226","a227","a228","a229","a230","a231","a232","a233","a234","a235","a236","a237","a238","a239","a240","a241","a242","a243","a244","a245","a246","a247","a248","a249","a250","a251","a252","a253","a254","a255","s27","s28","s29","s30","s31","s32","s33","s34","s35","s36","s37","s38","s39","s40","s41","s42","s43","s44","s45","s46","s47","s48","s49","s50","s51","s52","s53","s54","s55","s56","s57","s58","s59","s60","s61","s62","s63","s64","s65","s66","s67","s68","s69","s70","s71","s72","s73","s74","s75","s76","s77","s78","s79","s80","s81","s82","s83","s84","s85","s86","s87","s88","s89","s90","s91","s92","s93","s94","s95","s96","s97","s98","s99","vcc","m0","scc","memory");
}

extern "C" void kernel_launch(void* const* d_in, const int* in_sizes, int n_in, void* d_out, int out_size, void* d_ws, size_t ws_size, hipStream_t stream) {
  hipLaunchKernelGGL(fused_kernel, dim3(256), dim3(256), 0, stream, (const char*)d_in[0], (const char*)d_in[1], (const char*)d_in[2], (const char*)d_in[3],
                     (const char*)d_in[5], (const char*)d_in[6], (const char*)d_in[7], (const char*)d_in[8], (char*)d_ws, (const char*)d_in[4], (const char*)d_in[9], (char*)d_out);
}
```
